```python
import torch

def _softmax(x, axis=-1):
    return torch.softmax(x, dim=axis)
def _gelu(x):
    return torch.nn.functional.gelu(x, approximate="tanh")
def _silu(x):
    return torch.nn.functional.silu(x)
def _sigmoid(x):
    return torch.sigmoid(x)
def _layernorm(x, axis=-1, eps=1e-5):
    m = x.mean(dim=axis, keepdim=True)
    v = x.var(dim=axis, keepdim=True, unbiased=False)
    return (x - m) / torch.sqrt(v + eps)
def _scatter_add(tgt, idx, src, axis=0):
    return tgt.clone().index_add_(axis, idx, src)
def _segment_sum(src, seg, n):
    out = torch.zeros((n,) + src.shape[1:], dtype=src.dtype)
    return out.index_add_(0, seg, src)
def _pad(x, pw):
    flat = tuple(v for pair in reversed(pw) for v in pair)
    return torch.nn.functional.pad(x, flat)

kernel_name = 'gated_mix__linear_attn_block__glu_gated_swiglu__prompt'

def setup_inputs():

    _w = (lambda a: (lambda t: t.to(torch.bfloat16) if t.is_floating_point() else t)(torch.as_tensor(a)))
    def _A_setup():
        B, S, D = 1, 2048, 512
        return {
            "x": _w((lambda *s: torch.randn(*s, dtype=torch.bfloat16) * 0.05)(B, S, D)),
            "wqkv": _w((lambda *s: torch.randn(*s, dtype=torch.bfloat16) * 0.8)(D, 3 * D)),
            "wo": _w((lambda *s: torch.randn(*s, dtype=torch.bfloat16) * 0.05)(D, D)),
        }
    _A_in = _A_setup()

    def _B_setup():
        B, S, D, F = 1, 2048, 512, 1792
        return {
            "x": _w((lambda *s: torch.randn(*s, dtype=torch.bfloat16) * 0.05)(B, S, D)),
            "w1": _w((lambda *s: torch.randn(*s, dtype=torch.bfloat16) * 1.6)(D, F) * (2.0 / D) ** 0.5),
            "w3": _w((lambda *s: torch.randn(*s, dtype=torch.bfloat16) * 1.6)(D, F) * (2.0 / D) ** 0.5),
            "w2": _w((lambda *s: torch.randn(*s, dtype=torch.bfloat16) * 1.6)(F, D) * (2.0 / F) ** 0.5),
        }
    _B_in = _B_setup()

    return {
        "A_x": _A_in["x"],
        "A_wqkv": _A_in["wqkv"],
        "A_wo": _A_in["wo"],
        "B_x": _B_in["x"],
        "B_w1": _B_in["w1"],
        "B_w3": _B_in["w3"],
        "B_w2": _B_in["w2"],
        "gate_w": _w((lambda *s: torch.randn(*s, dtype=torch.bfloat16) * 0.05)(512) * 16.0),
    }

def reference(A_x, A_wqkv, A_wo, B_x, B_w1, B_w3, B_w2, gate_w):
    def _A_ref(x, wqkv, wo):
        B, S, D = x.shape
        H = 32; Dh = D // H
        qkv = (lambda x, *d: x.permute(*d))((x @ wqkv).reshape(B, S, 3, H, Dh), 2, 0, 3, 1, 4)
        q, k, v = qkv[0], qkv[1], qkv[2]
        phi = lambda t: torch.where(t > 0, t + 1, torch.exp(t)) + 1e-6
        pq, pk = phi(q), phi(k)
        kv = torch.einsum("bhsd,bhse->bhde", pk, v)
        z = torch.einsum("bhsd->bhd", pk)
        num = torch.einsum("bhqd,bhde->bhqe", pq, kv)
        den = torch.einsum("bhqd,bhd->bhq", pq, z)[..., None] + 1e-6
        o = num / den
        return (lambda x, *d: x.permute(*d))(o, 0, 2, 1, 3).reshape(B, S, D) @ wo

    def _B_ref(x, w1, w3, w2):
        gate = torch.einsum("bsd,df->bsf", x, w1)
        up = torch.einsum("bsd,df->bsf", x, w3)
        h = _silu(gate) * up
        out = torch.einsum("bsf,fd->bsd", h, w2)
        return out

    x = A_x
    g = _sigmoid((x * gate_w).sum(-1, keepdims=True))
    a = _A_ref(x, A_wqkv, A_wo)
    b = _B_ref(x, B_w1, B_w3, B_w2)
    return g * a + (1 - g) * b
```

```cpp
#include <hip/hip_runtime.h>
#include <hip/hip_bf16.h>
typedef __hip_bfloat16 bf16;
constexpr int S=2048, D=512, F=1792, H=32, DH=16;

__device__ inline float b2f(bf16 x){ return __bfloat162float(x); }

__global__ void gemm_naive(const bf16* A, const bf16* B, float* C, int M, int N, int K){
  __shared__ float As[16][17], Bs[16][17];
  int tx=threadIdx.x, ty=threadIdx.y;
  int row=blockIdx.y*16+ty, col=blockIdx.x*16+tx;
  float acc=0;
  for(int k0=0;k0<K;k0+=16){
    As[ty][tx]=b2f(A[row*K+k0+tx]);
    Bs[ty][tx]=b2f(B[(k0+ty)*N+col]);
    __syncthreads();
    for(int k=0;k<16;k++) acc+=As[ty][k]*Bs[k][tx];
    __syncthreads();
  }
  C[row*N+col]=acc;
}
__device__ inline float phi(float t){ return (t>0? t+1.f : __expf(t)) + 1e-6f; }
__global__ void kv_kernel(const float* qkv, float* kv, float* z){
  int h=blockIdx.x; int d=threadIdx.x/16, e=threadIdx.x%16;
  float acc=0, zs=0;
  for(int s=0;s<S;s++){
    float pk=phi(qkv[s*1536+512+h*16+d]);
    float v=qkv[s*1536+1024+h*16+e];
    acc+=pk*v; zs+=pk;
  }
  kv[h*256+d*16+e]=acc; if(e==0) z[h*16+d]=zs;
}
__global__ void o_kernel(const float* qkv, const float* kv, const float* z, bf16* o){
  int s=blockIdx.x; int c=threadIdx.x; int h=c/16, e=c%16;
  float num=0, den=0;
  for(int d=0;d<16;d++){ float pq=phi(qkv[s*1536+h*16+d]); num+=pq*kv[h*256+d*16+e]; den+=pq*z[h*16+d]; }
  o[s*512+c]=__float2bfloat16(num/(den+1e-6f));
}
__global__ void h_kernel(const float* g, const float* u, bf16* h){
  int i=blockIdx.x*256+threadIdx.x;
  float a=g[i]; h[i]=__float2bfloat16(a/(1.f+__expf(-a))*u[i]);
}
__global__ void final_kernel(const bf16* x, const bf16* gw, const float* a, const float* b, bf16* out){
  int s=blockIdx.x; __shared__ float red[512];
  int c=threadIdx.x; red[c]=b2f(x[s*512+c])*b2f(gw[c]); __syncthreads();
  for(int o=256;o>0;o>>=1){ if(c<o) red[c]+=red[c+o]; __syncthreads(); }
  float g=1.f/(1.f+__expf(-red[0]));
  out[s*512+c]=__float2bfloat16(g*a[s*512+c]+(1.f-g)*b[s*512+c]);
}
extern "C" void kernel_launch(void* const* d_in, const int* in_sizes, int n_in,
                              void* d_out, int out_size, void* d_ws, size_t ws_size,
                              hipStream_t stream) {
  const bf16* x=(const bf16*)d_in[0]; const bf16* wqkv=(const bf16*)d_in[1]; const bf16* wo=(const bf16*)d_in[2];
  const bf16* w1=(const bf16*)d_in[4]; const bf16* w3=(const bf16*)d_in[5]; const bf16* w2=(const bf16*)d_in[6]; const bf16* gw=(const bf16*)d_in[7];
  float* ws=(float*)d_ws;
  float* qkv=ws; float* gt=qkv+S*1536; float* up=gt+S*F; float* kv=up+S*F; float* z=kv+H*256; float* a=z+512; float* b=a+S*D;
  bf16* o=(bf16*)(b+S*D); bf16* h=o+S*D;
  dim3 t(16,16);
  gemm_naive<<<dim3(1536/16,S/16),t,0,stream>>>(x,wqkv,qkv,S,1536,D);
  gemm_naive<<<dim3(F/16,S/16),t,0,stream>>>(x,w1,gt,S,F,D);
  gemm_naive<<<dim3(F/16,S/16),t,0,stream>>>(x,w3,up,S,F,D);
  kv_kernel<<<H,256,0,stream>>>(qkv,kv,z);
  o_kernel<<<S,512,0,stream>>>(qkv,kv,z,o);
  h_kernel<<<S*F/256,256,0,stream>>>(gt,up,h);
  gemm_naive<<<dim3(D/16,S/16),t,0,stream>>>(o,wo,a,S,D,D);
  gemm_naive<<<dim3(D/16,S/16),t,0,stream>>>(h,w2,b,S,D,F);
  final_kernel<<<S,512,0,stream>>>(x,gw,a,b,(bf16*)d_out);
}
```

```cpp
#include <hip/hip_runtime.h>
#include <hip/hip_bf16.h>
#include <stdint.h>

#define GAS __attribute__((address_space(1)))
#define LAS __attribute__((address_space(3)))
typedef unsigned v4u __attribute__((ext_vector_type(4)));
typedef unsigned v2u __attribute__((ext_vector_type(2)));
typedef float f32x4 __attribute__((ext_vector_type(4)));
typedef float f32x2 __attribute__((ext_vector_type(2)));
typedef short s16x4 __attribute__((ext_vector_type(4)));
typedef __bf16 bf16x8 __attribute__((ext_vector_type(8)));
typedef __bf16 bf16x2 __attribute__((ext_vector_type(2)));
typedef unsigned long long u64;
#define RLX_AGENT __ATOMIC_RELAXED, __HIP_MEMORY_SCOPE_AGENT

namespace fk {
constexpr int XB = 32768, WB = 20480, SLOT = XB + WB;
constexpr int LDS_BYTES = 163840;
constexpr int WREG = 20480;
constexpr int OFF_G = 4096;
constexpr int OFF_HF = 32768;
constexpr int OFF_KVFLAG = 65536;
constexpr int OFF_KVF = 69632;
constexpr int OFF_KVP = 131072;
constexpr int KVP_STR = 2304;
constexpr int OFF_H = 1 << 20;
constexpr int WS_USED = OFF_H + 256 * 36864;
constexpr int SC1 = 16;
constexpr unsigned SPIN_CAP = 1u << 22;

#define ST(k) do {} while (0)

__device__ __forceinline__ unsigned f2u(float f) { return __builtin_bit_cast(unsigned, f); }
__device__ __forceinline__ float u2f(unsigned u) { return __builtin_bit_cast(float, u); }
__device__ __forceinline__ unsigned pk2(float a, float b) {
  return __builtin_bit_cast(unsigned, __builtin_convertvector((f32x2){a, b}, bf16x2));
}
__device__ __forceinline__ f32x4 mma32(v4u a, v4u b, f32x4 c) {
  return __builtin_amdgcn_mfma_f32_16x16x32_bf16(__builtin_bit_cast(bf16x8, a), __builtin_bit_cast(bf16x8, b), c, 0, 0, 0);
}
__device__ __forceinline__ f32x4 mma16(v2u a, v2u b, f32x4 c) {
  return __builtin_amdgcn_mfma_f32_16x16x16bf16_1k(__builtin_bit_cast(s16x4, a), __builtin_bit_cast(s16x4, b), c, 0, 0, 0);
}
__device__ __forceinline__ v2u tr_read(LAS char* p) {
  return __builtin_bit_cast(v2u, __builtin_amdgcn_ds_read_tr16_b64_v4i16((LAS s16x4*)p));
}
__device__ __forceinline__ v4u tr_frag(LAS char* p, int second) {
  v2u a = tr_read(p), b = tr_read(p + second);
  return (v4u){a[0], a[1], b[0], b[1]};
}
__device__ __forceinline__ void glds16(const void* g, LAS char* l) {
  unsigned keep;
  asm volatile("s_mov_b32 %0, m0\n\ts_mov_b32 m0, %2\n\ts_nop 0\n\tglobal_load_lds_dwordx4 %1, off\n\ts_mov_b32 m0, %0"
               : "=&s"(keep) : "v"(g), "s"((unsigned)(uintptr_t)l) : "memory");
}
__device__ __forceinline__ float fexp(float x) { return __builtin_amdgcn_exp2f(x * 1.44269504088896f); }
__device__ __forceinline__ float frcp(float x) { return __builtin_amdgcn_rcpf(x); }
__device__ __forceinline__ float phi(float t) { return (t > 0.f ? t + 1.f : fexp(t)) + 1e-6f; }
__device__ __forceinline__ float bflo(unsigned u) { return u2f(u << 16); }
__device__ __forceinline__ float bfhi(unsigned u) { return u2f(u & 0xffff0000u); }
#define WAIT_VM(n) asm volatile("s_waitcnt vmcnt(" #n ")" ::: "memory")
#define WAIT_LGKM0() asm volatile("s_waitcnt lgkmcnt(0)" ::: "memory")
#define BAR() __builtin_amdgcn_s_barrier()
#define SCHED() __builtin_amdgcn_sched_barrier(0)

struct Stage1 {
  const char* xp0; const char* xp1;
  const char* wp[3]; unsigned wstr[3];
};

__device__ __forceinline__ void stage1(const Stage1& s, LAS char* slot, int wave, int lane, int kt) {
  LAS char* lx = slot + wave * 4096;
  glds16(s.xp0 + kt * 128, lx);
  glds16(s.xp1 + kt * 128, lx + 1024);
  glds16(s.xp0 + kt * 128 + 16384, lx + 2048);
  glds16(s.xp1 + kt * 128 + 16384, lx + 3072);
  LAS char* lw = slot + XB + wave * 2560;
  glds16(s.wp[0] + (size_t)kt * s.wstr[0], lw);
  glds16(s.wp[1] + (size_t)kt * s.wstr[1], lw + 1024);
  if (lane < 32) glds16(s.wp[2] + (size_t)kt * s.wstr[2], lw + 2048);
}

struct Frags { v4u x[4]; v4u w[5]; };
__device__ __forceinline__ void read_frags(Frags& f, LAS char* slot, int xo, int we, int wod, int ks) {
#pragma unroll
  for (int G = 0; G < 5; ++G) f.w[G] = tr_frag(slot + ((G & 1) ? wod : we) + G * 32 + ks * 10240, 1280);
#pragma unroll
  for (int tt = 0; tt < 4; ++tt) f.x[tt] = *(LAS v4u*)(slot + (xo ^ (ks * 64)) + tt * 2048);
}
template <bool KT> __device__ __forceinline__ void mma_step(f32x4 (&acc)[4][5], const Frags& f) {
#pragma unroll
  for (int G = 0; G < 5; ++G)
#pragma unroll
    for (int tt = 0; tt < 4; ++tt)
      acc[tt][G] = (KT && G < 4) ? mma32(f.x[tt], f.w[G], acc[tt][G]) : mma32(f.w[G], f.x[tt], acc[tt][G]);
}

template <bool KT>
__device__ __forceinline__ void gemm1(f32x4 (&acc)[4][5], const Stage1& s, LAS char* smem, int wave, int lane, int rot,
                                      int xo, int we, int wod) {
  Frags f0, f1;
  WAIT_VM(14); BAR(); SCHED();
  read_frags(f0, smem, xo, we, wod, 0);
  int so = 0;
#pragma unroll 1
  for (int t = 0; t < 7; ++t) {
    int sn = so + SLOT; if (sn == 3 * SLOT) sn = 0;
    read_frags(f1, smem + so, xo, we, wod, 1);
    mma_step<KT>(acc, f0);
    SCHED();
    if (t < 6) { asm volatile("s_waitcnt vmcnt(7) lgkmcnt(0)" ::: "memory"); } else { asm volatile("s_waitcnt vmcnt(0) lgkmcnt(0)" ::: "memory"); }
    BAR(); SCHED();
    if (t < 5) stage1(s, smem + so, wave, lane, (rot + t + 3) & 7);
    read_frags(f0, smem + sn, xo, we, wod, 0);
    mma_step<KT>(acc, f1);
    SCHED();
    so = sn;
  }
  read_frags(f1, smem + so, xo, we, wod, 1);
  mma_step<KT>(acc, f0);
  SCHED();
  WAIT_LGKM0(); BAR(); SCHED();
  mma_step<KT>(acc, f1);
}

__device__ __forceinline__ unsigned ffn_pair(f32x4 a, float omg) {
  auto s0 = __builtin_amdgcn_permlane32_swap(f2u(a[0]), f2u(a[2]), false, false);
  auto s1 = __builtin_amdgcn_permlane32_swap(f2u(a[1]), f2u(a[3]), false, false);
  float g0 = u2f(s0[0]), u0 = u2f(s0[1]);
  float g1 = u2f(s1[0]), u1 = u2f(s1[1]);
  float h0 = g0 * frcp(1.f + fexp(-g0)) * (u0 * omg);
  float h1 = g1 * frcp(1.f + fexp(-g1)) * (u1 * omg);
  return pk2(h0, h1);
}

__device__ __forceinline__ void load_gates(float (&g)[4], const GAS u64* gp, unsigned tag32, int lane) {
  u64 v;
  for (unsigned spins = 0;; ++spins) {
    v = __hip_atomic_load(gp + lane, RLX_AGENT);
    if (__all((unsigned)(v >> 32) == tag32) || spins > SPIN_CAP) break;
    __builtin_amdgcn_s_sleep(1);
  }
  unsigned gv = (unsigned)v;
#pragma unroll
  for (int tt = 0; tt < 4; ++tt) g[tt] = u2f(__builtin_amdgcn_ds_bpermute((tt * 16 + (lane & 15)) * 4, gv));
}

__global__ __launch_bounds__(512, 2) void fused(const char* __restrict__ x, const char* __restrict__ wqkv, const char* __restrict__ wo,
                                                const char* __restrict__ w1, const char* __restrict__ w3, const char* __restrict__ w2,
                                                const char* __restrict__ gw, char* __restrict__ out, char* ws) {
  __shared__ __attribute__((aligned(16))) char smem_[LDS_BYTES];
  LAS char* smem = (LAS char*)smem_;
  ST(0);
  const int tid = threadIdx.x, lane = tid & 63, wave = __builtin_amdgcn_readfirstlane(tid >> 6);
  const int bid = blockIdx.x, xg = bid & 7, idx = bid >> 3;
  const int lg = lane >> 4, lc = lane & 15;
  const int p = (xg >> 2) * 4 + (idx & 3), j = idx >> 2, e = (xg & 3) * 2 + (j >> 2), ty = j & 3;
  const int rot = (j + 2 * (idx & 3)) & 7;
  const int wm = wave & 3, wn = wave >> 2;
  const int fq = 224 * e + ((e & 1) ? 160 : 0), ff0 = 224 * e + ((e & 1) ? 0 : 64);
  const __amdgpu_buffer_rsrc_t wsr = __builtin_amdgcn_make_buffer_rsrc(ws, 0, WS_USED, 0x00020000);

  const int gtok = p * 256 + (e * 4 + ty) * 8 + wave;
  v4u xr, gr; v2u nn;
  asm volatile("global_load_dwordx4 %0, %1, off" : "=v"(xr) : "v"(x + gtok * 1024 + lane * 16) : "memory");
  asm volatile("global_load_dwordx4 %0, %1, off" : "=v"(gr) : "v"(gw + lane * 16) : "memory");
  asm volatile("global_load_dwordx2 %0, %1, off sc1" : "=v"(nn) : "v"(ws) : "memory");

  Stage1 s1;
  {
    int r0 = p * 256 + wave * 32 + (lane >> 3);
    s1.xp0 = x + (size_t)r0 * 1024 + 16 * ((lane & 7) ^ lg);
    s1.xp1 = x + (size_t)(r0 + 8) * 1024 + 16 * ((lane & 7) ^ (4 + lg));
#pragma unroll
    for (int i = 0; i < 3; ++i) {
      int u = i * 64 + lane; if (u > 159) u = 159;
      int rr = u / 20, pos = u - rr * 20, row = wave * 8 + rr;
      int n = (pos >> 1) ^ (wave & 1), half = pos & 1;
      int cwn = n >= 5 ? 1 : 0, G = n - cwn * 5;
      const char* base; int col, str;
      if (G == 4 && ty < 2) { base = wqkv; str = 3072; col = 16 * (4 * e + 2 * ty + cwn) + 8 * half; }
      else if (ty == 0) { base = wqkv; str = 3072; col = 512 + 512 * (G & 1) + 16 * (4 * e + 2 * cwn + (G >> 1)) + 8 * half; }
      else { base = half ? w3 : w1; str = 3584; col = (ty == 1 ? fq + 32 * cwn : ff0 + 80 * (ty - 2) + 40 * cwn) + 8 * G; }
      s1.wp[i] = base + (size_t)row * str + col * 2;
      s1.wstr[i] = 64 * str;
    }
  }
  stage1(s1, smem, wave, lane, rot);
  stage1(s1, smem + SLOT, wave, lane, (rot + 1) & 7);
  stage1(s1, smem + 2 * SLOT, wave, lane, (rot + 2) & 7);
  asm volatile("s_waitcnt vmcnt(21)" : "+v"(xr), "+v"(gr), "+v"(nn)::"memory");
  const u64 tag = (((u64)__builtin_amdgcn_readfirstlane(nn[1]) << 32) | __builtin_amdgcn_readfirstlane(nn[0])) + 0x9E3779B97F4A7C15ull;
  const unsigned tag32 = (unsigned)tag;
  {
    float d = 0.f;
#pragma unroll
    for (int i = 0; i < 4; ++i) d += bflo(xr[i]) * bflo(gr[i]) + bfhi(xr[i]) * bfhi(gr[i]);
#pragma unroll
    for (int o = 32; o > 0; o >>= 1) d += __shfl_xor(d, o);
    float g = frcp(1.f + fexp(-d));
    if (lane == 0) __hip_atomic_store((GAS u64*)(ws + OFF_G) + gtok, ((u64)tag32 << 32) | f2u(g), RLX_AGENT);
  }
  ST(1);
  f32x4 acc[4][5];
#pragma unroll
  for (int tt = 0; tt < 4; ++tt)
#pragma unroll
    for (int G = 0; G < 5; ++G) acc[tt][G] = (f32x4){0.f, 0.f, 0.f, 0.f};
  {
    const int xo = (wm * 64 + lc) * 128 + 16 * (lg ^ (lc >> 1));
    const int wc = XB + (8 * lg + (lc >> 2)) * 320 + 8 * (lane & 3) + wn * 160;
    const int sg = (lg & 1) * 32;
    const int we = wn ? wc - sg : wc + sg, wod = wn ? wc + sg : wc - sg;
    if (ty == 0) gemm1<true>(acc, s1, smem, wave, lane, rot, xo, we, wod);
    else gemm1<false>(acc, s1, smem, wave, lane, rot, xo, we, wod);
  }
  ST(2);
  const int tb2 = xg * 4 + (idx & 3), nb = idx >> 2, e2 = wave;
  LAS char* wreg = smem + wave * WREG;
  const int jj = lane >> 3;
  const int fq2 = 224 * e2 + ((e2 & 1) ? 160 : 0), ff2 = 224 * e2 + ((e2 & 1) ? 0 : 64);
  unsigned wcol[2];
#pragma unroll
  for (int c = 0; c < 2; ++c) wcol[c] = nb * 128 + ((((lane & 7) >> 1) ^ (((jj >> 1) & 1) | (c << 1))) * 32) + (lane & 1) * 16;
  auto stage2 = [&](int s, LAS char* dst) {
    const char* base; int rl;
    if (s < 6) { base = w2 + (size_t)(s < 2 ? fq2 + 32 * s : ff2 + 40 * (s - 2)) * 1024; rl = 8 * (jj >> 1) + (jj & 1); }
    else if (s == 6) { base = w2 + (size_t)(ff2 + 32) * 1024; rl = 40 * (jj >> 1) + (jj & 1); }
    else { base = wo + (size_t)(64 * e2 + 32 * (s - 7)) * 1024; rl = 16 * (jj >> 2) + (jj & 3); }
#pragma unroll
    for (int c = 0; c < 4; ++c) {
      int ru = s < 7 ? 4 * (c & 1) + 2 * (c >> 1) : 4 * c;
      glds16(base + (rl + ru) * 1024 + wcol[c & 1], dst + c * 1024);
    }
  };
  const int tb1 = p * 4 + wm;
  float gt[4];
  if (ty != 0) load_gates(gt, (const GAS u64*)(ws + OFF_G) + tb1 * 64, tag32, lane);
  SCHED();
#pragma unroll
  for (int s = 0; s < 4; ++s) stage2(s, wreg + s * 4096);
  SCHED();
  const int hbase = OFF_H + (tb1 * 8 + e) * 36864 + lane * 16;
  if (ty == 0) {
    const v4u ones = {0x3f803f80u, 0x3f803f80u, 0x3f803f80u, 0x3f803f80u};
#pragma unroll
    for (int i2 = 0; i2 < 2; ++i2) {
      f32x4 kv = {0.f, 0.f, 0.f, 0.f}, z = {0.f, 0.f, 0.f, 0.f};
#pragma unroll
      for (int u = 0; u < 2; ++u) {
        f32x4 k0 = acc[2 * u][2 * i2], k1 = acc[2 * u + 1][2 * i2], v0 = acc[2 * u][2 * i2 + 1], v1 = acc[2 * u + 1][2 * i2 + 1];
        v4u pa = {pk2(phi(k0[0]), phi(k0[1])), pk2(phi(k0[2]), phi(k0[3])), pk2(phi(k1[0]), phi(k1[1])), pk2(phi(k1[2]), phi(k1[3]))};
        v4u vb = {pk2(v0[0], v0[1]), pk2(v0[2], v0[3]), pk2(v1[0], v1[1]), pk2(v1[2], v1[3])};
        kv = mma32(pa, vb, kv);
        z = mma32(pa, ones, z);
      }
      *(LAS f32x4*)(wreg + 16384 + i2 * 2048 + lane * 16) = kv;
      *(LAS f32x4*)(wreg + 16384 + i2 * 2048 + 1024 + lane * 16) = z;
    }
    WAIT_LGKM0(); BAR(); SCHED();
    if (wave < 4) {
      f32x4 kv = {0.f, 0.f, 0.f, 0.f}, z = {0.f, 0.f, 0.f, 0.f};
#pragma unroll
      for (int m = 0; m < 4; ++m) {
        LAS char* src = smem + ((wave >> 1) * 4 + m) * WREG + 16384 + (wave & 1) * 2048 + lane * 16;
        kv += *(LAS f32x4*)src; z += *(LAS f32x4*)(src + 1024);
      }
      int o = OFF_KVP + ((4 * e + wave) * 8 + p) * KVP_STR;
      v4u a = {f2u(kv[0]), tag32, f2u(kv[1]), tag32};
      v4u b = {f2u(kv[2]), tag32, f2u(kv[3]), tag32};
      __builtin_amdgcn_raw_buffer_store_b128(a, wsr, o + lane * 16, 0, SC1);
      __builtin_amdgcn_raw_buffer_store_b128(b, wsr, o + 1024 + lane * 16, 0, SC1);
      if (lc == 0) {
        v4u c = {f2u(z[0]), tag32, f2u(z[1]), tag32};
        v4u d = {f2u(z[2]), tag32, f2u(z[3]), tag32};
        __builtin_amdgcn_raw_buffer_store_b128(c, wsr, o + 2048 + lg * 32, 0, SC1);
        __builtin_amdgcn_raw_buffer_store_b128(d, wsr, o + 2048 + lg * 32 + 16, 0, SC1);
      }
    }
    WAIT_LGKM0(); BAR(); SCHED();
  } else {
    const int slot = 2 * (ty - 1) + wn;
#pragma unroll
    for (int tt = 0; tt < 4; ++tt) {
      float omg = 1.f - gt[tt];
      v4u hv = {ffn_pair(acc[tt][0], omg), ffn_pair(acc[tt][1], omg), ffn_pair(acc[tt][2], omg), ffn_pair(acc[tt][3], omg)};
      __builtin_amdgcn_raw_buffer_store_b128(hv, wsr, hbase + (slot * 4 + tt) * 1024, 0, SC1);
    }
    if (ty >= 2) {
      v4u hv = {ffn_pair(acc[0][4], 1.f - gt[0]), ffn_pair(acc[1][4], 1.f - gt[1]), ffn_pair(acc[2][4], 1.f - gt[2]), ffn_pair(acc[3][4], 1.f - gt[3])};
      __builtin_amdgcn_raw_buffer_store_b128(hv, wsr, hbase + (24 + (ty - 2) * 2 + wn) * 1024, 0, SC1);
    }
  }
  if (ty < 2) {
#pragma unroll
    for (int u = 0; u < 2; ++u) {
      f32x4 q0 = acc[2 * u][4], q1 = acc[2 * u + 1][4];
      v4u qv = {pk2(phi(q0[0]), phi(q0[1])), pk2(phi(q0[2]), phi(q0[3])), pk2(phi(q1[0]), phi(q1[1])), pk2(phi(q1[2]), phi(q1[3]))};
      __builtin_amdgcn_raw_buffer_store_b128(qv, wsr, hbase + (28 + (2 * ty + wn) * 2 + u) * 1024, 0, SC1);
    }
  }
  ST(3);
  WAIT_VM(0);
  if (lane == 0) __hip_atomic_store((GAS u64*)(ws + OFF_HF + (tb1 * 8 + e) * 128) + ty * 2 + wn, tag, RLX_AGENT);
  ST(4);
  if (ty == 0 && !(p & 1) && wave == 5) {
    const int h = 4 * e + (p >> 1);
    const int o = OFF_KVP + h * 8 * KVP_STR;
    f32x4 kv; float zs;
    for (unsigned spins = 0;; ++spins) {
      v4u a[8], b[8]; v2u zz[8];
#pragma unroll
      for (int q = 0; q < 8; ++q) {
        a[q] = __builtin_amdgcn_raw_buffer_load_b128(wsr, o + q * KVP_STR + lane * 16, 0, SC1);
        b[q] = __builtin_amdgcn_raw_buffer_load_b128(wsr, o + q * KVP_STR + 1024 + lane * 16, 0, SC1);
        zz[q] = __builtin_amdgcn_raw_buffer_load_b64(wsr, o + q * KVP_STR + 2048 + lc * 8, 0, SC1);
      }
      bool ok = true; kv = (f32x4){0.f, 0.f, 0.f, 0.f}; zs = 0.f;
#pragma unroll
      for (int q = 0; q < 8; ++q) {
        ok &= (a[q][1] == tag32) & (a[q][3] == tag32) & (b[q][1] == tag32) & (b[q][3] == tag32) & (zz[q][1] == tag32);
        kv += (f32x4){u2f(a[q][0]), u2f(a[q][2]), u2f(b[q][0]), u2f(b[q][2])};
        zs += u2f(zz[q][0]);
      }
      if (__all(ok) || spins > SPIN_CAP) break;
      __builtin_amdgcn_s_sleep(1);
    }
    float zf[4];
#pragma unroll
    for (int r = 0; r < 4; ++r) {
      float zv = u2f(__builtin_amdgcn_ds_bpermute((4 * lg + r) * 4, f2u(zs)));
      float hi = bflo(pk2(zv, 0.f));
      zf[r] = (lc & 3) == 0 ? hi : ((lc & 3) == 1 ? zv - hi : 0.f);
    }
    v4u f = {pk2(kv[0], kv[1]), pk2(kv[2], kv[3]), pk2(zf[0], zf[1]), pk2(zf[2], zf[3])};
    __builtin_amdgcn_raw_buffer_store_b128(f, wsr, OFF_KVF + h * 1024 + lane * 16, 0, SC1);
    WAIT_VM(0);
    if (lane == 0) __hip_atomic_store((GAS u64*)(ws + OFF_KVFLAG + e * 128) + (p >> 1), tag, RLX_AGENT);
  }
  if (bid == 8 * 4 && wave == 0) {
    for (unsigned spins = 0;; ++spins) {
      bool ok = true;
      v4u a[16];
#pragma unroll
      for (int q = 0; q < 16; ++q) a[q] = __builtin_amdgcn_raw_buffer_load_b128(wsr, OFF_G + q * 1024 + lane * 16, 0, SC1);
      SCHED();
#pragma unroll
      for (int q = 0; q < 16; ++q) ok &= (a[q][1] == tag32) & (a[q][3] == tag32);
      if (__all(ok) || spins > SPIN_CAP) break;
      __builtin_amdgcn_s_sleep(1);
    }
    if (lane == 0) __hip_atomic_store((GAS u64*)ws, tag, RLX_AGENT);
  }
  v4u W[9][4];
  int tro[4];
#pragma unroll
  for (int nt = 0; nt < 4; ++nt) tro[nt] = (8 * lg + (lc >> 2)) * 128 + ((nt ^ (((lc >> 3) & 1) | ((lg & 1) << 1))) * 32) + 8 * (lane & 3);
#pragma unroll
  for (int s = 0; s < 4; ++s)
#pragma unroll
    for (int nt = 0; nt < 4; ++nt) W[s][nt] = tr_frag(wreg + s * 4096 + tro[nt], 512);
  WAIT_LGKM0(); SCHED();
#pragma unroll
  for (int s = 4; s < 9; ++s) stage2(s, wreg + (s - 4) * 4096);
  SCHED();
  WAIT_VM(0); SCHED();
#pragma unroll
  for (int s = 4; s < 9; ++s)
#pragma unroll
    for (int nt = 0; nt < 4; ++nt) W[s][nt] = tr_frag(wreg + (s - 4) * 4096 + tro[nt], 512);
  ST(5);
  {
    const GAS u64* fl = (const GAS u64*)(ws + OFF_HF + (tb2 * 8 + e2) * 128) + (lane & 7);
    for (unsigned spins = 0;; ++spins) {
      u64 v = __hip_atomic_load(fl, RLX_AGENT);
      if (__all(v == tag) || spins > SPIN_CAP) break;
      __builtin_amdgcn_s_sleep(1);
    }
    __builtin_amdgcn_fence(__ATOMIC_ACQUIRE, "wavefront");
  }
  ST(6);
  f32x4 c2[4][4];
#pragma unroll
  for (int tt = 0; tt < 4; ++tt)
#pragma unroll
    for (int nt = 0; nt < 4; ++nt) c2[tt][nt] = (f32x4){0.f, 0.f, 0.f, 0.f};
  const int hb2 = OFF_H + (tb2 * 8 + e2) * 36864 + lane * 16;
  v4u hq[9][4];
  auto ldq = [&](int qd) {
#pragma unroll
    for (int i = 0; i < 4; ++i) hq[qd][i] = __builtin_amdgcn_raw_buffer_load_b128(wsr, hb2 + (qd * 4 + i) * 1024, 0, SC1);
  };
  auto mmq = [&](int s, const v4u (&hf)[4]) {
#pragma unroll
    for (int tt = 0; tt < 4; ++tt)
#pragma unroll
      for (int nt = 0; nt < 4; ++nt) c2[tt][nt] = mma32(W[s][nt], hf[tt], c2[tt][nt]);
  };
  ldq(0); ldq(1); SCHED();
  mmq(0, hq[0]); SCHED(); ldq(2); ldq(3); SCHED();
  mmq(1, hq[1]); SCHED(); ldq(4); ldq(5); SCHED();
  mmq(2, hq[2]); SCHED(); ldq(6); ldq(7); SCHED();
  mmq(3, hq[3]); SCHED(); ldq(8); SCHED();
  mmq(4, hq[4]); SCHED();
  mmq(5, hq[5]); SCHED();
  {
    v4u hf[4];
#pragma unroll
    for (int tt = 0; tt < 4; ++tt) hf[tt] = (v4u){hq[6][0][tt], hq[6][1][tt], hq[6][2][tt], hq[6][3][tt]};
    mmq(6, hf);
  }
  ST(7);
  {
    const GAS u64* fl = (const GAS u64*)(ws + OFF_KVFLAG + e2 * 128) + (lane & 3);
    for (unsigned spins = 0;; ++spins) {
      u64 v = __hip_atomic_load(fl, RLX_AGENT);
      if (__all(v == tag) || spins > SPIN_CAP) break;
      __builtin_amdgcn_s_sleep(1);
    }
    __builtin_amdgcn_fence(__ATOMIC_ACQUIRE, "wavefront");
  }
  v4u kvf[4];
#pragma unroll
  for (int i = 0; i < 4; ++i) kvf[i] = __builtin_amdgcn_raw_buffer_load_b128(wsr, OFF_KVF + (4 * e2 + i) * 1024 + lane * 16, 0, SC1);
  float g2[4];
  load_gates(g2, (const GAS u64*)(ws + OFF_G) + tb2 * 64, tag32, lane);
  ST(8);
#pragma unroll
  for (int so = 0; so < 2; ++so) {
    v4u of[4];
#pragma unroll
    for (int tt = 0; tt < 4; ++tt) {
#pragma unroll
      for (int hh = 0; hh < 2; ++hh) {
        const int i = 2 * so + hh;
        const v4u qq = hq[7 + so][hh * 2 + (tt >> 1)];
        const v2u pq = {qq[(tt & 1) * 2], qq[(tt & 1) * 2 + 1]};
        const f32x4 zero = {0.f, 0.f, 0.f, 0.f};
        f32x4 num = mma16((v2u){kvf[i][0], kvf[i][1]}, pq, zero);
        f32x4 den = mma16((v2u){kvf[i][2], kvf[i][3]}, pq, zero);
        float sc = g2[tt] * frcp(den[0] + den[1] + 1e-6f);
        of[tt][hh * 2] = pk2(num[0] * sc, num[1] * sc);
        of[tt][hh * 2 + 1] = pk2(num[2] * sc, num[3] * sc);
      }
    }
    mmq(7 + so, of);
  }
  ST(9);
#pragma unroll
  for (int tt = 0; tt < 4; ++tt)
#pragma unroll
    for (int nt = 0; nt < 4; ++nt)
      *(LAS f32x4*)(wreg + (tt * 4 + nt) * 1024 + lg * 256 + ((lc ^ (4 * nt + 2 * (lg >> 1))) & 15) * 16) = c2[tt][nt];
  WAIT_LGKM0(); BAR(); SCHED();
  ST(10);
  {
    const int o = lane & 7, c = (wave & 1) * 8 + (lane >> 3), tt = wave >> 1;
    const int ro = (tt * 4 + (o >> 1)) * 1024 + (o & 1) * 512 + ((c ^ (2 * o)) & 15) * 16;
    f32x4 a = {0.f, 0.f, 0.f, 0.f}, b = {0.f, 0.f, 0.f, 0.f};
#pragma unroll
    for (int w = 0; w < 8; ++w) { a += *(LAS f32x4*)(smem + w * WREG + ro); b += *(LAS f32x4*)(smem + w * WREG + ro + 256); }
    v4u r = {pk2(a[0], a[1]), pk2(a[2], a[3]), pk2(b[0], b[1]), pk2(b[2], b[3])};
    *(GAS v4u*)(out + (size_t)(tb2 * 64 + tt * 16 + c) * 1024 + nb * 128 + o * 16) = r;
  }
}
}

extern "C" void kernel_launch(void* const* d_in, const int* in_sizes, int n_in, void* d_out, int out_size, void* d_ws, size_t ws_size,
                              hipStream_t stream) {
  fk::fused<<<256, 512, 0, stream>>>((const char*)d_in[0], (const char*)d_in[1], (const char*)d_in[2], (const char*)d_in[4],
                                     (const char*)d_in[5], (const char*)d_in[6], (const char*)d_in[7], (char*)d_out, (char*)d_ws);
}
```

```cpp
#include <hip/hip_runtime.h>
#include <hip/hip_bf16.h>
#include <stdint.h>
#include <type_traits>

#define GAS __attribute__((address_space(1)))
#define LAS __attribute__((address_space(3)))
typedef unsigned v4u __attribute__((ext_vector_type(4)));
typedef unsigned v2u __attribute__((ext_vector_type(2)));
typedef float f32x4 __attribute__((ext_vector_type(4)));
typedef float f32x2 __attribute__((ext_vector_type(2)));
typedef short s16x4 __attribute__((ext_vector_type(4)));
typedef __bf16 bf16x8 __attribute__((ext_vector_type(8)));
typedef __bf16 bf16x2 __attribute__((ext_vector_type(2)));
typedef unsigned long long u64;
#define RLX_AGENT __ATOMIC_RELAXED, __HIP_MEMORY_SCOPE_AGENT

namespace fk {
constexpr int XB = 32768, WB = 20480, SLOT = XB + WB;
constexpr int LDS_BYTES = 3 * SLOT;
constexpr int PIECE = SLOT / 8;
constexpr int OFF_G = 4096;
constexpr int OFF_HF = 32768;
constexpr int OFF_KVF = 69632;
constexpr int OFF_KVP = 131072;
constexpr int KVP_STR = 2304;
constexpr int OFF_H = 1 << 20;
constexpr int WS_USED = OFF_H + 256 * 36864;
constexpr int SC1 = 16;
constexpr unsigned SPIN_CAP = 1u << 15;

#define ST(k) do {} while (0)
#define CY(t) do {} while (0)

__device__ __forceinline__ unsigned f2u(float f) { return __builtin_bit_cast(unsigned, f); }
__device__ __forceinline__ float u2f(unsigned u) { return __builtin_bit_cast(float, u); }
__device__ __forceinline__ unsigned pk2(float a, float b) {
  return __builtin_bit_cast(unsigned, __builtin_convertvector((f32x2){a, b}, bf16x2));
}
__device__ __forceinline__ f32x4 mma32(v4u a, v4u b, f32x4 c) {
  return __builtin_amdgcn_mfma_f32_16x16x32_bf16(__builtin_bit_cast(bf16x8, a), __builtin_bit_cast(bf16x8, b), c, 0, 0, 0);
}
__device__ __forceinline__ f32x4 mma16(v2u a, v2u b, f32x4 c) {
  return __builtin_amdgcn_mfma_f32_16x16x16bf16_1k(__builtin_bit_cast(s16x4, a), __builtin_bit_cast(s16x4, b), c, 0, 0, 0);
}
__device__ __forceinline__ v2u tr_read(LAS char* p) {
  return __builtin_bit_cast(v2u, __builtin_amdgcn_ds_read_tr16_b64_v4i16((LAS s16x4*)p));
}
__device__ __forceinline__ v4u cat(v2u a, v2u b) { return (v4u){a[0], a[1], b[0], b[1]}; }
__device__ __forceinline__ void glds16(const void* g, unsigned l) {
  asm volatile("s_mov_b32 m0, %1\n\ts_nop 0\n\tglobal_load_lds_dwordx4 %0, off" ::"v"(g), "s"(l) : "memory");
}
__device__ __forceinline__ void glds16s(unsigned voff, const void* sbase, unsigned l) {
  asm volatile("s_mov_b32 m0, %2\n\ts_nop 0\n\tglobal_load_lds_dwordx4 %0, %1" ::"v"(voff), "s"(sbase), "s"(l) : "memory");
}
__device__ __forceinline__ void glds16_lo(const void* g, unsigned l) {
  asm volatile("s_mov_b32 m0, %1\n\ts_mov_b32 exec_hi, 0\n\tglobal_load_lds_dwordx4 %0, off\n\ts_mov_b32 exec_hi, -1" ::"v"(g), "s"(l) : "memory");
}
__device__ __forceinline__ void ld8_sc1(v2u& d, unsigned voff, const void* sbase) {
  asm volatile("global_load_dwordx2 %0, %1, %2 sc1" : "=v"(d) : "v"(voff), "s"(sbase) : "memory");
}
__device__ __forceinline__ float fexp(float x) { return __builtin_amdgcn_exp2f(x * 1.44269504088896f); }
__device__ __forceinline__ float frcp(float x) { return __builtin_amdgcn_rcpf(x); }
__device__ __forceinline__ float bflo(unsigned u) { return u2f(u << 16); }
__device__ __forceinline__ float bfhi(unsigned u) { return u2f(u & 0xffff0000u); }
#define WAIT_VM(n) asm volatile("s_waitcnt vmcnt(" #n ")" ::: "memory")
#define WAIT_LGKM0() asm volatile("s_waitcnt lgkmcnt(0)" ::: "memory")
#define BAR() __builtin_amdgcn_s_barrier()
#define SCHED() __builtin_amdgcn_sched_barrier(0)

struct Frags { v4u x[4]; v2u wl[5], wh[5]; };
struct FragAddr { int xo, w0, w4; };
__device__ __forceinline__ void frag_read(Frags& f, int i, LAS char* sp, const FragAddr& a, int ks) {
  if (i >= 2 && i < 6) { f.x[i - 2] = *(LAS v4u*)(sp + (a.xo ^ (ks * 64)) + (i - 2) * 2048); return; }
  const int r = i < 2 ? i : i - 4, G = r >> 1;
  LAS char* p = G < 4 ? sp + (a.w0 ^ ((G >> 1) * 32)) + (G & 1) * 1024 + ks * 10240 + (r & 1) * 512 : sp + a.w4 + ks * 10240 + (r & 1) * 256;
  if (r & 1) f.wh[G] = tr_read(p); else f.wl[G] = tr_read(p);
}
template <bool KT, bool RD, int PER, class Act>
__device__ __forceinline__ void half_step(f32x4 (&acc)[4][5], const Frags& c, Frags& n, LAS char* sp, const FragAddr& a, int ks, Act act) {
#pragma unroll
  for (int i = 0; i < 20; ++i) {
    const int G = i >> 2, tt = i & 3;
    const v4u w = cat(c.wl[G], c.wh[G]);
    acc[tt][G] = (KT && G < 4) ? mma32(c.x[tt], w, acc[tt][G]) : mma32(w, c.x[tt], acc[tt][G]);
    if (RD && i < 14) frag_read(n, i, sp, a, ks);
    if (i % PER == 1) act(i / PER);
    SCHED();
  }
}
__device__ __forceinline__ unsigned swiglu2(f32x2 g, f32x2 u, float io) {
  const f32x2 t = g * -1.44269504088896f;
  const f32x2 e = {__builtin_amdgcn_exp2f(t[0]), __builtin_amdgcn_exp2f(t[1])};
  const f32x2 i2 = {io, io};
  const f32x2 d = __builtin_elementwise_fma(e, i2, i2);
  const f32x2 r = {frcp(d[0]), frcp(d[1])};
  return __builtin_bit_cast(unsigned, __builtin_convertvector((g * u) * r, bf16x2));
}
__device__ __forceinline__ f32x2 lo2(f32x4 a) { return (f32x2){a[0], a[1]}; }
__device__ __forceinline__ f32x2 hi2(f32x4 a) { return (f32x2){a[2], a[3]}; }
__device__ __forceinline__ unsigned ffn_mixed(f32x4 a, float io) {
  auto s0 = __builtin_amdgcn_permlane32_swap(f2u(a[0]), f2u(a[2]), false, false);
  auto s1 = __builtin_amdgcn_permlane32_swap(f2u(a[1]), f2u(a[3]), false, false);
  return swiglu2((f32x2){u2f(s0[0]), u2f(s1[0])}, (f32x2){u2f(s0[1]), u2f(s1[1])}, io);
}
__device__ __forceinline__ unsigned phi2(f32x2 t) {
  const f32x2 s = t * 1.44269504088896f;
  const f32x2 e = {__builtin_amdgcn_fmed3f(__builtin_amdgcn_exp2f(s[0]), 0.f, 1.f), __builtin_amdgcn_fmed3f(__builtin_amdgcn_exp2f(s[1]), 0.f, 1.f)};
  const f32x2 m = {__builtin_fmaxf(t[0], 0.f), __builtin_fmaxf(t[1], 0.f)};
  return __builtin_bit_cast(unsigned, __builtin_convertvector((m + e) + 1e-6f, bf16x2));
}
__device__ __forceinline__ v4u phi8(f32x4 a, f32x4 b) { return (v4u){phi2(lo2(a)), phi2(hi2(a)), phi2(lo2(b)), phi2(hi2(b))}; }
template <int CTRL, int RM> __device__ __forceinline__ float dpp_add(float v) {
  return v + u2f(__builtin_amdgcn_update_dpp(0u, f2u(v), CTRL, RM, 0xf, false));
}
__device__ __forceinline__ constexpr int chunk_off(int k) { return (k < 6 ? 2 * SLOT : (k < 12 ? 0 : SLOT)) + (k % 6) * 1024; }

#define SPREAD 1
#define PR(n) (SPREAD ? (n) : 3)
#define ROTMODE 0
__global__ __launch_bounds__(512, 2) void fused(const char* __restrict__ x, const char* __restrict__ wqkv, const char* __restrict__ wo,
                                                const char* __restrict__ w1, const char* __restrict__ w3, const char* __restrict__ w2,
                                                const char* __restrict__ gw, char* __restrict__ out, char* ws) {
  __shared__ __attribute__((aligned(16))) char smem_[LDS_BYTES];
  LAS char* smem = (LAS char*)smem_;
  const unsigned lds0 = (unsigned)(uintptr_t)smem;
  ST(0);
  asm volatile("" ::"s"(x), "s"(wqkv), "s"(wo), "s"(w1), "s"(w3), "s"(w2), "s"(gw), "s"(out), "s"(ws));
  ST(24);
  const int tid = threadIdx.x, lane = tid & 63, wave = __builtin_amdgcn_readfirstlane(tid >> 6);
  const int bid = blockIdx.x, xg = bid & 7, idx = bid >> 3;
  const int lg = lane >> 4, lc = lane & 15;
  const int p = (xg >> 2) * 4 + (idx & 3), j = idx >> 2, e = (xg & 3) * 2 + (j >> 2), ty = j & 3;
  const int rot = ROTMODE == 0 ? 0 : ROTMODE == 1 ? (2 * xg + (xg & 4)) & 7 : ROTMODE == 2 ? (j + 2 * (idx & 3)) & 7 : ((j ^ idx) & 1) * 4;
  const int wm = wave & 3, wn = wave >> 2;
  const __amdgpu_buffer_rsrc_t wsr = __builtin_amdgcn_make_buffer_rsrc(ws, 0, WS_USED, 0x00020000);
  const int gtok = p * 256 + (e * 4 + ty) * 8 + wave;
  v4u xr, gr; v2u nn;
  asm volatile("global_load_dwordx2 %0, %1, %2 sc1" : "=v"(nn) : "v"(0), "s"(ws) : "memory");
  asm volatile("global_load_dwordx4 %0, %1, %2" : "=v"(xr) : "v"(lane * 16), "s"(x + gtok * 1024) : "memory");
  asm volatile("global_load_dwordx4 %0, %1, %2" : "=v"(gr) : "v"(lane * 16), "s"(gw) : "memory");
  const unsigned xo0 = (lane >> 3) * 1024 + 16 * ((lane & 7) ^ lg), xo1 = (8 + (lane >> 3)) * 1024 + 16 * ((lane & 7) ^ (4 + lg));
  const char* xw = x + (size_t)(p * 256 + wave * 32) * 1024;
  auto stage_x = [&](int i, int kt, unsigned slot) {
    glds16s((i & 1) ? xo1 : xo0, xw + kt * 128 + (i >> 1) * 16384, lds0 + slot + wave * 4096 + i * 1024);
  };
#pragma unroll
  for (int i = 0; i < 4; ++i) stage_x(i, rot, 0);
  const int fo = 896 * (e >> 1);
  const unsigned sAB = ty == 0 ? 3072 : 3584, sC = ty < 2 ? 3072 : 3584;
  const char* bA = (ty == 0 ? wqkv + 1024 + 128 * e : w1 + fo + 384 * (e & 1) + 128 * (ty - 1)) + wave * 8 * sAB;
  const char* bB = (ty == 0 ? wqkv + 2048 + 128 * e : w3 + fo + 384 * (e & 1) + 128 * (ty - 1)) + wave * 8 * sAB;
  const unsigned voAB = (lane >> 3) * sAB + ((((lane >> 1) & 3) ^ ((lg & 1) | ((wave & 1) << 1))) * 32) + (lane & 1) * 16;
  auto stage_ab = [&](int i, int kt, unsigned slot) {
    glds16s(voAB, (i ? bB : bA) + (unsigned)(kt * 64) * sAB, lds0 + slot + XB + wave * 2560 + i * 1024);
  };
  stage_ab(0, rot, 0); stage_ab(1, rot, 0);
  const char* bC0 = ty < 2 ? wqkv + 128 * e + 64 * ty : w1 + fo + 768 + 64 * (e & 1) + 32 * (ty - 2);
  const char* bC1 = ty < 2 ? bC0 + 16 : w3 + fo + 768 + 64 * (e & 1) + 32 * (ty - 2);
  const char* pC = ((lane & 1) ? bC1 : bC0) + (unsigned)((wave * 8 + ((lane >> 2) & 7)) * sC + ((((lane >> 1) ^ wave) & 1) << (ty < 2 ? 5 : 4)));
  auto stage_w = [&](int i, int kt, unsigned slot) {
    if (i < 2) stage_ab(i, kt, slot);
    else glds16_lo(pC + (unsigned)(kt * 64) * sC, lds0 + slot + XB + wave * 2560 + 2048);
  };
  stage_w(2, rot, 0);
#pragma unroll
  for (int i = 0; i < 4; ++i) stage_x(i, (rot + 1) & 7, SLOT);
#pragma unroll
  for (int i = 0; i < 3; ++i) stage_w(i, (rot + 1) & 7, SLOT);
  ST(25);
  const int tb2 = xg * 4 + (idx & 3), nb = idx >> 2, e2 = wave;
  const int jj = lane >> 3;
  const unsigned wbase2 = lds0 + wave * PIECE;
  unsigned w2o[2][2];
#pragma unroll
  for (int c = 0; c < 2; ++c) {
    unsigned col = ((((lane & 7) >> 1) ^ (((jj >> 1) & 1) | (c << 1))) * 32) + (lane & 1) * 16;
    w2o[0][c] = col + (16 * (jj >> 2) + (jj & 3)) * 1024;
    w2o[1][c] = col + (8 * (jj >> 1) + (jj & 1)) * 1024;
  }
  const char* w2f = w2 + (size_t)(448 * (e2 >> 1) + 192 * (e2 & 1)) * 1024 + nb * 128;
  const char* w2l = w2 + (size_t)(448 * (e2 >> 1) + 384 + 32 * (e2 & 1)) * 1024 + nb * 128;
  const char* wob = wo + (size_t)(64 * e2) * 1024 + nb * 128;
  auto stage2 = [&](int n) {
    const int u = n >> 2, c = n & 3;
    const unsigned l = wbase2 + chunk_off(n % 18);
    if (u >= 7) glds16s(w2o[0][c & 1], wob + (32 * (u - 7) + 4 * c) * 1024, l);
    else if (u < 6) glds16s(w2o[0][c & 1], w2f + (32 * u + 4 * c) * 1024, l);
    else glds16s(w2o[1][c & 1], w2l + (4 * (c & 1) + 2 * (c >> 1)) * 1024, l);
  };
  const int tb1 = p * 4 + wm;
  FragAddr fa;
  fa.xo = (wm * 64 + lc) * 128 + 16 * (lg ^ (lc >> 1));
  fa.w0 = XB + lg * 2560 + (lc >> 2) * 128 + (((2 * wn) ^ ((lc >> 3) | ((lg & 1) << 1))) * 32) + 8 * (lane & 3);
  fa.w4 = XB + lg * 2560 + 2048 + (lc >> 2) * 64 + ((wn ^ (lg & 1)) * 32) + 8 * (lane & 3);
  asm volatile("s_waitcnt vmcnt(14)" : "+v"(xr), "+v"(gr), "+v"(nn)::"memory");
  ST(26);
  const u64 tag = (((u64)(unsigned)__builtin_amdgcn_readfirstlane(nn[1]) << 32) | (unsigned)__builtin_amdgcn_readfirstlane(nn[0])) + 0x9E3779B97F4A7C15ull;
  const unsigned tag32 = (unsigned)tag;
  {
    float d = 0.f;
#pragma unroll
    for (int i = 0; i < 4; ++i) d += bflo(xr[i]) * bflo(gr[i]) + bfhi(xr[i]) * bfhi(gr[i]);
    d = dpp_add<0x111, 0xf>(d); d = dpp_add<0x112, 0xf>(d); d = dpp_add<0x114, 0xf>(d); d = dpp_add<0x118, 0xf>(d);
    d = dpp_add<0x142, 0xa>(d); d = dpp_add<0x143, 0xc>(d);
    if (lane == 63) __hip_atomic_store((GAS u64*)(ws + OFF_G) + gtok, ((u64)tag32 << 32) | f2u(d), RLX_AGENT);
  }
  SCHED();
  WAIT_VM(8);
  ST(27);
  BAR();
  ST(1);

  f32x4 acc[4][5];
#pragma unroll
  for (int tt = 0; tt < 4; ++tt)
#pragma unroll
    for (int G = 0; G < 5; ++G) acc[tt][G] = (f32x4){0.f, 0.f, 0.f, 0.f};
  v2u gg = {0u, 0u};
  auto load_gg = [&]() { ld8_sc1(gg, lane * 8, ws + OFF_G + tb1 * 512); };
  auto gemm1 = [&](auto ktc) {
    constexpr bool KT = decltype(ktc)::value;
    Frags f0, f1;
#pragma unroll
    for (int i = 0; i < 14; ++i) frag_read(f0, i, smem, fa, 0);
    SCHED();
    half_step<KT, true, PR(3)>(acc, f0, f1, smem, fa, 1, [&](int a) {
      if (a < 4) stage_x(a, (rot + 2) & 7, 2 * SLOT); else stage_w(a - 4, (rot + 2) & 7, 2 * SLOT); });
    asm volatile("s_waitcnt vmcnt(7) lgkmcnt(0)" ::: "memory"); BAR(); SCHED(); ST(14);
    half_step<KT, true, PR(5)>(acc, f1, f0, smem + SLOT, fa, 0, [&](int a) { if (a < 4) stage_x(a, (rot + 3) & 7, 0); });
    unsigned sp = 0, so = SLOT, sn = 2 * SLOT;
#pragma unroll 1
    for (int t = 1; t < 5; ++t) {
      half_step<KT, true, PR(6)>(acc, f0, f1, smem + so, fa, 1, [&](int a) { if (a < 3) stage_w(a, (rot + t + 2) & 7, sp); });
      asm volatile("s_waitcnt vmcnt(7) lgkmcnt(0)" ::: "memory"); BAR(); SCHED(); ST(14 + t); CY(t);
      half_step<KT, true, PR(5)>(acc, f1, f0, smem + sn, fa, 0, [&](int a) { if (a < 4) stage_x(a, (rot + t + 3) & 7, so); });
      const unsigned tmp = sp; sp = so; so = sn; sn = tmp;
    }
    half_step<KT, true, PR(6)>(acc, f0, f1, smem + 2 * SLOT, fa, 1, [&](int a) { if (a < 3) stage_w(a, (rot + 7) & 7, SLOT); });
    asm volatile("s_waitcnt vmcnt(7) lgkmcnt(0)" ::: "memory"); BAR(); SCHED(); ST(19);
    half_step<KT, true, PR(6)>(acc, f1, f0, smem, fa, 0, [&](int a) { if (a < 3) stage2(a); });
    half_step<KT, true, PR(5)>(acc, f0, f1, smem, fa, 1, [&](int a) {
      if (a < 3) stage2(3 + a);
      if (!KT && a == 3) load_gg(); });
    if (KT) asm volatile("s_waitcnt vmcnt(6) lgkmcnt(0)" ::: "memory"); else asm volatile("s_waitcnt vmcnt(7) lgkmcnt(0)" ::: "memory");
    BAR(); SCHED(); ST(20);
    half_step<KT, true, PR(6)>(acc, f1, f0, smem + SLOT, fa, 0, [&](int a) { if (a < 3) stage2(6 + a); });
    half_step<KT, true, PR(6)>(acc, f0, f1, smem + SLOT, fa, 1, [&](int a) { if (a < 3) stage2(9 + a); });
    WAIT_LGKM0(); BAR(); SCHED(); ST(21);
    half_step<KT, false, PR(3)>(acc, f1, f0, smem, fa, 0, [&](int a) { if (!KT && a < 6) stage2(12 + a); });
  };
  LAS char* wreg = smem + wave * PIECE;
  v4u W[5][4];
  int tro[4];
#pragma unroll
  for (int nt = 0; nt < 4; ++nt) tro[nt] = lg * 1024 + (lc >> 2) * 128 + ((nt ^ (((lc >> 3) & 1) | ((lg & 1) << 1))) * 32) + 8 * (lane & 3);
  auto read_unit = [&](int u, v4u (&d)[4]) {
    const int k0 = (4 * u) % 18, k2 = (4 * u + 2) % 18;
    const int delta = chunk_off(k2) - chunk_off(k0) - 2048;
    const int dl = delta ? (lg >= 2 ? delta : 0) : 0;
#pragma unroll
    for (int nt = 0; nt < 4; ++nt) {
      LAS char* q = wreg + chunk_off(k0) + tro[nt] + dl;
      d[nt] = cat(tr_read(q), tr_read(q + 512));
    }
  };
  const int hbase = OFF_H + (tb1 * 8 + e) * 36864 + lane * 16;
  if (ty == 0) {
    gemm1(std::true_type{});
    ST(2);
    const v4u ones = {0x3f803f80u, 0x3f803f80u, 0x3f803f80u, 0x3f803f80u};
#pragma unroll
    for (int i2 = 0; i2 < 2; ++i2) {
      f32x4 kv = {0.f, 0.f, 0.f, 0.f}, z = {0.f, 0.f, 0.f, 0.f};
#pragma unroll
      for (int u = 0; u < 2; ++u) {
        f32x4 v0 = acc[2 * u][2 * i2 + 1], v1 = acc[2 * u + 1][2 * i2 + 1];
        v4u pa = phi8(acc[2 * u][2 * i2], acc[2 * u + 1][2 * i2]);
        v4u vb = {pk2(v0[0], v0[1]), pk2(v0[2], v0[3]), pk2(v1[0], v1[1]), pk2(v1[2], v1[3])};
        kv = mma32(pa, vb, kv);
        z = mma32(pa, ones, z);
      }
      *(LAS f32x4*)(wreg + SLOT + i2 * 2048 + lane * 16) = kv;
      *(LAS f32x4*)(wreg + SLOT + i2 * 2048 + 1024 + lane * 16) = z;
    }
    WAIT_LGKM0(); BAR(); SCHED();
    if (wave < 4) {
      f32x4 kv = {0.f, 0.f, 0.f, 0.f}, z = {0.f, 0.f, 0.f, 0.f};
#pragma unroll
      for (int m = 0; m < 4; ++m) {
        LAS char* src = smem + SLOT + ((wave >> 1) * 4 + m) * PIECE + (wave & 1) * 2048 + lane * 16;
        kv += *(LAS f32x4*)src; z += *(LAS f32x4*)(src + 1024);
      }
      int o = OFF_KVP + ((4 * e + wave) * 8 + p) * KVP_STR;
      v4u a = {f2u(kv[0]), tag32, f2u(kv[1]), tag32};
      v4u b = {f2u(kv[2]), tag32, f2u(kv[3]), tag32};
      __builtin_amdgcn_raw_buffer_store_b128(a, wsr, o + lane * 16, 0, SC1);
      __builtin_amdgcn_raw_buffer_store_b128(b, wsr, o + 1024 + lane * 16, 0, SC1);
      if (lc < 2) {
        v4u c = {f2u(lc ? z[2] : z[0]), tag32, f2u(lc ? z[3] : z[1]), tag32};
        __builtin_amdgcn_raw_buffer_store_b128(c, wsr, o + 2048 + lg * 32 + lc * 16, 0, SC1);
      }
    }
#pragma unroll
    for (int u = 0; u < 2; ++u)
      __builtin_amdgcn_raw_buffer_store_b128(phi8(acc[2 * u][4], acc[2 * u + 1][4]), wsr, hbase + (28 + wn * 2 + u) * 1024, 0, SC1);
    WAIT_LGKM0(); BAR(); SCHED();
    ST(3);
#pragma unroll
    for (int n = 12; n < 18; ++n) stage2(n);
    WAIT_VM(6);
    if (lane == 0) __hip_atomic_store((GAS u64*)(ws + OFF_HF + (tb1 * 8 + e) * 128) + wn, tag, RLX_AGENT);
    ST(4);
    read_unit(0, W[0]); read_unit(1, W[1]); read_unit(2, W[2]);
    WAIT_LGKM0(); SCHED();
#pragma unroll
    for (int n = 18; n < 30; ++n) stage2(n);
    if (!(p & 1) && wave == 5) {
      const int h = 4 * e + (p >> 1);
      const int o = OFF_KVP + h * 8 * KVP_STR;
      f32x4 kv; f32x2 zs;
      for (unsigned spins = 0;; ++spins) {
        v4u a[8], b[8], zz[8];
#pragma unroll
        for (int q = 0; q < 8; ++q) {
          a[q] = __builtin_amdgcn_raw_buffer_load_b128(wsr, o + q * KVP_STR + lane * 16, 0, SC1);
          b[q] = __builtin_amdgcn_raw_buffer_load_b128(wsr, o + q * KVP_STR + 1024 + lane * 16, 0, SC1);
          zz[q] = __builtin_amdgcn_raw_buffer_load_b128(wsr, o + q * KVP_STR + 2048 + lg * 32 + (lc & 2) * 8, 0, SC1);
        }
        SCHED();
        bool ok = true; kv = (f32x4){0.f, 0.f, 0.f, 0.f}; zs = (f32x2){0.f, 0.f};
#pragma unroll
        for (int q = 0; q < 8; ++q) {
          ok &= (a[q][1] == tag32) & (a[q][3] == tag32) & (b[q][1] == tag32) & (b[q][3] == tag32) & (zz[q][1] == tag32) & (zz[q][3] == tag32);
          kv += (f32x4){u2f(a[q][0]), u2f(a[q][2]), u2f(b[q][0]), u2f(b[q][2])};
          zs += (f32x2){u2f(zz[q][0]), u2f(zz[q][2])};
        }
        if (__all(ok) || spins > SPIN_CAP) break;
        __builtin_amdgcn_s_sleep(1);
      }
      v4u f = {pk2(kv[0], kv[1]), pk2(kv[2], kv[3]), pk2(zs[0], zs[1]), tag32};
      __builtin_amdgcn_raw_buffer_store_b128(f, wsr, OFF_KVF + h * 1024 + lane * 16, 0, SC1);
    }
    WAIT_VM(10);
    read_unit(3, W[3]); read_unit(4, W[4]);
    WAIT_LGKM0(); SCHED();
#pragma unroll
    for (int n = 30; n < 36; ++n) stage2(n);
  } else {
    gemm1(std::false_type{});
    ST(2);
    asm volatile("s_waitcnt vmcnt(12)" : "+v"(gg)::"memory");
    if (!__all(gg[1] == tag32)) {
      for (unsigned spins = 0;; ++spins) {
        u64 v = __hip_atomic_load((const GAS u64*)(ws + OFF_G) + tb1 * 64 + lane, RLX_AGENT);
        gg = (v2u){(unsigned)v, (unsigned)(v >> 32)};
        if (__all(gg[1] == tag32) || spins > SPIN_CAP) break;
        __builtin_amdgcn_s_sleep(1);
      }
    }
    const unsigned iol = f2u(__builtin_fminf(1.f + fexp(u2f(gg[0])), 1e30f));
    float io[4];
#pragma unroll
    for (int tt = 0; tt < 4; ++tt) io[tt] = u2f(__builtin_amdgcn_ds_bpermute((tt * 16 + lc) * 4, iol));
    const int slot = 2 * (ty - 1) + wn;
#pragma unroll
    for (int tt = 0; tt < 4; ++tt) {
      v4u hv = {swiglu2(lo2(acc[tt][0]), lo2(acc[tt][1]), io[tt]), swiglu2(hi2(acc[tt][0]), hi2(acc[tt][1]), io[tt]),
                swiglu2(lo2(acc[tt][2]), lo2(acc[tt][3]), io[tt]), swiglu2(hi2(acc[tt][2]), hi2(acc[tt][3]), io[tt])};
      __builtin_amdgcn_raw_buffer_store_b128(hv, wsr, hbase + (slot * 4 + tt) * 1024, 0, SC1);
    }
    if (ty >= 2) {
      v4u hv = {ffn_mixed(acc[0][4], io[0]), ffn_mixed(acc[1][4], io[1]), ffn_mixed(acc[2][4], io[2]), ffn_mixed(acc[3][4], io[3])};
      __builtin_amdgcn_raw_buffer_store_b128(hv, wsr, hbase + (24 + (ty - 2) * 2 + wn) * 1024, 0, SC1);
    } else {
#pragma unroll
      for (int u = 0; u < 2; ++u)
        __builtin_amdgcn_raw_buffer_store_b128(phi8(acc[2 * u][4], acc[2 * u + 1][4]), wsr, hbase + (28 + (2 + wn) * 2 + u) * 1024, 0, SC1);
    }
    SCHED();
    ST(3);
    WAIT_VM(5);
    read_unit(0, W[0]); read_unit(1, W[1]); read_unit(2, W[2]); read_unit(3, W[3]);
    asm volatile("s_waitcnt vmcnt(0) lgkmcnt(0)" ::: "memory");
    if (lane == 0) __hip_atomic_store((GAS u64*)(ws + OFF_HF + (tb1 * 8 + e) * 128) + ty * 2 + wn, tag, RLX_AGENT);
    ST(4);
    SCHED();
#pragma unroll
    for (int n = 18; n < 34; ++n) stage2(n);
    if (bid == 8 * 4 && wave == 0) {
      for (unsigned spins = 0;; ++spins) {
        bool ok = true;
        v4u a[16];
#pragma unroll
        for (int q = 0; q < 16; ++q) a[q] = __builtin_amdgcn_raw_buffer_load_b128(wsr, OFF_G + q * 1024 + lane * 16, 0, SC1);
        SCHED();
#pragma unroll
        for (int q = 0; q < 16; ++q) ok &= (a[q][1] == tag32) & (a[q][3] == tag32);
        if (__all(ok) || spins > SPIN_CAP) break;
        __builtin_amdgcn_s_sleep(1);
      }
      if (lane == 0) __hip_atomic_store((GAS u64*)ws, tag, RLX_AGENT);
    }
    WAIT_VM(14);
    read_unit(4, W[4]);
    WAIT_LGKM0(); SCHED();
    stage2(34); stage2(35);
  }
  ST(5);
  const GAS u64* fl = (const GAS u64*)(ws + OFF_HF + (tb2 * 8 + e2) * 128) + (lane & 7);
  for (unsigned spins = 0;; ++spins) {
    u64 v = __hip_atomic_load(fl, RLX_AGENT);
    if (__all(v == tag || (lane & 6) == 0) || spins > SPIN_CAP) break;
    __builtin_amdgcn_s_sleep(1);
  }
  WAIT_VM(0);
  ST(6);
  f32x4 c2[4][4];
#pragma unroll
  for (int tt = 0; tt < 4; ++tt)
#pragma unroll
    for (int nt = 0; nt < 4; ++nt) c2[tt][nt] = (f32x4){0.f, 0.f, 0.f, 0.f};
  const int hb2 = OFF_H + (tb2 * 8 + e2) * 36864 + lane * 16;
  v4u hq[9][4];
  auto ldq = [&](int qd) {
#pragma unroll
    for (int i = 0; i < 4; ++i) hq[qd][i] = __builtin_amdgcn_raw_buffer_load_b128(wsr, hb2 + (qd * 4 + i) * 1024, 0, SC1);
  };
  auto mmq = [&](const v4u (&w)[4], const v4u (&hf)[4]) {
#pragma unroll
    for (int tt = 0; tt < 4; ++tt)
#pragma unroll
      for (int nt = 0; nt < 4; ++nt) c2[tt][nt] = mma32(w[nt], hf[tt], c2[tt][nt]);
  };
  v4u kvf[4]; v2u g2;
  auto ldkv = [&]() {
#pragma unroll
    for (int i = 0; i < 4; ++i) kvf[i] = __builtin_amdgcn_raw_buffer_load_b128(wsr, OFF_KVF + (4 * e2 + i) * 1024 + lane * 16, 0, SC1);
    g2 = __builtin_amdgcn_raw_buffer_load_b64(wsr, OFF_G + (tb2 * 64 + lane) * 8, 0, SC1);
  };
  auto kvok = [&]() { return __all((kvf[0][3] == tag32) & (kvf[1][3] == tag32) & (kvf[2][3] == tag32) & (kvf[3][3] == tag32) & (g2[1] == tag32)); };
  ldq(0); ldq(1); ldq(2); ldq(3); SCHED();
  mmq(W[0], hq[0]); SCHED(); ldq(4); ldq(8); SCHED();
  mmq(W[1], hq[1]); SCHED();
  u64 kf = __hip_atomic_load(fl, RLX_AGENT);
  ldq(5); ldq(6); SCHED();
  mmq(W[2], hq[2]); SCHED();
  read_unit(5, W[0]); SCHED();
  mmq(W[3], hq[3]); SCHED();
  for (unsigned spins = 0; !__all(kf == tag) && spins <= SPIN_CAP; ++spins) {
    __builtin_amdgcn_s_sleep(1);
    kf = __hip_atomic_load(fl, RLX_AGENT);
  }
  ldq(7); ldkv(); SCHED();
  ST(7);
  read_unit(6, W[1]); SCHED();
  mmq(W[4], hq[4]); SCHED();
  read_unit(8, W[2]); SCHED();
  mmq(W[0], hq[5]); SCHED();
  read_unit(7, W[3]); SCHED();
  {
    v4u hf[4];
#pragma unroll
    for (int tt = 0; tt < 4; ++tt) hf[tt] = (v4u){hq[6][0][tt], hq[6][1][tt], hq[6][2][tt], hq[6][3][tt]};
    mmq(W[1], hf);
  }
  ST(8);
  const bool early = kvok();
  if (!early) {
    for (unsigned spins = 0;; ++spins) {
      ldkv();
      if (kvok() || spins > SPIN_CAP) break;
      __builtin_amdgcn_s_sleep(1);
    }
  }
  ST(9);
  {
    const unsigned igl = f2u(1.f + fexp(-u2f(g2[0])));
    f32x2 ig[2];
#pragma unroll
    for (int tt = 0; tt < 4; ++tt) ig[tt >> 1][tt & 1] = u2f(__builtin_amdgcn_ds_bpermute((tt * 16 + lc) * 4, igl));
    const f32x4 zero = {0.f, 0.f, 0.f, 0.f}, eps = {1e-6f, 1e-6f, 0.f, 0.f};
#pragma unroll
    for (int so = 1; so >= 0; --so) {
      v4u of[4];
#pragma unroll
      for (int hh = 0; hh < 2; ++hh) {
        const int i = 2 * so + hh;
        const unsigned zv = kvf[i][2];
        const v4u zf = {(lc & 3) == 0 ? zv : 0u, (lc & 3) == 2 ? zv : 0u, (lc & 3) == 1 ? zv : 0u, (lc & 3) == 3 ? zv : 0u};
        const v2u kv = {kvf[i][0], kvf[i][1]};
#pragma unroll
        for (int u = 0; u < 2; ++u) {
          const v4u qq = hq[7 + so][hh * 2 + u];
          const f32x4 den = mma32(zf, qq, eps);
          const f32x4 n0 = mma16(kv, (v2u){qq[0], qq[1]}, zero), n1 = mma16(kv, (v2u){qq[2], qq[3]}, zero);
          const f32x2 dd = (lo2(den) + hi2(den)) * ig[u];
          const float r0 = frcp(dd[0]), r1 = frcp(dd[1]);
          of[2 * u][hh * 2] = __builtin_bit_cast(unsigned, __builtin_convertvector(lo2(n0) * r0, bf16x2));
          of[2 * u][hh * 2 + 1] = __builtin_bit_cast(unsigned, __builtin_convertvector(hi2(n0) * r0, bf16x2));
          of[2 * u + 1][hh * 2] = __builtin_bit_cast(unsigned, __builtin_convertvector(lo2(n1) * r1, bf16x2));
          of[2 * u + 1][hh * 2 + 1] = __builtin_bit_cast(unsigned, __builtin_convertvector(hi2(n1) * r1, bf16x2));
        }
      }
      mmq(W[3 - so], of);
    }
  }
  ST(10);
#pragma unroll
  for (int tt = 0; tt < 4; ++tt)
#pragma unroll
    for (int nt = 0; nt < 4; ++nt)
      *(LAS f32x4*)(wreg + chunk_off(tt * 4 + nt) + lg * 256 + ((lc ^ (4 * nt + 2 * (lg >> 1))) & 15) * 16) = c2[tt][nt];
  WAIT_LGKM0(); BAR(); SCHED();
  ST(11);
  {
    const int o = lane & 7, c = (wave & 1) * 8 + (lane >> 3), tt = wave >> 1;
    const int k = tt * 4 + (o >> 1);
    const int ro = (k < 6 ? 2 * SLOT + k * 1024 : (k < 12 ? (k - 6) * 1024 : SLOT + (k - 12) * 1024)) + (o & 1) * 512 + ((c ^ (2 * o)) & 15) * 16;
    f32x4 a = {0.f, 0.f, 0.f, 0.f}, b = {0.f, 0.f, 0.f, 0.f};
#pragma unroll
    for (int w = 0; w < 8; ++w) { a += *(LAS f32x4*)(smem + w * PIECE + ro); b += *(LAS f32x4*)(smem + w * PIECE + ro + 256); }
    v4u r = {pk2(a[0], a[1]), pk2(a[2], a[3]), pk2(b[0], b[1]), pk2(b[2], b[3])};
    *(GAS v4u*)(out + (size_t)(tb2 * 64 + tt * 16 + c) * 1024 + nb * 128 + o * 16) = r;
  }
}
}

extern "C" void kernel_launch(void* const* d_in, const int* in_sizes, int n_in, void* d_out, int out_size, void* d_ws, size_t ws_size,
                              hipStream_t stream) {
  fk::fused<<<256, 512, 0, stream>>>((const char*)d_in[0], (const char*)d_in[1], (const char*)d_in[2], (const char*)d_in[4],
                                     (const char*)d_in[5], (const char*)d_in[6], (const char*)d_in[7], (char*)d_out, (char*)d_ws);
}
```

```cpp
#include <hip/hip_runtime.h>
#include <hip/hip_bf16.h>
#include <stdint.h>
#include <type_traits>

#define GAS __attribute__((address_space(1)))
#define LAS __attribute__((address_space(3)))
typedef unsigned v4u __attribute__((ext_vector_type(4)));
typedef unsigned v2u __attribute__((ext_vector_type(2)));
typedef float f32x4 __attribute__((ext_vector_type(4)));
typedef float f32x2 __attribute__((ext_vector_type(2)));
typedef short s16x4 __attribute__((ext_vector_type(4)));
typedef __bf16 bf16x8 __attribute__((ext_vector_type(8)));
typedef __bf16 bf16x2 __attribute__((ext_vector_type(2)));
typedef unsigned long long u64;
#define RLX_AGENT __ATOMIC_RELAXED, __HIP_MEMORY_SCOPE_AGENT

namespace fk {
constexpr int XB = 32768, WB = 20480, SLOT = XB + WB;
constexpr int LDS_BYTES = 3 * SLOT + 1024;
constexpr int PIECE = SLOT / 8;
constexpr int OFF_G = 4096;
constexpr int OFF_HF = 32768;
constexpr int OFF_KVF = 69632;
constexpr int OFF_KVP = 131072;
constexpr int KVP_STR = 2304;
constexpr int OFF_H = 1 << 20;
constexpr int WS_USED = OFF_H + 256 * 36864;
constexpr int SC1 = 16;
constexpr unsigned SPIN_CAP = 1u << 15;

#define ST(k) do {} while (0)
#define CY(t) do {} while (0)

__device__ __forceinline__ unsigned f2u(float f) { return __builtin_bit_cast(unsigned, f); }
__device__ __forceinline__ float u2f(unsigned u) { return __builtin_bit_cast(float, u); }
__device__ __forceinline__ unsigned pk2(float a, float b) {
  return __builtin_bit_cast(unsigned, __builtin_convertvector((f32x2){a, b}, bf16x2));
}
__device__ __forceinline__ f32x4 mma32(v4u a, v4u b, f32x4 c) {
  return __builtin_amdgcn_mfma_f32_16x16x32_bf16(__builtin_bit_cast(bf16x8, a), __builtin_bit_cast(bf16x8, b), c, 0, 0, 0);
}
__device__ __forceinline__ f32x4 mma16(v2u a, v2u b, f32x4 c) {
  return __builtin_amdgcn_mfma_f32_16x16x16bf16_1k(__builtin_bit_cast(s16x4, a), __builtin_bit_cast(s16x4, b), c, 0, 0, 0);
}
__device__ __forceinline__ v2u tr_read(LAS char* p) {
  return __builtin_bit_cast(v2u, __builtin_amdgcn_ds_read_tr16_b64_v4i16((LAS s16x4*)p));
}
__device__ __forceinline__ v4u cat(v2u a, v2u b) { return (v4u){a[0], a[1], b[0], b[1]}; }
__device__ __forceinline__ void glds16(const void* g, unsigned l) {
  asm volatile("s_mov_b32 m0, %1\n\ts_nop 0\n\tglobal_load_lds_dwordx4 %0, off" ::"v"(g), "s"(l) : "memory");
}
__device__ __forceinline__ void glds16s(unsigned voff, const void* sbase, unsigned l) {
  asm volatile("s_mov_b32 m0, %2\n\ts_nop 0\n\tglobal_load_lds_dwordx4 %0, %1" ::"v"(voff), "s"(sbase), "s"(l) : "memory");
}
__device__ __forceinline__ void glds16_lo(const void* g, unsigned l) {
  asm volatile("s_mov_b32 m0, %1\n\ts_mov_b32 exec_hi, 0\n\tglobal_load_lds_dwordx4 %0, off\n\ts_mov_b32 exec_hi, -1" ::"v"(g), "s"(l) : "memory");
}
__device__ __forceinline__ void ld8_sc1(v2u& d, unsigned voff, const void* sbase) {
  asm volatile("global_load_dwordx2 %0, %1, %2 sc1" : "=v"(d) : "v"(voff), "s"(sbase) : "memory");
}
__device__ __forceinline__ void touch4(unsigned& d, const void* g) {
  asm volatile("global_load_dword %0, %1, off" : "+v"(d) : "v"(g) : "memory");
}
__device__ __forceinline__ void glds4s(unsigned voff, const void* sbase, unsigned l) {
  asm volatile("s_mov_b32 m0, %2\n\ts_nop 0\n\tglobal_load_lds_dword %0, %1" ::"v"(voff), "s"(sbase), "s"(l) : "memory");
}
__device__ __forceinline__ void keep(unsigned& d) { asm volatile("" : "+v"(d)); }
__device__ __forceinline__ void st8_last(v2u d, const void* sbase) {
  asm volatile("s_mov_b32 exec_lo, 0\n\ts_brev_b32 exec_hi, 1\n\tglobal_store_dwordx2 %0, %1, %2 sc1\n\ts_mov_b64 exec, -1" ::"v"(0), "v"(d), "s"(sbase) : "memory");
}
__device__ __forceinline__ void ld16(v4u& d, unsigned voff, const void* sbase) {
  asm volatile("global_load_dwordx4 %0, %1, %2" : "=v"(d) : "v"(voff), "s"(sbase) : "memory");
}
__device__ __forceinline__ float fexp(float x) { return __builtin_amdgcn_exp2f(x * 1.44269504088896f); }
__device__ __forceinline__ float frcp(float x) { return __builtin_amdgcn_rcpf(x); }
__device__ __forceinline__ float bflo(unsigned u) { return u2f(u << 16); }
__device__ __forceinline__ float bfhi(unsigned u) { return u2f(u & 0xffff0000u); }
#define WAIT_VM(n) asm volatile("s_waitcnt vmcnt(" #n ")" ::: "memory")
#define WAIT_LGKM0() asm volatile("s_waitcnt lgkmcnt(0)" ::: "memory")
#define BAR() __builtin_amdgcn_s_barrier()
#define SCHED() __builtin_amdgcn_sched_barrier(0)
#define PRIO(n) __builtin_amdgcn_s_setprio(n)
#define PYOUNG 2
#define POLD 3
#define EARLY_OLD 1
#define P2Y 2
#define P2O 1
#define REDW 2

struct Frags { v4u x[4]; v2u wl[5], wh[5]; };
struct FragAddr { int xo, w0, w4; };
__device__ __forceinline__ void frag_read(Frags& f, int i, LAS char* sp, const FragAddr& a, int ks) {
  if (i >= 2 && i < 6) { f.x[i - 2] = *(LAS v4u*)(sp + (a.xo ^ (ks * 64)) + (i - 2) * 2048); return; }
  const int r = i < 2 ? i : i - 4, G = r >> 1;
  LAS char* p = G < 4 ? sp + (a.w0 ^ ((G >> 1) * 32)) + (G & 1) * 1024 + ks * 10240 + (r & 1) * 512 : sp + a.w4 + ks * 10240 + (r & 1) * 256;
  if (r & 1) f.wh[G] = tr_read(p); else f.wl[G] = tr_read(p);
}
struct NoFill { __device__ __forceinline__ void operator()(int) const {} };
template <bool KT, bool RD, int PER, class Act, class Fill = NoFill>
__device__ __forceinline__ void half_step(f32x4 (&acc)[4][5], const Frags& c, Frags& n, LAS char* sp, const FragAddr& a, int ks, Act act, Fill fill = Fill()) {
#pragma unroll
  for (int i = 0; i < 20; ++i) {
    const int G = i >> 2, tt = i & 3;
    const v4u w = cat(c.wl[G], c.wh[G]);
    acc[tt][G] = (KT && G < 4) ? mma32(c.x[tt], w, acc[tt][G]) : mma32(w, c.x[tt], acc[tt][G]);
    if (RD && i < 14) frag_read(n, i, sp, a, ks);
    if (i % PER == 1) act(i / PER);
    fill(i);
    SCHED();
  }
}
__device__ __forceinline__ unsigned swiglu2(f32x2 g, f32x2 u, float io) {
  const f32x2 t = g * -1.44269504088896f;
  const f32x2 e = {__builtin_amdgcn_exp2f(t[0]), __builtin_amdgcn_exp2f(t[1])};
  const f32x2 i2 = {io, io};
  const f32x2 d = __builtin_elementwise_fma(e, i2, i2);
  const f32x2 r = {frcp(d[0]), frcp(d[1])};
  return __builtin_bit_cast(unsigned, __builtin_convertvector((g * u) * r, bf16x2));
}
__device__ __forceinline__ f32x2 lo2(f32x4 a) { return (f32x2){a[0], a[1]}; }
__device__ __forceinline__ f32x2 hi2(f32x4 a) { return (f32x2){a[2], a[3]}; }
__device__ __forceinline__ unsigned ffn_mixed(f32x4 a, float io) {
  auto s0 = __builtin_amdgcn_permlane32_swap(f2u(a[0]), f2u(a[2]), false, false);
  auto s1 = __builtin_amdgcn_permlane32_swap(f2u(a[1]), f2u(a[3]), false, false);
  return swiglu2((f32x2){u2f(s0[0]), u2f(s1[0])}, (f32x2){u2f(s0[1]), u2f(s1[1])}, io);
}
__device__ __forceinline__ unsigned phi2(f32x2 t) {
  const f32x2 s = t * 1.44269504088896f;
  const f32x2 e = {__builtin_amdgcn_fmed3f(__builtin_amdgcn_exp2f(s[0]), 0.f, 1.f), __builtin_amdgcn_fmed3f(__builtin_amdgcn_exp2f(s[1]), 0.f, 1.f)};
  const f32x2 m = {__builtin_fmaxf(t[0], 0.f), __builtin_fmaxf(t[1], 0.f)};
  return __builtin_bit_cast(unsigned, __builtin_convertvector((m + e) + 1e-6f, bf16x2));
}
__device__ __forceinline__ v4u phi8(f32x4 a, f32x4 b) { return (v4u){phi2(lo2(a)), phi2(hi2(a)), phi2(lo2(b)), phi2(hi2(b))}; }
template <int CTRL, int RM> __device__ __forceinline__ float dpp_add(float v) {
  return v + u2f(__builtin_amdgcn_update_dpp(0u, f2u(v), CTRL, RM, 0xf, false));
}
__device__ __forceinline__ constexpr int chunk_off(int k) { return (k < 6 ? 2 * SLOT : (k < 12 ? 0 : SLOT)) + (k % 6) * 1024; }

#define SPREAD 1
#define PR(n) (SPREAD ? (n) : 3)
#define ROTMODE 0
typedef unsigned v16u __attribute__((ext_vector_type(16)));
__device__ __forceinline__ char* mkptr(unsigned lo, unsigned hi) { u64 a = ((u64)hi << 32) | lo; asm volatile("" : "+s"(a)); return (char*)a; }
__global__ __launch_bounds__(512, 2) void fused(const char* x_, const char* wqkv_, const char* wo_, const char* w1_, const char* w3_,
                                                const char* w2_, const char* gw_, char* out_, char* ws_) {
  v16u ka; v2u kb;
  asm volatile("s_load_dwordx16 %0, %2, 0x0\n\ts_load_dwordx2 %1, %2, 0x40" : "=&s"(ka), "=&s"(kb) : "s"(__builtin_amdgcn_kernarg_segment_ptr()));
  __shared__ __attribute__((aligned(16))) char smem_[LDS_BYTES];
  LAS char* smem = (LAS char*)smem_;
  const unsigned lds0 = (unsigned)(uintptr_t)smem;
  ST(0);
  int lane = threadIdx.x & 63, wave = __builtin_amdgcn_readfirstlane(threadIdx.x >> 6), bid = blockIdx.x;
  int p = (bid & 4) + ((bid >> 3) & 3);
  const int rot = ROTMODE == 0 ? 0 : ROTMODE == 1 ? (2 * bid + (bid & 4)) & 7 : ROTMODE == 2 ? ((bid >> 5) + 2 * (bid >> 3)) & 7 : (((bid >> 5) ^ (bid >> 3)) & 1) * 4;
  const unsigned xo0 = (lane >> 3) * 1024 + 16 * ((lane & 7) ^ (lane >> 4)), xo1 = (xo0 ^ 64) + 8192;
  unsigned oX = (p * 8 + wave) * 32768;
  asm volatile("s_waitcnt lgkmcnt(0)" : "+s"(ka), "+s"(kb), "+s"(oX));
  const char* x = mkptr(ka[0], ka[1]);
  ST(24);
  const char* xw = x + oX;
  auto stage_x = [&](int i, int kt, unsigned slot) {
    glds16s((i & 1) ? xo1 : xo0, xw + kt * 128 + (i >> 1) * 16384, lds0 + slot + wave * 4096 + i * 1024);
  };
#pragma unroll
  for (int i = 0; i < 4; ++i) stage_x(i, rot, 0);
  asm volatile("" : "+s"(wave), "+s"(bid));
  asm volatile("" : "+v"(lane));
  const char *wqkv = mkptr(ka[2], ka[3]), *w1 = mkptr(ka[6], ka[7]), *w3 = mkptr(ka[8], ka[9]);
  const int e = (bid & 3) * 2 + (bid >> 7), ty = (bid >> 5) & 3;
  const unsigned fo = 896 * (bid & 3);
  const unsigned sAB = ty == 0 ? 3072 : 3584, sC = ty < 2 ? 3072 : 3584;
  const unsigned oF = fo + 384 * (bid >> 7) + 128 * ty - 128, oK = 1024 + 128 * e;
  const unsigned oA = (ty == 0 ? oK : oF) + wave * 8 * sAB, oB = oA + (ty == 0 ? 1024 : 0);
  const unsigned voAB = (lane >> 3) * sAB + ((((lane >> 1) & 3) ^ (((lane >> 4) & 1) | ((wave & 1) << 1))) * 32) + (lane & 1) * 16;
  const char* bA = (ty == 0 ? wqkv : w1) + oA;
  const char* bB = (ty == 0 ? wqkv : w3) + oB;
  auto stage_ab = [&](int i, int kt, unsigned slot) {
    glds16s(voAB, (i ? bB : bA) + (unsigned)(kt * 64) * sAB, lds0 + slot + XB + wave * 2560 + i * 1024);
  };
  stage_ab(0, rot, 0); stage_ab(1, rot, 0);
  asm volatile("" : "+s"(wave));
  asm volatile("" : "+v"(lane));
  const unsigned oL = fo + 704 + 64 * (bid >> 7) + 32 * ty, oQ = 128 * e + 64 * ty;
  const char* bC0 = (ty < 2 ? wqkv : w1) + ((ty < 2 ? oQ : oL) + wave * 8 * sC);
  const char* bC1 = (ty < 2 ? wqkv : w3) + ((ty < 2 ? oQ + 16 : oL) + wave * 8 * sC);
  const char* pC = ((lane & 1) ? bC1 : bC0) + (unsigned)(((lane >> 2) & 7) * sC + ((((lane >> 1) ^ wave) & 1) << (ty < 2 ? 5 : 4)));
  auto stage_w = [&](int i, int kt, unsigned slot) {
    if (i < 2) stage_ab(i, kt, slot);
    else glds16_lo(pC + (unsigned)(kt * 64) * sC, lds0 + slot + XB + wave * 2560 + 2048);
  };
  stage_w(2, rot, 0);
  asm volatile("" : "+s"(wave), "+s"(xw), "+s"(bA), "+s"(bB));
  BAR();
#pragma unroll
  for (int i = 0; i < 4; ++i) stage_x(i, (rot + 1) & 7, SLOT);
#pragma unroll
  for (int i = 0; i < 3; ++i) stage_w(i, (rot + 1) & 7, SLOT);
  const char *gw = mkptr(ka[12], ka[13]), *wo = mkptr(ka[4], ka[5]), *w2 = mkptr(ka[10], ka[11]);
  char *ws = mkptr(kb[0], kb[1]), *out = mkptr(ka[14], ka[15]);
  asm volatile("" : "+s"(wave), "+s"(bid), "+s"(p));
  asm volatile("" : "+v"(lane));
  const int xg = bid & 7, idx = bid >> 3, lg = lane >> 4, lc = lane & 15;
  const int wm = wave & 3, wn = wave >> 2;
  const int gtok = p * 256 + (e * 4 + ty) * 8 + wave;
  const __amdgpu_buffer_rsrc_t wsr = __builtin_amdgcn_make_buffer_rsrc(ws, 0, WS_USED, 0x00020000);
  v4u xr, gr; v2u nn;
  auto load_gate = [&](int i) {
    if (i == 0) ld8_sc1(nn, 0, ws);
    if (i == 1) ld16(xr, lane * 16, x + gtok * 1024);
    if (i == 2) ld16(gr, lane * 16, gw);
  };
  const int tb2 = xg * 4 + (idx & 3), nb = idx >> 2, e2 = wave;
  const int jj = lane >> 3;
  const unsigned wbase2 = lds0 + wave * PIECE;
  unsigned w2o[2][2];
#pragma unroll
  for (int c = 0; c < 2; ++c) {
    unsigned col = ((((lane & 7) >> 1) ^ (((jj >> 1) & 1) | (c << 1))) * 32) + (lane & 1) * 16;
    w2o[0][c] = col + (16 * (jj >> 2) + (jj & 3)) * 1024;
    w2o[1][c] = col + (8 * (jj >> 1) + (jj & 1)) * 1024;
  }
  const int par = (idx ^ (idx >> 2)) & 1, sgn = 1 - 2 * par;
  const char* w2f = w2 + (size_t)(448 * (e2 >> 1) + 192 * (e2 & 1) + 128 * par) * 1024 + nb * 128;
  const char* w2l = w2 + (size_t)(448 * (e2 >> 1) + 384 + 32 * (e2 & 1)) * 1024 + nb * 128;
  const char* wob = wo + (size_t)(64 * e2) * 1024 + nb * 128;
  auto stage2 = [&](int n) {
    const int u = n >> 2, c = n & 3;
    const unsigned l = wbase2 + chunk_off(n % 18);
    if (u == 5 || u == 6) glds16s(w2o[0][c & 1], wob + (32 * (u - 5) + 4 * c) * 1024, l);
    else if (u < 8) glds16s(w2o[0][c & 1], w2f + (sgn * ((u < 5 ? u : 5) % 3) * 65536 + (u < 3 ? 0 : 32768) + 4096 * c), l);
    else glds16s(w2o[1][c & 1], w2l + (4 * (c & 1) + 2 * (c >> 1)) * 1024, l);
  };
  const int tb1 = p * 4 + wm;
  FragAddr fa;
  fa.xo = (wm * 64 + lc) * 128 + 16 * (lg ^ (lc >> 1));
  fa.w0 = XB + lg * 2560 + (lc >> 2) * 128 + (((2 * wn) ^ ((lc >> 3) | ((lg & 1) << 1))) * 32) + 8 * (lane & 3);
  fa.w4 = XB + lg * 2560 + 2048 + (lc >> 2) * 64 + ((wn ^ (lg & 1)) * 32) + 8 * (lane & 3);
  const int i16 = (idx & 3) + 4 * (ty & 1) + 8 * (bid >> 7);
  const char* pf = ty < 2 ? w2 + (size_t)(448 * (i16 >> 2) + ((i16 & 1) && wn ? 384 + 16 * (i16 & 2) : 96 * (i16 & 2) + 128 * (i16 & 1) + 64 * wn) + 8 * wm) * 1024
                          : wo + (size_t)(i16 * 8 + wave) * 4096;
  const unsigned pfo = (lane & (ty < 2 ? 15 : 7)) * 128, pfs = ty < 2 ? 2048 : 1024;
  const char* pf2 = w2 + (size_t)(448 * (idx >> 3) + 192 * ((idx >> 2) & 1) + 32 + (idx & 3) * 8 + wave) * 1024;
  const unsigned pfo2 = (lane & 7) * 128 + ((lane >> 3) % 3) * 65536;
  auto touch2 = [&]() { glds4s(pfo2, pf2, lds0 + 3 * SLOT + 768); };
  auto touch = [&](int s) { glds4s(pfo, pf + (unsigned)s * pfs, lds0 + 3 * SLOT + 768); };
  SCHED();
  WAIT_VM(7);
  ST(27);
  BAR();
  ST(1);

  f32x4 acc[4][5];
#pragma unroll
  for (int tt = 0; tt < 4; ++tt)
#pragma unroll
    for (int G = 0; G < 5; ++G) acc[tt][G] = (f32x4){0.f, 0.f, 0.f, 0.f};
  v2u gg = {0u, 0u};
  auto load_gg = [&]() { ld8_sc1(gg, lane * 8, ws + OFF_G + tb1 * 512); };
  u64 tag = 0; unsigned tag32 = 0; float gd = 0.f;
  auto wait_gate = [&]() {
    asm volatile("s_waitcnt vmcnt(7) lgkmcnt(0)" : "+v"(xr), "+v"(gr), "+v"(nn)::"memory");
    tag = (((u64)(unsigned)__builtin_amdgcn_readfirstlane(nn[1]) << 32) | (unsigned)__builtin_amdgcn_readfirstlane(nn[0])) + 0x9E3779B97F4A7C15ull;
    tag32 = (unsigned)tag;
  };
  auto gate_piece = [&](int i) {
    if (i < 8) gd += (i & 1) ? bfhi(xr[i >> 1]) * bfhi(gr[i >> 1]) : bflo(xr[i >> 1]) * bflo(gr[i >> 1]);
    if (i == 8) gd = dpp_add<0x111, 0xf>(gd);
    if (i == 9) gd = dpp_add<0x112, 0xf>(gd);
    if (i == 10) gd = dpp_add<0x114, 0xf>(gd);
    if (i == 11) gd = dpp_add<0x118, 0xf>(gd);
    if (i == 12) gd = dpp_add<0x142, 0xa>(gd);
    if (i == 13) gd = dpp_add<0x143, 0xc>(gd);
    if (i == 14) st8_last((v2u){f2u(gd), tag32}, ws + OFF_G + gtok * 8);
  };
  auto gemm1 = [&](auto ktc) {
    constexpr bool KT = decltype(ktc)::value;
    Frags f0, f1;
#pragma unroll
    for (int i = 0; i < 14; ++i) frag_read(f0, i, smem, fa, 0);
    SCHED();
    half_step<KT, true, PR(3)>(acc, f0, f1, smem, fa, 1, [&](int a) {
      if (a < 4) stage_x(a, (rot + 2) & 7, 2 * SLOT); else stage_w(a - 4, (rot + 2) & 7, 2 * SLOT); });
    asm volatile("s_waitcnt vmcnt(7) lgkmcnt(0)" ::: "memory"); BAR(); SCHED(); ST(14);
    half_step<KT, true, 3>(acc, f1, f0, smem + SLOT, fa, 0, [&](int a) { if (a < 3) load_gate(a); else stage_x(a - 3, (rot + 3) & 7, 0); });
    half_step<KT, true, 6>(acc, f0, f1, smem + SLOT, fa, 1, [&](int a) { if (a < 3) stage_w(a, (rot + 3) & 7, 0); });
    wait_gate(); BAR(); SCHED(); ST(15); CY(1);
    half_step<KT, true, 4>(acc, f1, f0, smem + 2 * SLOT, fa, 0, [&](int a) { if (a < 4) stage_x(a, (rot + 4) & 7, SLOT); else touch(0); }, gate_piece);
    unsigned sp = SLOT, so = 2 * SLOT, sn = 0;
#pragma unroll 1
    for (int t = 2; t < 5; ++t) {
      half_step<KT, true, 6>(acc, f0, f1, smem + so, fa, 1, [&](int a) { if (a < 3) stage_w(a, (rot + t + 2) & 7, sp); });
      asm volatile("s_waitcnt vmcnt(8) lgkmcnt(0)" ::: "memory"); BAR(); SCHED(); ST(14 + t); CY(t);
      half_step<KT, true, 4>(acc, f1, f0, smem + sn, fa, 0, [&](int a) { if (a < 4) stage_x(a, (rot + t + 3) & 7, so); else touch(t - 1); });
      const unsigned tmp = sp; sp = so; so = sn; sn = tmp;
    }
    half_step<KT, true, 6>(acc, f0, f1, smem + 2 * SLOT, fa, 1, [&](int a) { if (a < 3) stage_w(a, (rot + 7) & 7, SLOT); });
    asm volatile("s_waitcnt vmcnt(8) lgkmcnt(0)" ::: "memory"); BAR(); SCHED(); ST(19);
    half_step<KT, true, 5>(acc, f1, f0, smem, fa, 0, [&](int a) { if (a < 3) stage2(a); else touch2(); });
    half_step<KT, true, PR(5)>(acc, f0, f1, smem, fa, 1, [&](int a) {
      if (a < 3) stage2(3 + a);
      if (!KT && a == 3) load_gg(); });
    if (KT) asm volatile("s_waitcnt vmcnt(7) lgkmcnt(0)" ::: "memory"); else asm volatile("s_waitcnt vmcnt(8) lgkmcnt(0)" ::: "memory");
    BAR(); SCHED(); ST(20);
    half_step<KT, true, PR(6)>(acc, f1, f0, smem + SLOT, fa, 0, [&](int a) { if (a < 3) stage2(6 + a); });
    half_step<KT, true, PR(6)>(acc, f0, f1, smem + SLOT, fa, 1, [&](int a) { if (a < 3) stage2(9 + a); });
    WAIT_LGKM0(); BAR(); SCHED(); ST(21);
    half_step<KT, false, PR(3)>(acc, f1, f0, smem, fa, 0, [&](int a) { if (a < 6) stage2(12 + a); });
  };
  LAS char* wreg = smem + wave * PIECE;
  v4u W[5][4];
  int tro[4];
#pragma unroll
  for (int nt = 0; nt < 4; ++nt) tro[nt] = lg * 1024 + (lc >> 2) * 128 + ((nt ^ (((lc >> 3) & 1) | ((lg & 1) << 1))) * 32) + 8 * (lane & 3);
  auto read_unit = [&](int u, v4u (&d)[4]) {
    const int k0 = (4 * u) % 18, k2 = (4 * u + 2) % 18;
    const int delta = chunk_off(k2) - chunk_off(k0) - 2048;
    const int dl = delta ? (lg >= 2 ? delta : 0) : 0;
#pragma unroll
    for (int nt = 0; nt < 4; ++nt) {
      LAS char* q = wreg + chunk_off(k0) + tro[nt] + dl;
      d[nt] = cat(tr_read(q), tr_read(q + 512));
    }
  };
  const int hbase = OFF_H + (tb1 * 8 + e) * 36864 + lane * 16;
  auto stage_rng = [&](int a, int b) {
#pragma unroll
    for (int n = a; n < b; ++n) stage2(n);
  };
  auto head = [&]() {
    read_unit(0, W[0]); read_unit(1, W[1]); read_unit(2, W[2]);
    WAIT_LGKM0(); SCHED();
    if (EARLY_OLD || wn) stage_rng(18, 30);
    if (wn) PRIO(PYOUNG); else PRIO(POLD);
    SCHED();
  };
  auto tail = [&]() {
    PRIO(0);
    if (EARLY_OLD || wn) {
      read_unit(3, W[3]); read_unit(4, W[4]);
      WAIT_LGKM0(); SCHED();
      stage_rng(30, 36);
    } else {
      read_unit(3, W[3]);
      stage_rng(18, 30);
      WAIT_LGKM0(); SCHED();
      stage_rng(30, 34);
      WAIT_VM(14);
      read_unit(4, W[4]);
      WAIT_LGKM0(); SCHED();
      stage_rng(34, 36);
    }
  };
  if (ty == 0) {
    gemm1(std::true_type{});
    ST(2);
    WAIT_VM(6);
    head();
    const v4u ones = {0x3f803f80u, 0x3f803f80u, 0x3f803f80u, 0x3f803f80u};
    f32x4 okv = {0.f, 0.f, 0.f, 0.f}, oz = {0.f, 0.f, 0.f, 0.f};
    auto kv_slot = [&](int s) { return smem + (s % 3) * SLOT + ((s / 3) * 2 + (lane >> 5)) * PIECE + 6144 + (lane & 31) * 16; };
#pragma unroll
    for (int i2 = 0; i2 < 2; ++i2) {
      f32x4 kv = {0.f, 0.f, 0.f, 0.f}, z = {0.f, 0.f, 0.f, 0.f};
#pragma unroll
      for (int u = 0; u < 2; ++u) {
        f32x4 v0 = acc[2 * u][2 * i2 + 1], v1 = acc[2 * u + 1][2 * i2 + 1];
        v4u pa = phi8(acc[2 * u][2 * i2], acc[2 * u + 1][2 * i2]);
        v4u vb = {pk2(v0[0], v0[1]), pk2(v0[2], v0[3]), pk2(v1[0], v1[1]), pk2(v1[2], v1[3])};
        kv = mma32(pa, vb, kv);
        z = mma32(pa, ones, z);
      }
      if (wm == i2) { okv = kv; oz = z; }
      else {
        const int s = (wn * 2 + i2) * 3 + (wm > i2 ? wm - 1 : wm);
        *(LAS f32x4*)kv_slot(s) = kv;
        if (lc == 0) *(LAS f32x4*)(smem + 3 * SLOT + s * 64 + lg * 16) = z;
      }
    }
    WAIT_LGKM0(); BAR(); SCHED();
    if (wm < 2) {
#pragma unroll
      for (int m = 0; m < 3; ++m) {
        const int s = (wn * 2 + wm) * 3 + m;
        okv += *(LAS f32x4*)kv_slot(s); oz += *(LAS f32x4*)(smem + 3 * SLOT + s * 64 + lg * 16);
      }
      int o = OFF_KVP + ((4 * e + 2 * wn + wm) * 8 + p) * KVP_STR;
      v4u a = {f2u(okv[0]), tag32, f2u(okv[1]), tag32};
      v4u b = {f2u(okv[2]), tag32, f2u(okv[3]), tag32};
      __builtin_amdgcn_raw_buffer_store_b128(a, wsr, o + lane * 16, 0, SC1);
      __builtin_amdgcn_raw_buffer_store_b128(b, wsr, o + 1024 + lane * 16, 0, SC1);
      if (lc < 2) {
        v4u c = {f2u(lc ? oz[2] : oz[0]), tag32, f2u(lc ? oz[3] : oz[1]), tag32};
        __builtin_amdgcn_raw_buffer_store_b128(c, wsr, o + 2048 + lg * 32 + lc * 16, 0, SC1);
      }
    }
#pragma unroll
    for (int u = 0; u < 2; ++u)
      __builtin_amdgcn_raw_buffer_store_b128(phi8(acc[2 * u][4], acc[2 * u + 1][4]), wsr, hbase + (28 + wn * 2 + u) * 1024, 0, SC1);
    ST(3);
    WAIT_VM(0);
    if (lane == 0) __hip_atomic_store((GAS u64*)(ws + OFF_HF + (tb1 * 8 + e) * 128) + wn, tag, RLX_AGENT);
    ST(4);
    tail();
    if (!(p & 1) && wave == REDW) {
      PRIO(3);
      const int h = 4 * e + (p >> 1);
      const int o = OFF_KVP + h * 8 * KVP_STR;
      f32x4 kv; f32x2 zs;
      for (unsigned spins = 0;; ++spins) {
        v4u a[8], b[8], zz[8];
#pragma unroll
        for (int q = 0; q < 8; ++q) {
          a[q] = __builtin_amdgcn_raw_buffer_load_b128(wsr, o + q * KVP_STR + lane * 16, 0, SC1);
          b[q] = __builtin_amdgcn_raw_buffer_load_b128(wsr, o + q * KVP_STR + 1024 + lane * 16, 0, SC1);
          zz[q] = __builtin_amdgcn_raw_buffer_load_b128(wsr, o + q * KVP_STR + 2048 + lg * 32 + (lc & 2) * 8, 0, SC1);
        }
        SCHED();
        bool ok = true; kv = (f32x4){0.f, 0.f, 0.f, 0.f}; zs = (f32x2){0.f, 0.f};
#pragma unroll
        for (int q = 0; q < 8; ++q) {
          ok &= (a[q][1] == tag32) & (a[q][3] == tag32) & (b[q][1] == tag32) & (b[q][3] == tag32) & (zz[q][1] == tag32) & (zz[q][3] == tag32);
          kv += (f32x4){u2f(a[q][0]), u2f(a[q][2]), u2f(b[q][0]), u2f(b[q][2])};
          zs += (f32x2){u2f(zz[q][0]), u2f(zz[q][2])};
        }
        if (__all(ok) || spins > SPIN_CAP) break;
        __builtin_amdgcn_s_sleep(1);
      }
      v4u f = {pk2(kv[0], kv[1]), pk2(kv[2], kv[3]), pk2(zs[0], zs[1]), tag32};
      __builtin_amdgcn_raw_buffer_store_b128(f, wsr, OFF_KVF + h * 1024 + lane * 16, 0, SC1);
      ST(26);
    }
  } else {
    gemm1(std::false_type{});
    ST(2);
    asm volatile("s_waitcnt vmcnt(6)" : "+v"(gg)::"memory");
    if (!__all(gg[1] == tag32)) {
      for (unsigned spins = 0;; ++spins) {
        u64 v = __hip_atomic_load((const GAS u64*)(ws + OFF_G) + tb1 * 64 + lane, RLX_AGENT);
        gg = (v2u){(unsigned)v, (unsigned)(v >> 32)};
        if (__all(gg[1] == tag32) || spins > SPIN_CAP) break;
        __builtin_amdgcn_s_sleep(1);
      }
    }
    const unsigned iol = f2u(__builtin_fminf(1.f + fexp(u2f(gg[0])), 1e30f));
    float io[4];
#pragma unroll
    for (int tt = 0; tt < 4; ++tt) io[tt] = u2f(__builtin_amdgcn_ds_bpermute((tt * 16 + lc) * 4, iol));
    head();
    const int slot = 2 * (ty - 1) + wn;
#pragma unroll
    for (int tt = 0; tt < 4; ++tt) {
      v4u hv = {swiglu2(lo2(acc[tt][0]), lo2(acc[tt][1]), io[tt]), swiglu2(hi2(acc[tt][0]), hi2(acc[tt][1]), io[tt]),
                swiglu2(lo2(acc[tt][2]), lo2(acc[tt][3]), io[tt]), swiglu2(hi2(acc[tt][2]), hi2(acc[tt][3]), io[tt])};
      __builtin_amdgcn_raw_buffer_store_b128(hv, wsr, hbase + (slot * 4 + tt) * 1024, 0, SC1);
    }
    if (ty >= 2) {
      v4u hv = {ffn_mixed(acc[0][4], io[0]), ffn_mixed(acc[1][4], io[1]), ffn_mixed(acc[2][4], io[2]), ffn_mixed(acc[3][4], io[3])};
      __builtin_amdgcn_raw_buffer_store_b128(hv, wsr, hbase + (24 + (ty - 2) * 2 + wn) * 1024, 0, SC1);
    } else {
#pragma unroll
      for (int u = 0; u < 2; ++u)
        __builtin_amdgcn_raw_buffer_store_b128(phi8(acc[2 * u][4], acc[2 * u + 1][4]), wsr, hbase + (28 + (2 + wn) * 2 + u) * 1024, 0, SC1);
    }
    SCHED();
    ST(3);
    WAIT_VM(0);
    if (lane == 0) __hip_atomic_store((GAS u64*)(ws + OFF_HF + (tb1 * 8 + e) * 128) + ty * 2 + wn, tag, RLX_AGENT);
    ST(4);
    SCHED();
    tail();
    if (bid == 8 * 4 && wave == 0) {
      for (unsigned spins = 0;; ++spins) {
        bool ok = true;
        v4u a[16];
#pragma unroll
        for (int q = 0; q < 16; ++q) a[q] = __builtin_amdgcn_raw_buffer_load_b128(wsr, OFF_G + q * 1024 + lane * 16, 0, SC1);
        SCHED();
#pragma unroll
        for (int q = 0; q < 16; ++q) ok &= (a[q][1] == tag32) & (a[q][3] == tag32);
        if (__all(ok) || spins > SPIN_CAP) break;
        __builtin_amdgcn_s_sleep(1);
      }
      if (lane == 0) __hip_atomic_store((GAS u64*)ws, tag, RLX_AGENT);
    }
  }
  ST(5);
  if (wn) PRIO(P2Y); else PRIO(P2O);
  const GAS u64* fl = (const GAS u64*)(ws + OFF_HF + (tb2 * 8 + e2) * 128) + (lane & 7);
  for (unsigned spins = 0;; ++spins) {
    u64 v = __hip_atomic_load(fl, RLX_AGENT);
    if (__all(v == tag || (lane & 1) || (lane & 7) == 0) || spins > SPIN_CAP) break;
    __builtin_amdgcn_s_sleep(1);
  }
  WAIT_VM(0);
  ST(6);
  f32x4 c2[4][4];
#pragma unroll
  for (int tt = 0; tt < 4; ++tt)
#pragma unroll
    for (int nt = 0; nt < 4; ++nt) c2[tt][nt] = (f32x4){0.f, 0.f, 0.f, 0.f};
  const int hb2 = OFF_H + (tb2 * 8 + e2) * 36864 + lane * 16;
  const int hs0 = par * 16384, hsd = sgn * 8192;
  v4u hq[9][4];
  auto ldq = [&](int qd) {
#pragma unroll
    for (int i = 0; i < 4; ++i)
      hq[qd][i] = qd < 6 ? __builtin_amdgcn_raw_buffer_load_b128(wsr, hb2 + i * 1024, hs0 + (qd % 3) * hsd + (qd < 3 ? 0 : 4096), SC1)
                         : __builtin_amdgcn_raw_buffer_load_b128(wsr, hb2 + (qd * 4 + i) * 1024, 0, SC1);
  };
  auto mmq = [&](const v4u (&w)[4], const v4u (&hf)[4]) {
#pragma unroll
    for (int tt = 0; tt < 4; ++tt)
#pragma unroll
      for (int nt = 0; nt < 4; ++nt) c2[tt][nt] = mma32(w[nt], hf[tt], c2[tt][nt]);
  };
  v4u kvf[4]; v2u g2;
  auto ldkv = [&]() {
#pragma unroll
    for (int i = 0; i < 4; ++i) kvf[i] = __builtin_amdgcn_raw_buffer_load_b128(wsr, OFF_KVF + (4 * e2 + i) * 1024 + lane * 16, 0, SC1);
    g2 = __builtin_amdgcn_raw_buffer_load_b64(wsr, OFF_G + (tb2 * 64 + lane) * 8, 0, SC1);
  };
  auto kvok = [&]() { return __all((kvf[0][3] == tag32) & (kvf[1][3] == tag32) & (kvf[2][3] == tag32) & (kvf[3][3] == tag32) & (g2[1] == tag32)); };
  auto mmf = [&](const v4u (&w)[4], const v4u (&hf)[4], auto fill) {
#pragma unroll
    for (int i = 0; i < 16; ++i) { c2[i >> 2][i & 3] = mma32(w[i & 3], hf[i >> 2], c2[i >> 2][i & 3]); fill(i); SCHED(); }
  };
  ldq(0);
  u64 kf = __hip_atomic_load(fl, RLX_AGENT);
  ldq(1); ldq(2); SCHED();
  mmq(W[0], hq[0]); SCHED(); ST(28);
  for (unsigned spins = 0; !__all(kf == tag) && spins <= SPIN_CAP; ++spins) {
    __builtin_amdgcn_s_sleep(1);
    kf = __hip_atomic_load(fl, RLX_AGENT);
  }
  ST(7);
  ldq(3); ldq(7); ldq(8); SCHED();
  ST(29); mmq(W[1], hq[1]); SCHED(); ST(30); ldkv(); ldq(4); SCHED();
  mmq(W[2], hq[2]); SCHED(); ST(31); ldq(5); SCHED();
  mmq(W[3], hq[3]); SCHED(); ldq(6); SCHED();
  ST(8);
  const bool early = kvok();
  if (!early) {
    for (unsigned spins = 0;; ++spins) {
      ldkv();
      if (kvok() || spins > SPIN_CAP) break;
      __builtin_amdgcn_s_sleep(1);
    }
  }
  ST(9);
  {
    const unsigned igl = f2u(1.f + fexp(-u2f(g2[0])));
    f32x2 ig[2];
#pragma unroll
    for (int tt = 0; tt < 4; ++tt) ig[tt >> 1][tt & 1] = u2f(__builtin_amdgcn_ds_bpermute((tt * 16 + lc) * 4, igl));
    const f32x4 zero = {0.f, 0.f, 0.f, 0.f}, eps = {1e-6f, 1e-6f, 0.f, 0.f};
    f32x4 den[4], n0[4], n1[4]; f32x2 rr[4];
    v4u of[2][4];
    auto small = [&](int so) {
#pragma unroll
      for (int hh = 0; hh < 2; ++hh) {
        const int i = 2 * so + hh;
        const unsigned zv = kvf[i][2];
        const v4u zf = {(lc & 3) == 0 ? zv : 0u, (lc & 3) == 2 ? zv : 0u, (lc & 3) == 1 ? zv : 0u, (lc & 3) == 3 ? zv : 0u};
#pragma unroll
        for (int u = 0; u < 2; ++u) den[hh * 2 + u] = mma32(zf, hq[7 + so][hh * 2 + u], eps);
      }
#pragma unroll
      for (int c = 0; c < 4; ++c) {
        const v2u kv = {kvf[2 * so + (c >> 1)][0], kvf[2 * so + (c >> 1)][1]};
        const v4u qq = hq[7 + so][c];
        n0[c] = mma16(kv, (v2u){qq[0], qq[1]}, zero); n1[c] = mma16(kv, (v2u){qq[2], qq[3]}, zero);
      }
      SCHED();
    };
    auto piece = [&](int so, int i) {
      const int c = i & 3, hh = c >> 1, u = c & 1;
      if (i < 4) { const f32x2 dd = (lo2(den[c]) + hi2(den[c])) * ig[u]; rr[c] = (f32x2){frcp(dd[0]), frcp(dd[1])}; }
      else if (i < 8) {
        of[so][2 * u][hh * 2] = __builtin_bit_cast(unsigned, __builtin_convertvector(lo2(n0[c]) * rr[c][0], bf16x2));
        of[so][2 * u][hh * 2 + 1] = __builtin_bit_cast(unsigned, __builtin_convertvector(hi2(n0[c]) * rr[c][0], bf16x2));
      } else if (i < 12) {
        of[so][2 * u + 1][hh * 2] = __builtin_bit_cast(unsigned, __builtin_convertvector(lo2(n1[c]) * rr[c][1], bf16x2));
        of[so][2 * u + 1][hh * 2 + 1] = __builtin_bit_cast(unsigned, __builtin_convertvector(hi2(n1[c]) * rr[c][1], bf16x2));
      }
    };
    small(0); ST(32);
    mmf(W[4], hq[4], [&](int i) { piece(0, i); });
    ST(33); read_unit(5, W[0]); read_unit(6, W[1]); SCHED();
    small(1); ST(34);
    mmf(W[0], of[0], [&](int i) { piece(1, i); });
    ST(35); read_unit(7, W[2]); SCHED();
    mmq(W[1], of[1]); SCHED();
    read_unit(8, W[3]); SCHED();
  }
  ST(25);
  mmq(W[2], hq[5]); SCHED(); ST(36);
  {
    v4u hf[4];
#pragma unroll
    for (int tt = 0; tt < 4; ++tt) hf[tt] = (v4u){hq[6][0][tt], hq[6][1][tt], hq[6][2][tt], hq[6][3][tt]};
    mmq(W[3], hf);
  }
  ST(10);
#pragma unroll
  for (int tt = 0; tt < 4; ++tt)
#pragma unroll
    for (int nt = 0; nt < 4; ++nt)
      *(LAS f32x4*)(wreg + chunk_off(tt * 4 + nt) + lg * 256 + ((lc ^ (4 * nt + 2 * (lg >> 1))) & 15) * 16) = c2[tt][nt];
  WAIT_LGKM0(); BAR(); SCHED();
  ST(11);
  {
    const int o = lane & 7, c = (wave & 1) * 8 + (lane >> 3), tt = wave >> 1;
    const int k = tt * 4 + (o >> 1);
    const int ro = (k < 6 ? 2 * SLOT + k * 1024 : (k < 12 ? (k - 6) * 1024 : SLOT + (k - 12) * 1024)) + (o & 1) * 512 + ((c ^ (2 * o)) & 15) * 16;
    f32x4 a = {0.f, 0.f, 0.f, 0.f}, b = {0.f, 0.f, 0.f, 0.f};
#pragma unroll
    for (int w = 0; w < 8; ++w) { a += *(LAS f32x4*)(smem + w * PIECE + ro); b += *(LAS f32x4*)(smem + w * PIECE + ro + 256); }
    v4u r = {pk2(a[0], a[1]), pk2(a[2], a[3]), pk2(b[0], b[1]), pk2(b[2], b[3])};
    *(GAS v4u*)(out + (size_t)(tb2 * 64 + tt * 16 + c) * 1024 + nb * 128 + o * 16) = r;
  }
}
}

extern "C" void kernel_launch(void* const* d_in, const int* in_sizes, int n_in, void* d_out, int out_size, void* d_ws, size_t ws_size,
                              hipStream_t stream) {
  fk::fused<<<256, 512, 0, stream>>>((const char*)d_in[0], (const char*)d_in[1], (const char*)d_in[2], (const char*)d_in[4],
                                     (const char*)d_in[5], (const char*)d_in[6], (const char*)d_in[7], (char*)d_out, (char*)d_ws);
}
```

```cpp
#define STAMP
#define DB 0
#include <hip/hip_runtime.h>
#include <hip/hip_bf16.h>
#include <stdint.h>
#include <type_traits>

#define GAS __attribute__((address_space(1)))
#define LAS __attribute__((address_space(3)))
typedef unsigned v4u __attribute__((ext_vector_type(4)));
typedef unsigned v2u __attribute__((ext_vector_type(2)));
typedef float f32x4 __attribute__((ext_vector_type(4)));
typedef float f32x2 __attribute__((ext_vector_type(2)));
typedef short s16x4 __attribute__((ext_vector_type(4)));
typedef __bf16 bf16x8 __attribute__((ext_vector_type(8)));
typedef __bf16 bf16x2 __attribute__((ext_vector_type(2)));
typedef unsigned long long u64;
#define RLX_AGENT __ATOMIC_RELAXED, __HIP_MEMORY_SCOPE_AGENT

namespace fk {
constexpr int XB = 32768, WB = 20480, SLOT = XB + WB;
constexpr int LDS_BYTES = 3 * SLOT + 1024;
constexpr int PIECE = SLOT / 8;
constexpr int OFF_G = 4096;
constexpr int OFF_HF = 32768;
constexpr int OFF_KVF = 69632;
constexpr int OFF_KVP = 131072;
constexpr int KVP_STR = 2304;
constexpr int OFF_H = 1 << 20;
constexpr int WS_USED = OFF_H + 256 * 36864;
constexpr int SC1 = 16;
constexpr unsigned SPIN_CAP = 1u << 15;

#define NSTAMP 38
#define STAMP_OFF (64u << 20)
#define CLK() __builtin_amdgcn_s_memrealtime()
#define REL 0
#define CY(t) do {} while (0)
#define ST(k) do { if ((k) == 0 || (k) == DB) { __builtin_amdgcn_sched_barrier(0); st[k] = (unsigned)CLK(); __builtin_amdgcn_sched_barrier(0); } } while (0)

__device__ __forceinline__ unsigned f2u(float f) { return __builtin_bit_cast(unsigned, f); }
__device__ __forceinline__ float u2f(unsigned u) { return __builtin_bit_cast(float, u); }
__device__ __forceinline__ unsigned pk2(float a, float b) {
  return __builtin_bit_cast(unsigned, __builtin_convertvector((f32x2){a, b}, bf16x2));
}
__device__ __forceinline__ f32x4 mma32(v4u a, v4u b, f32x4 c) {
  return __builtin_amdgcn_mfma_f32_16x16x32_bf16(__builtin_bit_cast(bf16x8, a), __builtin_bit_cast(bf16x8, b), c, 0, 0, 0);
}
__device__ __forceinline__ f32x4 mma16(v2u a, v2u b, f32x4 c) {
  return __builtin_amdgcn_mfma_f32_16x16x16bf16_1k(__builtin_bit_cast(s16x4, a), __builtin_bit_cast(s16x4, b), c, 0, 0, 0);
}
__device__ __forceinline__ v2u tr_read(LAS char* p) {
  return __builtin_bit_cast(v2u, __builtin_amdgcn_ds_read_tr16_b64_v4i16((LAS s16x4*)p));
}
__device__ __forceinline__ v4u cat(v2u a, v2u b) { return (v4u){a[0], a[1], b[0], b[1]}; }
__device__ __forceinline__ void glds16(const void* g, unsigned l) {
  asm volatile("s_mov_b32 m0, %1\n\ts_nop 0\n\tglobal_load_lds_dwordx4 %0, off" ::"v"(g), "s"(l) : "memory");
}
__device__ __forceinline__ void glds16s(unsigned voff, const void* sbase, unsigned l) {
  asm volatile("s_mov_b32 m0, %2\n\ts_nop 0\n\tglobal_load_lds_dwordx4 %0, %1" ::"v"(voff), "s"(sbase), "s"(l) : "memory");
}
__device__ __forceinline__ void glds16_lo(const void* g, unsigned l) {
  asm volatile("s_mov_b32 m0, %1\n\ts_mov_b32 exec_hi, 0\n\tglobal_load_lds_dwordx4 %0, off\n\ts_mov_b32 exec_hi, -1" ::"v"(g), "s"(l) : "memory");
}
__device__ __forceinline__ void ld8_sc1(v2u& d, unsigned voff, const void* sbase) {
  asm volatile("global_load_dwordx2 %0, %1, %2 sc1" : "=v"(d) : "v"(voff), "s"(sbase) : "memory");
}
__device__ __forceinline__ void touch4(unsigned& d, const void* g) {
  asm volatile("global_load_dword %0, %1, off" : "+v"(d) : "v"(g) : "memory");
}
__device__ __forceinline__ void glds4s(unsigned voff, const void* sbase, unsigned l) {
  asm volatile("s_mov_b32 m0, %2\n\ts_nop 0\n\tglobal_load_lds_dword %0, %1" ::"v"(voff), "s"(sbase), "s"(l) : "memory");
}
__device__ __forceinline__ void keep(unsigned& d) { asm volatile("" : "+v"(d)); }
__device__ __forceinline__ void st8_last(v2u d, const void* sbase) {
  asm volatile("s_mov_b32 exec_lo, 0\n\ts_brev_b32 exec_hi, 1\n\tglobal_store_dwordx2 %0, %1, %2 sc1\n\ts_mov_b64 exec, -1" ::"v"(0), "v"(d), "s"(sbase) : "memory");
}
__device__ __forceinline__ void ld16(v4u& d, unsigned voff, const void* sbase) {
  asm volatile("global_load_dwordx4 %0, %1, %2" : "=v"(d) : "v"(voff), "s"(sbase) : "memory");
}
__device__ __forceinline__ float fexp(float x) { return __builtin_amdgcn_exp2f(x * 1.44269504088896f); }
__device__ __forceinline__ float frcp(float x) { return __builtin_amdgcn_rcpf(x); }
__device__ __forceinline__ float bflo(unsigned u) { return u2f(u << 16); }
__device__ __forceinline__ float bfhi(unsigned u) { return u2f(u & 0xffff0000u); }
#define WAIT_VM(n) asm volatile("s_waitcnt vmcnt(" #n ")" ::: "memory")
#define WAIT_LGKM0() asm volatile("s_waitcnt lgkmcnt(0)" ::: "memory")
#define BAR() __builtin_amdgcn_s_barrier()
#define SCHED() __builtin_amdgcn_sched_barrier(0)
#define PRIO(n) __builtin_amdgcn_s_setprio(n)
#define PYOUNG 2
#define POLD 3
#define EARLY_OLD 1
#define P2Y 2
#define P2O 1
#define REDW 2

struct Frags { v4u x[4]; v2u wl[5], wh[5]; };
struct FragAddr { int xo, w0, w4; };
__device__ __forceinline__ void frag_read(Frags& f, int i, LAS char* sp, const FragAddr& a, int ks) {
  if (i >= 2 && i < 6) { f.x[i - 2] = *(LAS v4u*)(sp + (a.xo ^ (ks * 64)) + (i - 2) * 2048); return; }
  const int r = i < 2 ? i : i - 4, G = r >> 1;
  LAS char* p = G < 4 ? sp + (a.w0 ^ ((G >> 1) * 32)) + (G & 1) * 1024 + ks * 10240 + (r & 1) * 512 : sp + a.w4 + ks * 10240 + (r & 1) * 256;
  if (r & 1) f.wh[G] = tr_read(p); else f.wl[G] = tr_read(p);
}
struct NoFill { __device__ __forceinline__ void operator()(int) const {} };
template <bool KT, bool RD, int PER, class Act, class Fill = NoFill>
__device__ __forceinline__ void half_step(f32x4 (&acc)[4][5], const Frags& c, Frags& n, LAS char* sp, const FragAddr& a, int ks, Act act, Fill fill = Fill()) {
#pragma unroll
  for (int i = 0; i < 20; ++i) {
    const int G = i >> 2, tt = i & 3;
    const v4u w = cat(c.wl[G], c.wh[G]);
    acc[tt][G] = (KT && G < 4) ? mma32(c.x[tt], w, acc[tt][G]) : mma32(w, c.x[tt], acc[tt][G]);
    if (RD && i < 14) frag_read(n, i, sp, a, ks);
    if (i % PER == 1) act(i / PER);
    fill(i);
    SCHED();
  }
}
__device__ __forceinline__ unsigned swiglu2(f32x2 g, f32x2 u, float io) {
  const f32x2 t = g * -1.44269504088896f;
  const f32x2 e = {__builtin_amdgcn_exp2f(t[0]), __builtin_amdgcn_exp2f(t[1])};
  const f32x2 i2 = {io, io};
  const f32x2 d = __builtin_elementwise_fma(e, i2, i2);
  const f32x2 r = {frcp(d[0]), frcp(d[1])};
  return __builtin_bit_cast(unsigned, __builtin_convertvector((g * u) * r, bf16x2));
}
__device__ __forceinline__ f32x2 lo2(f32x4 a) { return (f32x2){a[0], a[1]}; }
__device__ __forceinline__ f32x2 hi2(f32x4 a) { return (f32x2){a[2], a[3]}; }
__device__ __forceinline__ unsigned ffn_mixed(f32x4 a, float io) {
  auto s0 = __builtin_amdgcn_permlane32_swap(f2u(a[0]), f2u(a[2]), false, false);
  auto s1 = __builtin_amdgcn_permlane32_swap(f2u(a[1]), f2u(a[3]), false, false);
  return swiglu2((f32x2){u2f(s0[0]), u2f(s1[0])}, (f32x2){u2f(s0[1]), u2f(s1[1])}, io);
}
__device__ __forceinline__ unsigned phi2(f32x2 t) {
  const f32x2 s = t * 1.44269504088896f;
  const f32x2 e = {__builtin_amdgcn_fmed3f(__builtin_amdgcn_exp2f(s[0]), 0.f, 1.f), __builtin_amdgcn_fmed3f(__builtin_amdgcn_exp2f(s[1]), 0.f, 1.f)};
  const f32x2 m = {__builtin_fmaxf(t[0], 0.f), __builtin_fmaxf(t[1], 0.f)};
  return __builtin_bit_cast(unsigned, __builtin_convertvector((m + e) + 1e-6f, bf16x2));
}
__device__ __forceinline__ v4u phi8(f32x4 a, f32x4 b) { return (v4u){phi2(lo2(a)), phi2(hi2(a)), phi2(lo2(b)), phi2(hi2(b))}; }
template <int CTRL, int RM> __device__ __forceinline__ float dpp_add(float v) {
  return v + u2f(__builtin_amdgcn_update_dpp(0u, f2u(v), CTRL, RM, 0xf, false));
}
__device__ __forceinline__ constexpr int chunk_off(int k) { return (k < 6 ? 2 * SLOT : (k < 12 ? 0 : SLOT)) + (k % 6) * 1024; }

#define SPREAD 1
#define PR(n) (SPREAD ? (n) : 3)
#define ROTMODE 0
typedef unsigned v16u __attribute__((ext_vector_type(16)));
__device__ __forceinline__ char* mkptr(unsigned lo, unsigned hi) { u64 a = ((u64)hi << 32) | lo; asm volatile("" : "+s"(a)); return (char*)a; }
__global__ __launch_bounds__(512, 2) void fused(const char* x_, const char* wqkv_, const char* wo_, const char* w1_, const char* w3_,
                                                const char* w2_, const char* gw_, char* out_, char* ws_) {
  v16u ka; v2u kb;
  asm volatile("s_load_dwordx16 %0, %2, 0x0\n\ts_load_dwordx2 %1, %2, 0x40" : "=&s"(ka), "=&s"(kb) : "s"(__builtin_amdgcn_kernarg_segment_ptr()));
  __shared__ __attribute__((aligned(16))) char smem_[LDS_BYTES];
  LAS char* smem = (LAS char*)smem_;
  const unsigned lds0 = (unsigned)(uintptr_t)smem;
  unsigned st[NSTAMP];
#pragma unroll
  for (int k = 0; k < NSTAMP; ++k) st[k] = 0;
  ST(0);
  int lane = threadIdx.x & 63, wave = __builtin_amdgcn_readfirstlane(threadIdx.x >> 6), bid = blockIdx.x;
  int p = (bid & 4) + ((bid >> 3) & 3);
  const int rot = ROTMODE == 0 ? 0 : ROTMODE == 1 ? (2 * bid + (bid & 4)) & 7 : ROTMODE == 2 ? ((bid >> 5) + 2 * (bid >> 3)) & 7 : (((bid >> 5) ^ (bid >> 3)) & 1) * 4;
  const unsigned xo0 = (lane >> 3) * 1024 + 16 * ((lane & 7) ^ (lane >> 4)), xo1 = (xo0 ^ 64) + 8192;
  unsigned oX = (p * 8 + wave) * 32768;
  asm volatile("s_waitcnt lgkmcnt(0)" : "+s"(ka), "+s"(kb), "+s"(oX));
  const char* x = mkptr(ka[0], ka[1]);
  ST(24);
  const char* xw = x + oX;
  auto stage_x = [&](int i, int kt, unsigned slot) {
    glds16s((i & 1) ? xo1 : xo0, xw + kt * 128 + (i >> 1) * 16384, lds0 + slot + wave * 4096 + i * 1024);
  };
#pragma unroll
  for (int i = 0; i < 4; ++i) stage_x(i, rot, 0);
  asm volatile("" : "+s"(wave), "+s"(bid));
  asm volatile("" : "+v"(lane));
  const char *wqkv = mkptr(ka[2], ka[3]), *w1 = mkptr(ka[6], ka[7]), *w3 = mkptr(ka[8], ka[9]);
  const int e = (bid & 3) * 2 + (bid >> 7), ty = (bid >> 5) & 3;
  const unsigned fo = 896 * (bid & 3);
  const unsigned sAB = ty == 0 ? 3072 : 3584, sC = ty < 2 ? 3072 : 3584;
  const unsigned oF = fo + 384 * (bid >> 7) + 128 * ty - 128, oK = 1024 + 128 * e;
  const unsigned oA = (ty == 0 ? oK : oF) + wave * 8 * sAB, oB = oA + (ty == 0 ? 1024 : 0);
  const unsigned voAB = (lane >> 3) * sAB + ((((lane >> 1) & 3) ^ (((lane >> 4) & 1) | ((wave & 1) << 1))) * 32) + (lane & 1) * 16;
  const char* bA = (ty == 0 ? wqkv : w1) + oA;
  const char* bB = (ty == 0 ? wqkv : w3) + oB;
  auto stage_ab = [&](int i, int kt, unsigned slot) {
    glds16s(voAB, (i ? bB : bA) + (unsigned)(kt * 64) * sAB, lds0 + slot + XB + wave * 2560 + i * 1024);
  };
  stage_ab(0, rot, 0); stage_ab(1, rot, 0);
  asm volatile("" : "+s"(wave));
  asm volatile("" : "+v"(lane));
  const unsigned oL = fo + 704 + 64 * (bid >> 7) + 32 * ty, oQ = 128 * e + 64 * ty;
  const char* bC0 = (ty < 2 ? wqkv : w1) + ((ty < 2 ? oQ : oL) + wave * 8 * sC);
  const char* bC1 = (ty < 2 ? wqkv : w3) + ((ty < 2 ? oQ + 16 : oL) + wave * 8 * sC);
  const char* pC = ((lane & 1) ? bC1 : bC0) + (unsigned)(((lane >> 2) & 7) * sC + ((((lane >> 1) ^ wave) & 1) << (ty < 2 ? 5 : 4)));
  auto stage_w = [&](int i, int kt, unsigned slot) {
    if (i < 2) stage_ab(i, kt, slot);
    else glds16_lo(pC + (unsigned)(kt * 64) * sC, lds0 + slot + XB + wave * 2560 + 2048);
  };
  stage_w(2, rot, 0);
  asm volatile("" : "+s"(wave), "+s"(xw), "+s"(bA), "+s"(bB));
  BAR();
#pragma unroll
  for (int i = 0; i < 4; ++i) stage_x(i, (rot + 1) & 7, SLOT);
#pragma unroll
  for (int i = 0; i < 3; ++i) stage_w(i, (rot + 1) & 7, SLOT);
  const char *gw = mkptr(ka[12], ka[13]), *wo = mkptr(ka[4], ka[5]), *w2 = mkptr(ka[10], ka[11]);
  char *ws = mkptr(kb[0], kb[1]), *out = mkptr(ka[14], ka[15]);
  asm volatile("" : "+s"(wave), "+s"(bid), "+s"(p));
  asm volatile("" : "+v"(lane));
  const int xg = bid & 7, idx = bid >> 3, lg = lane >> 4, lc = lane & 15;
  const int wm = wave & 3, wn = wave >> 2;
  const int gtok = p * 256 + (e * 4 + ty) * 8 + wave;
  const __amdgpu_buffer_rsrc_t wsr = __builtin_amdgcn_make_buffer_rsrc(ws, 0, WS_USED, 0x00020000);
  v4u xr, gr; v2u nn;
  auto load_gate = [&](int i) {
    if (i == 0) ld8_sc1(nn, 0, ws);
    if (i == 1) ld16(xr, lane * 16, x + gtok * 1024);
    if (i == 2) ld16(gr, lane * 16, gw);
  };
  const int tb2 = xg * 4 + (idx & 3), nb = idx >> 2, e2 = wave;
  const int jj = lane >> 3;
  const unsigned wbase2 = lds0 + wave * PIECE;
  unsigned w2o[2][2];
#pragma unroll
  for (int c = 0; c < 2; ++c) {
    unsigned col = ((((lane & 7) >> 1) ^ (((jj >> 1) & 1) | (c << 1))) * 32) + (lane & 1) * 16;
    w2o[0][c] = col + (16 * (jj >> 2) + (jj & 3)) * 1024;
    w2o[1][c] = col + (8 * (jj >> 1) + (jj & 1)) * 1024;
  }
  const int par = (idx ^ (idx >> 2)) & 1, sgn = 1 - 2 * par;
  const char* w2f = w2 + (size_t)(448 * (e2 >> 1) + 192 * (e2 & 1) + 128 * par) * 1024 + nb * 128;
  const char* w2l = w2 + (size_t)(448 * (e2 >> 1) + 384 + 32 * (e2 & 1)) * 1024 + nb * 128;
  const char* wob = wo + (size_t)(64 * e2) * 1024 + nb * 128;
  auto stage2 = [&](int n) {
    const int u = n >> 2, c = n & 3;
    const unsigned l = wbase2 + chunk_off(n % 18);
    if (u == 5 || u == 6) glds16s(w2o[0][c & 1], wob + (32 * (u - 5) + 4 * c) * 1024, l);
    else if (u < 8) glds16s(w2o[0][c & 1], w2f + (sgn * ((u < 5 ? u : 5) % 3) * 65536 + (u < 3 ? 0 : 32768) + 4096 * c), l);
    else glds16s(w2o[1][c & 1], w2l + (4 * (c & 1) + 2 * (c >> 1)) * 1024, l);
  };
  const int tb1 = p * 4 + wm;
  FragAddr fa;
  fa.xo = (wm * 64 + lc) * 128 + 16 * (lg ^ (lc >> 1));
  fa.w0 = XB + lg * 2560 + (lc >> 2) * 128 + (((2 * wn) ^ ((lc >> 3) | ((lg & 1) << 1))) * 32) + 8 * (lane & 3);
  fa.w4 = XB + lg * 2560 + 2048 + (lc >> 2) * 64 + ((wn ^ (lg & 1)) * 32) + 8 * (lane & 3);
  const int i16 = (idx & 3) + 4 * (ty & 1) + 8 * (bid >> 7);
  const char* pf = ty < 2 ? w2 + (size_t)(448 * (i16 >> 2) + ((i16 & 1) && wn ? 384 + 16 * (i16 & 2) : 96 * (i16 & 2) + 128 * (i16 & 1) + 64 * wn) + 8 * wm) * 1024
                          : wo + (size_t)(i16 * 8 + wave) * 4096;
  const unsigned pfo = (lane & (ty < 2 ? 15 : 7)) * 128, pfs = ty < 2 ? 2048 : 1024;
  const char* pf2 = w2 + (size_t)(448 * (idx >> 3) + 192 * ((idx >> 2) & 1) + 32 + (idx & 3) * 8 + wave) * 1024;
  const unsigned pfo2 = (lane & 7) * 128 + ((lane >> 3) % 3) * 65536;
  auto touch2 = [&]() { glds4s(pfo2, pf2, lds0 + 3 * SLOT + 768); };
  auto touch = [&](int s) { glds4s(pfo, pf + (unsigned)s * pfs, lds0 + 3 * SLOT + 768); };
  SCHED();
  WAIT_VM(7);
  ST(27);
  BAR();
  ST(1);

  f32x4 acc[4][5];
#pragma unroll
  for (int tt = 0; tt < 4; ++tt)
#pragma unroll
    for (int G = 0; G < 5; ++G) acc[tt][G] = (f32x4){0.f, 0.f, 0.f, 0.f};
  v2u gg = {0u, 0u};
  auto load_gg = [&]() { ld8_sc1(gg, lane * 8, ws + OFF_G + tb1 * 512); };
  u64 tag = 0; unsigned tag32 = 0; float gd = 0.f;
  auto wait_gate = [&]() {
    asm volatile("s_waitcnt vmcnt(7) lgkmcnt(0)" : "+v"(xr), "+v"(gr), "+v"(nn)::"memory");
    tag = (((u64)(unsigned)__builtin_amdgcn_readfirstlane(nn[1]) << 32) | (unsigned)__builtin_amdgcn_readfirstlane(nn[0])) + 0x9E3779B97F4A7C15ull;
    tag32 = (unsigned)tag;
  };
  auto gate_piece = [&](int i) {
    if (i < 8) gd += (i & 1) ? bfhi(xr[i >> 1]) * bfhi(gr[i >> 1]) : bflo(xr[i >> 1]) * bflo(gr[i >> 1]);
    if (i == 8) gd = dpp_add<0x111, 0xf>(gd);
    if (i == 9) gd = dpp_add<0x112, 0xf>(gd);
    if (i == 10) gd = dpp_add<0x114, 0xf>(gd);
    if (i == 11) gd = dpp_add<0x118, 0xf>(gd);
    if (i == 12) gd = dpp_add<0x142, 0xa>(gd);
    if (i == 13) gd = dpp_add<0x143, 0xc>(gd);
    if (i == 14) st8_last((v2u){f2u(gd), tag32}, ws + OFF_G + gtok * 8);
  };
  auto gemm1 = [&](auto ktc) {
    constexpr bool KT = decltype(ktc)::value;
    Frags f0, f1;
#pragma unroll
    for (int i = 0; i < 14; ++i) frag_read(f0, i, smem, fa, 0);
    SCHED();
    half_step<KT, true, PR(3)>(acc, f0, f1, smem, fa, 1, [&](int a) {
      if (a < 4) stage_x(a, (rot + 2) & 7, 2 * SLOT); else stage_w(a - 4, (rot + 2) & 7, 2 * SLOT); });
    asm volatile("s_waitcnt vmcnt(7) lgkmcnt(0)" ::: "memory"); BAR(); SCHED(); ST(14);
    half_step<KT, true, 3>(acc, f1, f0, smem + SLOT, fa, 0, [&](int a) { if (a < 3) load_gate(a); else stage_x(a - 3, (rot + 3) & 7, 0); });
    half_step<KT, true, 6>(acc, f0, f1, smem + SLOT, fa, 1, [&](int a) { if (a < 3) stage_w(a, (rot + 3) & 7, 0); });
    wait_gate(); BAR(); SCHED(); ST(15); CY(1);
    half_step<KT, true, 4>(acc, f1, f0, smem + 2 * SLOT, fa, 0, [&](int a) { if (a < 4) stage_x(a, (rot + 4) & 7, SLOT); else touch(0); }, gate_piece);
    unsigned sp = SLOT, so = 2 * SLOT, sn = 0;
#pragma unroll 1
    for (int t = 2; t < 5; ++t) {
      half_step<KT, true, 6>(acc, f0, f1, smem + so, fa, 1, [&](int a) { if (a < 3) stage_w(a, (rot + t + 2) & 7, sp); });
      asm volatile("s_waitcnt vmcnt(8) lgkmcnt(0)" ::: "memory"); BAR(); SCHED(); ST(14 + t); CY(t);
      half_step<KT, true, 4>(acc, f1, f0, smem + sn, fa, 0, [&](int a) { if (a < 4) stage_x(a, (rot + t + 3) & 7, so); else touch(t - 1); });
      const unsigned tmp = sp; sp = so; so = sn; sn = tmp;
    }
    half_step<KT, true, 6>(acc, f0, f1, smem + 2 * SLOT, fa, 1, [&](int a) { if (a < 3) stage_w(a, (rot + 7) & 7, SLOT); });
    asm volatile("s_waitcnt vmcnt(8) lgkmcnt(0)" ::: "memory"); BAR(); SCHED(); ST(19);
    half_step<KT, true, 5>(acc, f1, f0, smem, fa, 0, [&](int a) { if (a < 3) stage2(a); else touch2(); });
    half_step<KT, true, PR(5)>(acc, f0, f1, smem, fa, 1, [&](int a) {
      if (a < 3) stage2(3 + a);
      if (!KT && a == 3) load_gg(); });
    if (KT) asm volatile("s_waitcnt vmcnt(7) lgkmcnt(0)" ::: "memory"); else asm volatile("s_waitcnt vmcnt(8) lgkmcnt(0)" ::: "memory");
    BAR(); SCHED(); ST(20);
    half_step<KT, true, PR(6)>(acc, f1, f0, smem + SLOT, fa, 0, [&](int a) { if (a < 3) stage2(6 + a); });
    half_step<KT, true, PR(6)>(acc, f0, f1, smem + SLOT, fa, 1, [&](int a) { if (a < 3) stage2(9 + a); });
    WAIT_LGKM0(); BAR(); SCHED(); ST(21);
    half_step<KT, false, PR(3)>(acc, f1, f0, smem, fa, 0, [&](int a) { if (a < 6) stage2(12 + a); });
  };
  LAS char* wreg = smem + wave * PIECE;
  v4u W[5][4];
  int tro[4];
#pragma unroll
  for (int nt = 0; nt < 4; ++nt) tro[nt] = lg * 1024 + (lc >> 2) * 128 + ((nt ^ (((lc >> 3) & 1) | ((lg & 1) << 1))) * 32) + 8 * (lane & 3);
  auto read_unit = [&](int u, v4u (&d)[4]) {
    const int k0 = (4 * u) % 18, k2 = (4 * u + 2) % 18;
    const int delta = chunk_off(k2) - chunk_off(k0) - 2048;
    const int dl = delta ? (lg >= 2 ? delta : 0) : 0;
#pragma unroll
    for (int nt = 0; nt < 4; ++nt) {
      LAS char* q = wreg + chunk_off(k0) + tro[nt] + dl;
      d[nt] = cat(tr_read(q), tr_read(q + 512));
    }
  };
  const int hbase = OFF_H + (tb1 * 8 + e) * 36864 + lane * 16;
  auto stage_rng = [&](int a, int b) {
#pragma unroll
    for (int n = a; n < b; ++n) stage2(n);
  };
  auto head = [&]() {
    read_unit(0, W[0]); read_unit(1, W[1]); read_unit(2, W[2]);
    WAIT_LGKM0(); SCHED();
    if (EARLY_OLD || wn) stage_rng(18, 30);
    if (wn) PRIO(PYOUNG); else PRIO(POLD);
    SCHED();
  };
  auto tail = [&]() {
    PRIO(0);
    if (EARLY_OLD || wn) {
      read_unit(3, W[3]); read_unit(4, W[4]);
      WAIT_LGKM0(); SCHED();
      stage_rng(30, 36);
    } else {
      read_unit(3, W[3]);
      stage_rng(18, 30);
      WAIT_LGKM0(); SCHED();
      stage_rng(30, 34);
      WAIT_VM(14);
      read_unit(4, W[4]);
      WAIT_LGKM0(); SCHED();
      stage_rng(34, 36);
    }
  };
  if (ty == 0) {
    gemm1(std::true_type{});
    ST(2);
    WAIT_VM(6);
    head();
    const v4u ones = {0x3f803f80u, 0x3f803f80u, 0x3f803f80u, 0x3f803f80u};
    f32x4 okv = {0.f, 0.f, 0.f, 0.f}, oz = {0.f, 0.f, 0.f, 0.f};
    auto kv_slot = [&](int s) { return smem + (s % 3) * SLOT + ((s / 3) * 2 + (lane >> 5)) * PIECE + 6144 + (lane & 31) * 16; };
#pragma unroll
    for (int i2 = 0; i2 < 2; ++i2) {
      f32x4 kv = {0.f, 0.f, 0.f, 0.f}, z = {0.f, 0.f, 0.f, 0.f};
#pragma unroll
      for (int u = 0; u < 2; ++u) {
        f32x4 v0 = acc[2 * u][2 * i2 + 1], v1 = acc[2 * u + 1][2 * i2 + 1];
        v4u pa = phi8(acc[2 * u][2 * i2], acc[2 * u + 1][2 * i2]);
        v4u vb = {pk2(v0[0], v0[1]), pk2(v0[2], v0[3]), pk2(v1[0], v1[1]), pk2(v1[2], v1[3])};
        kv = mma32(pa, vb, kv);
        z = mma32(pa, ones, z);
      }
      if (wm == i2) { okv = kv; oz = z; }
      else {
        const int s = (wn * 2 + i2) * 3 + (wm > i2 ? wm - 1 : wm);
        *(LAS f32x4*)kv_slot(s) = kv;
        if (lc == 0) *(LAS f32x4*)(smem + 3 * SLOT + s * 64 + lg * 16) = z;
      }
    }
    WAIT_LGKM0(); BAR(); SCHED();
    if (wm < 2) {
#pragma unroll
      for (int m = 0; m < 3; ++m) {
        const int s = (wn * 2 + wm) * 3 + m;
        okv += *(LAS f32x4*)kv_slot(s); oz += *(LAS f32x4*)(smem + 3 * SLOT + s * 64 + lg * 16);
      }
      int o = OFF_KVP + ((4 * e + 2 * wn + wm) * 8 + p) * KVP_STR;
      v4u a = {f2u(okv[0]), tag32, f2u(okv[1]), tag32};
      v4u b = {f2u(okv[2]), tag32, f2u(okv[3]), tag32};
      __builtin_amdgcn_raw_buffer_store_b128(a, wsr, o + lane * 16, 0, SC1);
      __builtin_amdgcn_raw_buffer_store_b128(b, wsr, o + 1024 + lane * 16, 0, SC1);
      if (lc < 2) {
        v4u c = {f2u(lc ? oz[2] : oz[0]), tag32, f2u(lc ? oz[3] : oz[1]), tag32};
        __builtin_amdgcn_raw_buffer_store_b128(c, wsr, o + 2048 + lg * 32 + lc * 16, 0, SC1);
      }
    }
#pragma unroll
    for (int u = 0; u < 2; ++u)
      __builtin_amdgcn_raw_buffer_store_b128(phi8(acc[2 * u][4], acc[2 * u + 1][4]), wsr, hbase + (28 + wn * 2 + u) * 1024, 0, SC1);
    ST(3);
    WAIT_VM(0);
    if (lane == 0) __hip_atomic_store((GAS u64*)(ws + OFF_HF + (tb1 * 8 + e) * 128) + wn, tag, RLX_AGENT);
    ST(4);
    tail();
    if (!(p & 1) && wave == REDW) {
      PRIO(3);
      const int h = 4 * e + (p >> 1);
      const int o = OFF_KVP + h * 8 * KVP_STR;
      f32x4 kv; f32x2 zs;
      for (unsigned spins = 0;; ++spins) {
        v4u a[8], b[8], zz[8];
#pragma unroll
        for (int q = 0; q < 8; ++q) {
          a[q] = __builtin_amdgcn_raw_buffer_load_b128(wsr, o + q * KVP_STR + lane * 16, 0, SC1);
          b[q] = __builtin_amdgcn_raw_buffer_load_b128(wsr, o + q * KVP_STR + 1024 + lane * 16, 0, SC1);
          zz[q] = __builtin_amdgcn_raw_buffer_load_b128(wsr, o + q * KVP_STR + 2048 + lg * 32 + (lc & 2) * 8, 0, SC1);
        }
        SCHED();
        bool ok = true; kv = (f32x4){0.f, 0.f, 0.f, 0.f}; zs = (f32x2){0.f, 0.f};
#pragma unroll
        for (int q = 0; q < 8; ++q) {
          ok &= (a[q][1] == tag32) & (a[q][3] == tag32) & (b[q][1] == tag32) & (b[q][3] == tag32) & (zz[q][1] == tag32) & (zz[q][3] == tag32);
          kv += (f32x4){u2f(a[q][0]), u2f(a[q][2]), u2f(b[q][0]), u2f(b[q][2])};
          zs += (f32x2){u2f(zz[q][0]), u2f(zz[q][2])};
        }
        if (__all(ok) || spins > SPIN_CAP) break;
        __builtin_amdgcn_s_sleep(1);
      }
      v4u f = {pk2(kv[0], kv[1]), pk2(kv[2], kv[3]), pk2(zs[0], zs[1]), tag32};
      __builtin_amdgcn_raw_buffer_store_b128(f, wsr, OFF_KVF + h * 1024 + lane * 16, 0, SC1);
      ST(26);
    }
  } else {
    gemm1(std::false_type{});
    ST(2);
    asm volatile("s_waitcnt vmcnt(6)" : "+v"(gg)::"memory");
    if (!__all(gg[1] == tag32)) {
      for (unsigned spins = 0;; ++spins) {
        u64 v = __hip_atomic_load((const GAS u64*)(ws + OFF_G) + tb1 * 64 + lane, RLX_AGENT);
        gg = (v2u){(unsigned)v, (unsigned)(v >> 32)};
        if (__all(gg[1] == tag32) || spins > SPIN_CAP) break;
        __builtin_amdgcn_s_sleep(1);
      }
    }
    const unsigned iol = f2u(__builtin_fminf(1.f + fexp(u2f(gg[0])), 1e30f));
    float io[4];
#pragma unroll
    for (int tt = 0; tt < 4; ++tt) io[tt] = u2f(__builtin_amdgcn_ds_bpermute((tt * 16 + lc) * 4, iol));
    head();
    const int slot = 2 * (ty - 1) + wn;
#pragma unroll
    for (int tt = 0; tt < 4; ++tt) {
      v4u hv = {swiglu2(lo2(acc[tt][0]), lo2(acc[tt][1]), io[tt]), swiglu2(hi2(acc[tt][0]), hi2(acc[tt][1]), io[tt]),
                swiglu2(lo2(acc[tt][2]), lo2(acc[tt][3]), io[tt]), swiglu2(hi2(acc[tt][2]), hi2(acc[tt][3]), io[tt])};
      __builtin_amdgcn_raw_buffer_store_b128(hv, wsr, hbase + (slot * 4 + tt) * 1024, 0, SC1);
    }
    if (ty >= 2) {
      v4u hv = {ffn_mixed(acc[0][4], io[0]), ffn_mixed(acc[1][4], io[1]), ffn_mixed(acc[2][4], io[2]), ffn_mixed(acc[3][4], io[3])};
      __builtin_amdgcn_raw_buffer_store_b128(hv, wsr, hbase + (24 + (ty - 2) * 2 + wn) * 1024, 0, SC1);
    } else {
#pragma unroll
      for (int u = 0; u < 2; ++u)
        __builtin_amdgcn_raw_buffer_store_b128(phi8(acc[2 * u][4], acc[2 * u + 1][4]), wsr, hbase + (28 + (2 + wn) * 2 + u) * 1024, 0, SC1);
    }
    SCHED();
    ST(3);
    WAIT_VM(0);
    if (lane == 0) __hip_atomic_store((GAS u64*)(ws + OFF_HF + (tb1 * 8 + e) * 128) + ty * 2 + wn, tag, RLX_AGENT);
    ST(4);
    SCHED();
    tail();
    if (bid == 8 * 4 && wave == 0) {
      for (unsigned spins = 0;; ++spins) {
        bool ok = true;
        v4u a[16];
#pragma unroll
        for (int q = 0; q < 16; ++q) a[q] = __builtin_amdgcn_raw_buffer_load_b128(wsr, OFF_G + q * 1024 + lane * 16, 0, SC1);
        SCHED();
#pragma unroll
        for (int q = 0; q < 16; ++q) ok &= (a[q][1] == tag32) & (a[q][3] == tag32);
        if (__all(ok) || spins > SPIN_CAP) break;
        __builtin_amdgcn_s_sleep(1);
      }
      if (lane == 0) __hip_atomic_store((GAS u64*)ws, tag, RLX_AGENT);
    }
  }
  ST(5);
  if (wn) PRIO(P2Y); else PRIO(P2O);
  const GAS u64* fl = (const GAS u64*)(ws + OFF_HF + (tb2 * 8 + e2) * 128) + (lane & 7);
  for (unsigned spins = 0;; ++spins) {
    u64 v = __hip_atomic_load(fl, RLX_AGENT);
    if (__all(v == tag || (lane & 1) || (lane & 7) == 0) || spins > SPIN_CAP) break;
    __builtin_amdgcn_s_sleep(1);
  }
  WAIT_VM(0);
  ST(6);
  f32x4 c2[4][4];
#pragma unroll
  for (int tt = 0; tt < 4; ++tt)
#pragma unroll
    for (int nt = 0; nt < 4; ++nt) c2[tt][nt] = (f32x4){0.f, 0.f, 0.f, 0.f};
  const int hb2 = OFF_H + (tb2 * 8 + e2) * 36864 + lane * 16;
  const int hs0 = par * 16384, hsd = sgn * 8192;
  v4u hq[9][4];
  auto ldq = [&](int qd) {
#pragma unroll
    for (int i = 0; i < 4; ++i)
      hq[qd][i] = qd < 6 ? __builtin_amdgcn_raw_buffer_load_b128(wsr, hb2 + i * 1024, hs0 + (qd % 3) * hsd + (qd < 3 ? 0 : 4096), SC1)
                         : __builtin_amdgcn_raw_buffer_load_b128(wsr, hb2 + (qd * 4 + i) * 1024, 0, SC1);
  };
  auto mmq = [&](const v4u (&w)[4], const v4u (&hf)[4]) {
#pragma unroll
    for (int tt = 0; tt < 4; ++tt)
#pragma unroll
      for (int nt = 0; nt < 4; ++nt) c2[tt][nt] = mma32(w[nt], hf[tt], c2[tt][nt]);
  };
  v4u kvf[4]; v2u g2;
  auto ldkv = [&]() {
#pragma unroll
    for (int i = 0; i < 4; ++i) kvf[i] = __builtin_amdgcn_raw_buffer_load_b128(wsr, OFF_KVF + (4 * e2 + i) * 1024 + lane * 16, 0, SC1);
    g2 = __builtin_amdgcn_raw_buffer_load_b64(wsr, OFF_G + (tb2 * 64 + lane) * 8, 0, SC1);
  };
  auto kvok = [&]() { return __all((kvf[0][3] == tag32) & (kvf[1][3] == tag32) & (kvf[2][3] == tag32) & (kvf[3][3] == tag32) & (g2[1] == tag32)); };
  auto mmf = [&](const v4u (&w)[4], const v4u (&hf)[4], auto fill) {
#pragma unroll
    for (int i = 0; i < 16; ++i) { c2[i >> 2][i & 3] = mma32(w[i & 3], hf[i >> 2], c2[i >> 2][i & 3]); fill(i); SCHED(); }
  };
  ldq(0);
  u64 kf = __hip_atomic_load(fl, RLX_AGENT);
  ldq(1); ldq(2); SCHED();
  mmq(W[0], hq[0]); SCHED(); ST(28);
  for (unsigned spins = 0; !__all(kf == tag) && spins <= SPIN_CAP; ++spins) {
    __builtin_amdgcn_s_sleep(1);
    kf = __hip_atomic_load(fl, RLX_AGENT);
  }
  ST(7);
  ldq(3); ldq(7); ldq(8); SCHED();
  ST(29); mmq(W[1], hq[1]); SCHED(); ST(30); ldkv(); ldq(4); SCHED();
  mmq(W[2], hq[2]); SCHED(); ST(31); ldq(5); SCHED();
  mmq(W[3], hq[3]); SCHED(); ldq(6); SCHED();
  ST(8);
  const bool early = kvok();
  if (!early) {
    for (unsigned spins = 0;; ++spins) {
      ldkv();
      if (kvok() || spins > SPIN_CAP) break;
      __builtin_amdgcn_s_sleep(1);
    }
  }
  ST(9);
  {
    const unsigned igl = f2u(1.f + fexp(-u2f(g2[0])));
    f32x2 ig[2];
#pragma unroll
    for (int tt = 0; tt < 4; ++tt) ig[tt >> 1][tt & 1] = u2f(__builtin_amdgcn_ds_bpermute((tt * 16 + lc) * 4, igl));
    const f32x4 zero = {0.f, 0.f, 0.f, 0.f}, eps = {1e-6f, 1e-6f, 0.f, 0.f};
    f32x4 den[4], n0[4], n1[4]; f32x2 rr[4];
    v4u of[2][4];
    auto small = [&](int so) {
#pragma unroll
      for (int hh = 0; hh < 2; ++hh) {
        const int i = 2 * so + hh;
        const unsigned zv = kvf[i][2];
        const v4u zf = {(lc & 3) == 0 ? zv : 0u, (lc & 3) == 2 ? zv : 0u, (lc & 3) == 1 ? zv : 0u, (lc & 3) == 3 ? zv : 0u};
#pragma unroll
        for (int u = 0; u < 2; ++u) den[hh * 2 + u] = mma32(zf, hq[7 + so][hh * 2 + u], eps);
      }
#pragma unroll
      for (int c = 0; c < 4; ++c) {
        const v2u kv = {kvf[2 * so + (c >> 1)][0], kvf[2 * so + (c >> 1)][1]};
        const v4u qq = hq[7 + so][c];
        n0[c] = mma16(kv, (v2u){qq[0], qq[1]}, zero); n1[c] = mma16(kv, (v2u){qq[2], qq[3]}, zero);
      }
      SCHED();
    };
    auto piece = [&](int so, int i) {
      const int c = i & 3, hh = c >> 1, u = c & 1;
      if (i < 4) { const f32x2 dd = (lo2(den[c]) + hi2(den[c])) * ig[u]; rr[c] = (f32x2){frcp(dd[0]), frcp(dd[1])}; }
      else if (i < 8) {
        of[so][2 * u][hh * 2] = __builtin_bit_cast(unsigned, __builtin_convertvector(lo2(n0[c]) * rr[c][0], bf16x2));
        of[so][2 * u][hh * 2 + 1] = __builtin_bit_cast(unsigned, __builtin_convertvector(hi2(n0[c]) * rr[c][0], bf16x2));
      } else if (i < 12) {
        of[so][2 * u + 1][hh * 2] = __builtin_bit_cast(unsigned, __builtin_convertvector(lo2(n1[c]) * rr[c][1], bf16x2));
        of[so][2 * u + 1][hh * 2 + 1] = __builtin_bit_cast(unsigned, __builtin_convertvector(hi2(n1[c]) * rr[c][1], bf16x2));
      }
    };
    small(0); ST(32);
    mmf(W[4], hq[4], [&](int i) { piece(0, i); });
    ST(33); read_unit(5, W[0]); read_unit(6, W[1]); SCHED();
    small(1); ST(34);
    mmf(W[0], of[0], [&](int i) { piece(1, i); });
    ST(35); read_unit(7, W[2]); SCHED();
    mmq(W[1], of[1]); SCHED();
    read_unit(8, W[3]); SCHED();
  }
  ST(25);
  mmq(W[2], hq[5]); SCHED(); ST(36);
  {
    v4u hf[4];
#pragma unroll
    for (int tt = 0; tt < 4; ++tt) hf[tt] = (v4u){hq[6][0][tt], hq[6][1][tt], hq[6][2][tt], hq[6][3][tt]};
    mmq(W[3], hf);
  }
  ST(10);
#pragma unroll
  for (int tt = 0; tt < 4; ++tt)
#pragma unroll
    for (int nt = 0; nt < 4; ++nt)
      *(LAS f32x4*)(wreg + chunk_off(tt * 4 + nt) + lg * 256 + ((lc ^ (4 * nt + 2 * (lg >> 1))) & 15) * 16) = c2[tt][nt];
  WAIT_LGKM0(); BAR(); SCHED();
  ST(11);
  {
    const int o = lane & 7, c = (wave & 1) * 8 + (lane >> 3), tt = wave >> 1;
    const int k = tt * 4 + (o >> 1);
    const int ro = (k < 6 ? 2 * SLOT + k * 1024 : (k < 12 ? (k - 6) * 1024 : SLOT + (k - 12) * 1024)) + (o & 1) * 512 + ((c ^ (2 * o)) & 15) * 16;
    f32x4 a = {0.f, 0.f, 0.f, 0.f}, b = {0.f, 0.f, 0.f, 0.f};
#pragma unroll
    for (int w = 0; w < 8; ++w) { a += *(LAS f32x4*)(smem + w * PIECE + ro); b += *(LAS f32x4*)(smem + w * PIECE + ro + 256); }
    v4u r = {pk2(a[0], a[1]), pk2(a[2], a[3]), pk2(b[0], b[1]), pk2(b[2], b[3])};
    *(GAS v4u*)(out + (size_t)(tb2 * 64 + tt * 16 + c) * 1024 + nb * 128 + o * 16) = r;
  }
  ST(12);
  WAIT_VM(0);
  ST(13);
  { const unsigned dl = 6000 + 4 * (st[DB] - st[0]); while ((unsigned)__builtin_amdgcn_s_memrealtime() - st[0] < dl) __builtin_amdgcn_s_sleep(2); }
}
}

extern "C" void kernel_launch(void* const* d_in, const int* in_sizes, int n_in, void* d_out, int out_size, void* d_ws, size_t ws_size,
                              hipStream_t stream) {
  fk::fused<<<256, 512, 0, stream>>>((const char*)d_in[0], (const char*)d_in[1], (const char*)d_in[2], (const char*)d_in[4],
                                     (const char*)d_in[5], (const char*)d_in[6], (const char*)d_in[7], (char*)d_out, (char*)d_ws);
}
```

```cpp
#include <hip/hip_runtime.h>
#include <hip/hip_bf16.h>
#include <stdint.h>
#include <type_traits>

#define GAS __attribute__((address_space(1)))
#define LAS __attribute__((address_space(3)))
typedef unsigned v4u __attribute__((ext_vector_type(4)));
typedef unsigned v2u __attribute__((ext_vector_type(2)));
typedef float f32x4 __attribute__((ext_vector_type(4)));
typedef float f32x2 __attribute__((ext_vector_type(2)));
typedef short s16x4 __attribute__((ext_vector_type(4)));
typedef __bf16 bf16x8 __attribute__((ext_vector_type(8)));
typedef __bf16 bf16x2 __attribute__((ext_vector_type(2)));
typedef unsigned long long u64;
#define RLX_AGENT __ATOMIC_RELAXED, __HIP_MEMORY_SCOPE_AGENT

namespace fk {
constexpr int XB = 32768, WB = 20480, SLOT = XB + WB;
constexpr int LDS_BYTES = 3 * SLOT + 1024;
constexpr int PIECE = SLOT / 8;
constexpr int OFF_G = 4096;
constexpr int OFF_HF = 32768;
constexpr int OFF_KVF = 69632;
constexpr int OFF_KVP = 131072;
constexpr int KVP_STR = 2304;
constexpr int OFF_H = 1 << 20;
constexpr int WS_USED = OFF_H + 256 * 36864;
constexpr int SC1 = 16;
constexpr unsigned SPIN_CAP = 1u << 15;

#define ST(k) do {} while (0)
#define CY(t) do {} while (0)

__device__ __forceinline__ unsigned f2u(float f) { return __builtin_bit_cast(unsigned, f); }
__device__ __forceinline__ float u2f(unsigned u) { return __builtin_bit_cast(float, u); }
__device__ __forceinline__ unsigned pk2(float a, float b) {
  return __builtin_bit_cast(unsigned, __builtin_convertvector((f32x2){a, b}, bf16x2));
}
__device__ __forceinline__ f32x4 mma32(v4u a, v4u b, f32x4 c) {
  return __builtin_amdgcn_mfma_f32_16x16x32_bf16(__builtin_bit_cast(bf16x8, a), __builtin_bit_cast(bf16x8, b), c, 0, 0, 0);
}
__device__ __forceinline__ f32x4 mma16(v2u a, v2u b, f32x4 c) {
  return __builtin_amdgcn_mfma_f32_16x16x16bf16_1k(__builtin_bit_cast(s16x4, a), __builtin_bit_cast(s16x4, b), c, 0, 0, 0);
}
__device__ __forceinline__ v2u tr_read(LAS char* p) {
  return __builtin_bit_cast(v2u, __builtin_amdgcn_ds_read_tr16_b64_v4i16((LAS s16x4*)p));
}
__device__ __forceinline__ v4u cat(v2u a, v2u b) { return (v4u){a[0], a[1], b[0], b[1]}; }
__device__ __forceinline__ void glds16(const void* g, unsigned l) {
  asm volatile("s_mov_b32 m0, %1\n\ts_nop 0\n\tglobal_load_lds_dwordx4 %0, off" ::"v"(g), "s"(l) : "memory");
}
__device__ __forceinline__ void glds16s(unsigned voff, const void* sbase, unsigned l) {
  asm volatile("s_mov_b32 m0, %2\n\ts_nop 0\n\tglobal_load_lds_dwordx4 %0, %1" ::"v"(voff), "s"(sbase), "s"(l) : "memory");
}
__device__ __forceinline__ void glds16_lo(const void* g, unsigned l) {
  asm volatile("s_mov_b32 m0, %1\n\ts_mov_b32 exec_hi, 0\n\tglobal_load_lds_dwordx4 %0, off\n\ts_mov_b32 exec_hi, -1" ::"v"(g), "s"(l) : "memory");
}
__device__ __forceinline__ void ld8_sc1(v2u& d, unsigned voff, const void* sbase) {
  asm volatile("global_load_dwordx2 %0, %1, %2 sc1" : "=v"(d) : "v"(voff), "s"(sbase) : "memory");
}
__device__ __forceinline__ void touch4(unsigned& d, const void* g) {
  asm volatile("global_load_dword %0, %1, off" : "+v"(d) : "v"(g) : "memory");
}
__device__ __forceinline__ void glds4s(unsigned voff, const void* sbase, unsigned l) {
  asm volatile("s_mov_b32 m0, %2\n\ts_nop 0\n\tglobal_load_lds_dword %0, %1" ::"v"(voff), "s"(sbase), "s"(l) : "memory");
}
__device__ __forceinline__ void glds4(const void* g, unsigned l) {
  asm volatile("s_mov_b32 m0, %1\n\ts_nop 0\n\tglobal_load_lds_dword %0, off" ::"v"(g), "s"(l) : "memory");
}
__device__ __forceinline__ void keep(unsigned& d) { asm volatile("" : "+v"(d)); }
__device__ __forceinline__ void st8_last(v2u d, const void* sbase) {
  asm volatile("s_mov_b32 exec_lo, 0\n\ts_brev_b32 exec_hi, 1\n\tglobal_store_dwordx2 %0, %1, %2 sc1\n\ts_mov_b64 exec, -1" ::"v"(0), "v"(d), "s"(sbase) : "memory");
}
__device__ __forceinline__ void ld16(v4u& d, unsigned voff, const void* sbase) {
  asm volatile("global_load_dwordx4 %0, %1, %2" : "=v"(d) : "v"(voff), "s"(sbase) : "memory");
}
__device__ __forceinline__ float fexp(float x) { return __builtin_amdgcn_exp2f(x * 1.44269504088896f); }
__device__ __forceinline__ float frcp(float x) { return __builtin_amdgcn_rcpf(x); }
__device__ __forceinline__ float bflo(unsigned u) { return u2f(u << 16); }
__device__ __forceinline__ float bfhi(unsigned u) { return u2f(u & 0xffff0000u); }
#define WAIT_VM(n) asm volatile("s_waitcnt vmcnt(" #n ")" ::: "memory")
#define WAIT_LGKM0() asm volatile("s_waitcnt lgkmcnt(0)" ::: "memory")
#define BAR() __builtin_amdgcn_s_barrier()
#define SCHED() __builtin_amdgcn_sched_barrier(0)
#define PRIO(n) __builtin_amdgcn_s_setprio(n)
#define PYOUNG 2
#define POLD 3
#define EARLY_OLD 1
#define P2Y 2
#define P2O 1
#define MPI 19
#define REDW 2

struct Frags { v4u x[4]; v2u wl[5], wh[5]; };
struct FragAddr { int xo, w0, w4; };
__device__ __forceinline__ void frag_read(Frags& f, int i, LAS char* sp, const FragAddr& a, int ks) {
  if (i >= 2 && i < 6) { f.x[i - 2] = *(LAS v4u*)(sp + (a.xo ^ (ks * 64)) + (i - 2) * 2048); return; }
  const int r = i < 2 ? i : i - 4, G = r >> 1;
  LAS char* p = G < 4 ? sp + (a.w0 ^ ((G >> 1) * 32)) + (G & 1) * 1024 + ks * 10240 + (r & 1) * 512 : sp + a.w4 + ks * 10240 + (r & 1) * 256;
  if (r & 1) f.wh[G] = tr_read(p); else f.wl[G] = tr_read(p);
}
struct NoFill { __device__ __forceinline__ void operator()(int) const {} };
template <bool KT, bool RD, int PER, class Act, class Fill = NoFill>
__device__ __forceinline__ void half_step(f32x4 (&acc)[4][5], const Frags& c, Frags& n, LAS char* sp, const FragAddr& a, int ks, Act act, Fill fill = Fill()) {
#pragma unroll
  for (int i = 0; i < 20; ++i) {
    const int G = i >> 2, tt = i & 3;
    const v4u w = cat(c.wl[G], c.wh[G]);
    acc[tt][G] = (KT && G < 4) ? mma32(c.x[tt], w, acc[tt][G]) : mma32(w, c.x[tt], acc[tt][G]);
    if (RD && i < 14) frag_read(n, i, sp, a, ks);
    if (i % PER == 1) act(i / PER);
    fill(i);
    SCHED();
  }
}
__device__ __forceinline__ unsigned swiglu2(f32x2 g, f32x2 u, float io) {
  const f32x2 t = g * -1.44269504088896f;
  const f32x2 e = {__builtin_amdgcn_exp2f(t[0]), __builtin_amdgcn_exp2f(t[1])};
  const f32x2 i2 = {io, io};
  const f32x2 d = __builtin_elementwise_fma(e, i2, i2);
  const f32x2 r = {frcp(d[0]), frcp(d[1])};
  return __builtin_bit_cast(unsigned, __builtin_convertvector((g * u) * r, bf16x2));
}
__device__ __forceinline__ f32x2 lo2(f32x4 a) { return (f32x2){a[0], a[1]}; }
__device__ __forceinline__ f32x2 hi2(f32x4 a) { return (f32x2){a[2], a[3]}; }
__device__ __forceinline__ unsigned ffn_mixed(f32x4 a, float io) {
  auto s0 = __builtin_amdgcn_permlane32_swap(f2u(a[0]), f2u(a[2]), false, false);
  auto s1 = __builtin_amdgcn_permlane32_swap(f2u(a[1]), f2u(a[3]), false, false);
  return swiglu2((f32x2){u2f(s0[0]), u2f(s1[0])}, (f32x2){u2f(s0[1]), u2f(s1[1])}, io);
}
__device__ __forceinline__ unsigned phi2(f32x2 t) {
  const f32x2 s = t * 1.44269504088896f;
  const f32x2 e = {__builtin_amdgcn_fmed3f(__builtin_amdgcn_exp2f(s[0]), 0.f, 1.f), __builtin_amdgcn_fmed3f(__builtin_amdgcn_exp2f(s[1]), 0.f, 1.f)};
  const f32x2 m = {__builtin_fmaxf(t[0], 0.f), __builtin_fmaxf(t[1], 0.f)};
  return __builtin_bit_cast(unsigned, __builtin_convertvector((m + e) + 1e-6f, bf16x2));
}
__device__ __forceinline__ v4u phi8(f32x4 a, f32x4 b) { return (v4u){phi2(lo2(a)), phi2(hi2(a)), phi2(lo2(b)), phi2(hi2(b))}; }
template <int CTRL, int RM> __device__ __forceinline__ float dpp_add(float v) {
  return v + u2f(__builtin_amdgcn_update_dpp(0u, f2u(v), CTRL, RM, 0xf, false));
}
__device__ __forceinline__ constexpr int chunk_off(int k) { return (k < 6 ? 2 * SLOT : (k < 12 ? 0 : SLOT)) + (k % 6) * 1024; }

#define SPREAD 1
#define PR(n) (SPREAD ? (n) : 3)
#define ROTMODE 0
typedef unsigned v16u __attribute__((ext_vector_type(16)));
__device__ __forceinline__ char* mkptr(unsigned lo, unsigned hi) { u64 a = ((u64)hi << 32) | lo; asm volatile("" : "+s"(a)); return (char*)a; }
__global__ __launch_bounds__(512, 2) void fused(const char* x_, const char* wqkv_, const char* wo_, const char* w1_, const char* w3_,
                                                const char* w2_, const char* gw_, char* out_, char* ws_) {
  v16u ka; v2u kb;
  asm volatile("s_load_dwordx16 %0, %2, 0x0\n\ts_load_dwordx2 %1, %2, 0x40" : "=&s"(ka), "=&s"(kb) : "s"(__builtin_amdgcn_kernarg_segment_ptr()));
  __shared__ __attribute__((aligned(16))) char smem_[LDS_BYTES];
  LAS char* smem = (LAS char*)smem_;
  const unsigned lds0 = (unsigned)(uintptr_t)smem;
  ST(0);
  int lane = threadIdx.x & 63, wave = __builtin_amdgcn_readfirstlane(threadIdx.x >> 6), bid = blockIdx.x;
  int p = (bid & 4) + ((bid >> 3) & 3);
  const int rot = ROTMODE == 0 ? 0 : ROTMODE == 1 ? (2 * bid + (bid & 4)) & 7 : ROTMODE == 2 ? ((bid >> 5) + 2 * (bid >> 3)) & 7 : (((bid >> 5) ^ (bid >> 3)) & 1) * 4;
  const unsigned xo0 = (lane >> 3) * 1024 + 16 * ((lane & 7) ^ (lane >> 4)), xo1 = (xo0 ^ 64) + 8192;
  unsigned oX = (p * 8 + wave) * 32768;
  asm volatile("s_waitcnt lgkmcnt(0)" : "+s"(ka), "+s"(kb), "+s"(oX));
  const char* x = mkptr(ka[0], ka[1]);
  ST(24);
  const char* xw = x + oX;
  auto stage_x = [&](int i, int kt, unsigned slot) {
    glds16s((i & 1) ? xo1 : xo0, xw + kt * 128 + (i >> 1) * 16384, lds0 + slot + wave * 4096 + i * 1024);
  };
#pragma unroll
  for (int i = 0; i < 4; ++i) stage_x(i, rot, 0);
  asm volatile("" : "+s"(wave), "+s"(bid));
  asm volatile("" : "+v"(lane));
  const char *wqkv = mkptr(ka[2], ka[3]), *w1 = mkptr(ka[6], ka[7]), *w3 = mkptr(ka[8], ka[9]);
  const int e = (bid & 3) * 2 + (bid >> 7), ty = (bid >> 5) & 3;
  const unsigned fo = 896 * (bid & 3);
  const unsigned sAB = ty == 0 ? 3072 : 3584, sC = ty < 2 ? 3072 : 3584;
  const unsigned oF = fo + 384 * (bid >> 7) + 128 * ty - 128, oK = 1024 + 128 * e;
  const unsigned oA = (ty == 0 ? oK : oF) + wave * 8 * sAB, oB = oA + (ty == 0 ? 1024 : 0);
  const unsigned voAB = (lane >> 3) * sAB + ((((lane >> 1) & 3) ^ (((lane >> 4) & 1) | ((wave & 1) << 1))) * 32) + (lane & 1) * 16;
  const char* bA = (ty == 0 ? wqkv : w1) + oA;
  const char* bB = (ty == 0 ? wqkv : w3) + oB;
  auto stage_ab = [&](int i, int kt, unsigned slot) {
    glds16s(voAB, (i ? bB : bA) + (unsigned)(kt * 64) * sAB, lds0 + slot + XB + wave * 2560 + i * 1024);
  };
  stage_ab(0, rot, 0); stage_ab(1, rot, 0);
  asm volatile("" : "+s"(wave));
  asm volatile("" : "+v"(lane));
  const unsigned oL = fo + 704 + 64 * (bid >> 7) + 32 * ty, oQ = 128 * e + 64 * ty;
  const char* bC0 = (ty < 2 ? wqkv : w1) + ((ty < 2 ? oQ : oL) + wave * 8 * sC);
  const char* bC1 = (ty < 2 ? wqkv : w3) + ((ty < 2 ? oQ + 16 : oL) + wave * 8 * sC);
  const char* pC = ((lane & 1) ? bC1 : bC0) + (unsigned)(((lane >> 2) & 7) * sC + ((((lane >> 1) ^ wave) & 1) << (ty < 2 ? 5 : 4)));
  auto stage_w = [&](int i, int kt, unsigned slot) {
    if (i < 2) stage_ab(i, kt, slot);
    else glds16_lo(pC + (unsigned)(kt * 64) * sC, lds0 + slot + XB + wave * 2560 + 2048);
  };
  stage_w(2, rot, 0);
  asm volatile("" : "+s"(wave), "+s"(xw), "+s"(bA), "+s"(bB));
  BAR();
#pragma unroll
  for (int i = 0; i < 4; ++i) stage_x(i, (rot + 1) & 7, SLOT);
#pragma unroll
  for (int i = 0; i < 3; ++i) stage_w(i, (rot + 1) & 7, SLOT);
  const char *gw = mkptr(ka[12], ka[13]), *wo = mkptr(ka[4], ka[5]), *w2 = mkptr(ka[10], ka[11]);
  char *ws = mkptr(kb[0], kb[1]), *out = mkptr(ka[14], ka[15]);
  asm volatile("" : "+s"(wave), "+s"(bid), "+s"(p));
  asm volatile("" : "+v"(lane));
  const int xg = bid & 7, idx = bid >> 3, lg = lane >> 4, lc = lane & 15;
  const int wm = wave & 3, wn = wave >> 2;
  const int gtok = p * 256 + (e * 4 + ty) * 8 + wave;
  const __amdgpu_buffer_rsrc_t wsr = __builtin_amdgcn_make_buffer_rsrc(ws, 0, WS_USED, 0x00020000);
  v4u xr, gr; v2u nn;
  auto load_gate = [&](int i) {
    if (i == 0) ld8_sc1(nn, 0, ws);
    if (i == 1) ld16(xr, lane * 16, x + gtok * 1024);
    if (i == 2) ld16(gr, lane * 16, gw);
  };
  const int tb2 = xg * 4 + (idx & 3), nb = idx >> 2, e2 = wave;
  const int jj = lane >> 3;
  const unsigned wbase2 = lds0 + wave * PIECE;
  unsigned w2o[2][2];
#pragma unroll
  for (int c = 0; c < 2; ++c) {
    unsigned col = ((((lane & 7) >> 1) ^ (((jj >> 1) & 1) | (c << 1))) * 32) + (lane & 1) * 16;
    w2o[0][c] = col + (16 * (jj >> 2) + (jj & 3)) * 1024;
    w2o[1][c] = col + (8 * (jj >> 1) + (jj & 1)) * 1024;
  }
  const int par = (idx ^ (idx >> 2)) & 1, sgn = 1 - 2 * par;
  const char* w2f = w2 + (size_t)(448 * (e2 >> 1) + 192 * (e2 & 1) + 128 * par) * 1024 + nb * 128;
  const char* w2l = w2 + (size_t)(448 * (e2 >> 1) + 384 + 32 * (e2 & 1)) * 1024 + nb * 128;
  const char* wob = wo + (size_t)(64 * e2) * 1024 + nb * 128;
  auto stage2 = [&](int n) {
    const int u = n >> 2, c = n & 3;
    const unsigned l = wbase2 + chunk_off(n % 18);
    if (u == 5 || u == 6) glds16s(w2o[0][c & 1], wob + (32 * (u - 5) + 4 * c) * 1024, l);
    else if (u < 8) glds16s(w2o[0][c & 1], w2f + (sgn * ((u < 5 ? u : 5) % 3) * 65536 + (u < 3 ? 0 : 32768) + 4096 * c), l);
    else glds16s(w2o[1][c & 1], w2l + (4 * (c & 1) + 2 * (c >> 1)) * 1024, l);
  };
  const int tb1 = p * 4 + wm;
  FragAddr fa;
  fa.xo = (wm * 64 + lc) * 128 + 16 * (lg ^ (lc >> 1));
  fa.w0 = XB + lg * 2560 + (lc >> 2) * 128 + (((2 * wn) ^ ((lc >> 3) | ((lg & 1) << 1))) * 32) + 8 * (lane & 3);
  fa.w4 = XB + lg * 2560 + 2048 + (lc >> 2) * 64 + ((wn ^ (lg & 1)) * 32) + 8 * (lane & 3);
  const int i16 = (idx & 3) + 4 * (ty & 1) + 8 * (bid >> 7);
  const char* pf = ty < 2 ? w2 + (size_t)(448 * (i16 >> 2) + ((i16 & 1) && wn ? 384 + 16 * (i16 & 2) : 96 * (i16 & 2) + 128 * (i16 & 1) + 64 * wn) + 8 * wm) * 1024
                          : wo + (size_t)(i16 * 8 + wave) * 4096;
  const unsigned pfo = (lane & (ty < 2 ? 15 : 7)) * 128, pfs = ty < 2 ? 2048 : 1024;
  const char* pf2 = w2 + (size_t)(448 * (idx >> 3) + 192 * ((idx >> 2) & 1) + 32 + (idx & 3) * 8 + wave) * 1024;
  const unsigned pfo2 = (lane & 7) * 128 + ((lane >> 3) % 3) * 65536;
  const char* mp;
  {
    const unsigned gwv = bid * 8 + wave;
    const unsigned i4 = gwv * 4 + (lane & 3), i4c = i4 < 7168 ? i4 : 7167;
    const char* b = lane < 4 || lane > 24 ? x : lane < 8 ? wqkv : lane < 12 ? w1 : lane < 16 ? w3 : lane < 23 ? w2 : wo;
    const unsigned o = lane < 4 || lane > 24 ? gwv * 8 + 4 + (lane & 3) : lane < 8 ? 6144 + gwv * 3 + (lane == 7 ? 0 : lane - 4) : lane < 16 ? 7168 + i4c
                     : lane < 23 ? gwv * 7 + lane - 16 : gwv * 2 + lane - 23;
    mp = b + o * 128;
  }
  auto touch2 = [&]() { glds4s(pfo2, pf2, lds0 + 3 * SLOT + 768); };
  auto touch = [&](int s) { glds4s(pfo, pf + (unsigned)s * pfs, lds0 + 3 * SLOT + 768); };
  SCHED();
  WAIT_VM(7);
  ST(27);
  BAR();
  ST(1);

  f32x4 acc[4][5];
#pragma unroll
  for (int tt = 0; tt < 4; ++tt)
#pragma unroll
    for (int G = 0; G < 5; ++G) acc[tt][G] = (f32x4){0.f, 0.f, 0.f, 0.f};
  v2u gg = {0u, 0u};
  auto load_gg = [&]() { ld8_sc1(gg, lane * 8, ws + OFF_G + tb1 * 512); };
  u64 tag = 0; unsigned tag32 = 0; float gd = 0.f;
  auto wait_gate = [&]() {
    asm volatile("s_waitcnt vmcnt(8) lgkmcnt(0)" : "+v"(xr), "+v"(gr), "+v"(nn)::"memory");
    tag = (((u64)(unsigned)__builtin_amdgcn_readfirstlane(nn[1]) << 32) | (unsigned)__builtin_amdgcn_readfirstlane(nn[0])) + 0x9E3779B97F4A7C15ull;
    tag32 = (unsigned)tag;
  };
  auto gate_piece = [&](int i) {
    if (i < 8) gd += (i & 1) ? bfhi(xr[i >> 1]) * bfhi(gr[i >> 1]) : bflo(xr[i >> 1]) * bflo(gr[i >> 1]);
    if (i == 8) gd = dpp_add<0x111, 0xf>(gd);
    if (i == 9) gd = dpp_add<0x112, 0xf>(gd);
    if (i == 10) gd = dpp_add<0x114, 0xf>(gd);
    if (i == 11) gd = dpp_add<0x118, 0xf>(gd);
    if (i == 12) gd = dpp_add<0x142, 0xa>(gd);
    if (i == 13) gd = dpp_add<0x143, 0xc>(gd);
    if (i == 14) st8_last((v2u){f2u(gd), tag32}, ws + OFF_G + gtok * 8);
  };
  auto gemm1 = [&](auto ktc) {
    constexpr bool KT = decltype(ktc)::value;
    Frags f0, f1;
#pragma unroll
    for (int i = 0; i < 14; ++i) frag_read(f0, i, smem, fa, 0);
    SCHED();
    half_step<KT, true, PR(3)>(acc, f0, f1, smem, fa, 1, [&](int a) {
      if (a < 4) stage_x(a, (rot + 2) & 7, 2 * SLOT); else stage_w(a - 4, (rot + 2) & 7, 2 * SLOT); });
    asm volatile("s_waitcnt vmcnt(7) lgkmcnt(0)" ::: "memory"); BAR(); SCHED(); ST(14);
    half_step<KT, true, 3>(acc, f1, f0, smem + SLOT, fa, 0, [&](int a) { if (a < 3) load_gate(a); else stage_x(a - 3, (rot + 3) & 7, 0); }, [&](int i) { if (i == MPI) glds4(mp, lds0 + 3 * SLOT + 768); });
    half_step<KT, true, 6>(acc, f0, f1, smem + SLOT, fa, 1, [&](int a) { if (a < 3) stage_w(a, (rot + 3) & 7, 0); });
    wait_gate(); BAR(); SCHED(); ST(15); CY(1);
    half_step<KT, true, 4>(acc, f1, f0, smem + 2 * SLOT, fa, 0, [&](int a) { if (a < 4) stage_x(a, (rot + 4) & 7, SLOT); else touch(0); }, gate_piece);
    unsigned sp = SLOT, so = 2 * SLOT, sn = 0;
#pragma unroll 1
    for (int t = 2; t < 5; ++t) {
      half_step<KT, true, 6>(acc, f0, f1, smem + so, fa, 1, [&](int a) { if (a < 3) stage_w(a, (rot + t + 2) & 7, sp); });
      asm volatile("s_waitcnt vmcnt(8) lgkmcnt(0)" ::: "memory"); BAR(); SCHED(); ST(14 + t); CY(t);
      half_step<KT, true, 4>(acc, f1, f0, smem + sn, fa, 0, [&](int a) { if (a < 4) stage_x(a, (rot + t + 3) & 7, so); else touch(t - 1); });
      const unsigned tmp = sp; sp = so; so = sn; sn = tmp;
    }
    half_step<KT, true, 6>(acc, f0, f1, smem + 2 * SLOT, fa, 1, [&](int a) { if (a < 3) stage_w(a, (rot + 7) & 7, SLOT); });
    asm volatile("s_waitcnt vmcnt(8) lgkmcnt(0)" ::: "memory"); BAR(); SCHED(); ST(19);
    half_step<KT, true, 5>(acc, f1, f0, smem, fa, 0, [&](int a) { if (a < 3) stage2(a); else touch2(); });
    half_step<KT, true, PR(5)>(acc, f0, f1, smem, fa, 1, [&](int a) {
      if (a < 3) stage2(3 + a);
      if (!KT && a == 3) load_gg(); });
    if (KT) asm volatile("s_waitcnt vmcnt(7) lgkmcnt(0)" ::: "memory"); else asm volatile("s_waitcnt vmcnt(8) lgkmcnt(0)" ::: "memory");
    BAR(); SCHED(); ST(20);
    half_step<KT, true, PR(6)>(acc, f1, f0, smem + SLOT, fa, 0, [&](int a) { if (a < 3) stage2(6 + a); });
    half_step<KT, true, PR(6)>(acc, f0, f1, smem + SLOT, fa, 1, [&](int a) { if (a < 3) stage2(9 + a); });
    WAIT_LGKM0(); BAR(); SCHED(); ST(21);
    half_step<KT, false, PR(3)>(acc, f1, f0, smem, fa, 0, [&](int a) { if (a < 6) stage2(12 + a); });
  };
  LAS char* wreg = smem + wave * PIECE;
  v4u W[5][4];
  int tro[4];
#pragma unroll
  for (int nt = 0; nt < 4; ++nt) tro[nt] = lg * 1024 + (lc >> 2) * 128 + ((nt ^ (((lc >> 3) & 1) | ((lg & 1) << 1))) * 32) + 8 * (lane & 3);
  auto read_unit = [&](int u, v4u (&d)[4]) {
    const int k0 = (4 * u) % 18, k2 = (4 * u + 2) % 18;
    const int delta = chunk_off(k2) - chunk_off(k0) - 2048;
    const int dl = delta ? (lg >= 2 ? delta : 0) : 0;
#pragma unroll
    for (int nt = 0; nt < 4; ++nt) {
      LAS char* q = wreg + chunk_off(k0) + tro[nt] + dl;
      d[nt] = cat(tr_read(q), tr_read(q + 512));
    }
  };
  const int hbase = OFF_H + (tb1 * 8 + e) * 36864 + lane * 16;
  auto stage_rng = [&](int a, int b) {
#pragma unroll
    for (int n = a; n < b; ++n) stage2(n);
  };
  auto head = [&]() {
    read_unit(0, W[0]); read_unit(1, W[1]); read_unit(2, W[2]);
    WAIT_LGKM0(); SCHED();
    if (EARLY_OLD || wn) stage_rng(18, 30);
    if (wn) PRIO(PYOUNG); else PRIO(POLD);
    SCHED();
  };
  auto tail = [&]() {
    PRIO(0);
    if (EARLY_OLD || wn) {
      read_unit(3, W[3]); read_unit(4, W[4]);
      WAIT_LGKM0(); SCHED();
      stage_rng(30, 36);
    } else {
      read_unit(3, W[3]);
      stage_rng(18, 30);
      WAIT_LGKM0(); SCHED();
      stage_rng(30, 34);
      WAIT_VM(14);
      read_unit(4, W[4]);
      WAIT_LGKM0(); SCHED();
      stage_rng(34, 36);
    }
  };
  if (ty == 0) {
    gemm1(std::true_type{});
    ST(2);
    WAIT_VM(6);
    head();
    const v4u ones = {0x3f803f80u, 0x3f803f80u, 0x3f803f80u, 0x3f803f80u};
    f32x4 okv = {0.f, 0.f, 0.f, 0.f}, oz = {0.f, 0.f, 0.f, 0.f};
    auto kv_slot = [&](int s) { return smem + (s % 3) * SLOT + ((s / 3) * 2 + (lane >> 5)) * PIECE + 6144 + (lane & 31) * 16; };
#pragma unroll
    for (int i2 = 0; i2 < 2; ++i2) {
      f32x4 kv = {0.f, 0.f, 0.f, 0.f}, z = {0.f, 0.f, 0.f, 0.f};
#pragma unroll
      for (int u = 0; u < 2; ++u) {
        f32x4 v0 = acc[2 * u][2 * i2 + 1], v1 = acc[2 * u + 1][2 * i2 + 1];
        v4u pa = phi8(acc[2 * u][2 * i2], acc[2 * u + 1][2 * i2]);
        v4u vb = {pk2(v0[0], v0[1]), pk2(v0[2], v0[3]), pk2(v1[0], v1[1]), pk2(v1[2], v1[3])};
        kv = mma32(pa, vb, kv);
        z = mma32(pa, ones, z);
      }
      if (wm == i2) { okv = kv; oz = z; }
      else {
        const int s = (wn * 2 + i2) * 3 + (wm > i2 ? wm - 1 : wm);
        *(LAS f32x4*)kv_slot(s) = kv;
        if (lc == 0) *(LAS f32x4*)(smem + 3 * SLOT + s * 64 + lg * 16) = z;
      }
    }
    WAIT_LGKM0(); BAR(); SCHED();
    if (wm < 2) {
#pragma unroll
      for (int m = 0; m < 3; ++m) {
        const int s = (wn * 2 + wm) * 3 + m;
        okv += *(LAS f32x4*)kv_slot(s); oz += *(LAS f32x4*)(smem + 3 * SLOT + s * 64 + lg * 16);
      }
      int o = OFF_KVP + ((4 * e + 2 * wn + wm) * 8 + p) * KVP_STR;
      v4u a = {f2u(okv[0]), tag32, f2u(okv[1]), tag32};
      v4u b = {f2u(okv[2]), tag32, f2u(okv[3]), tag32};
      __builtin_amdgcn_raw_buffer_store_b128(a, wsr, o + lane * 16, 0, SC1);
      __builtin_amdgcn_raw_buffer_store_b128(b, wsr, o + 1024 + lane * 16, 0, SC1);
      if (lc < 2) {
        v4u c = {f2u(lc ? oz[2] : oz[0]), tag32, f2u(lc ? oz[3] : oz[1]), tag32};
        __builtin_amdgcn_raw_buffer_store_b128(c, wsr, o + 2048 + lg * 32 + lc * 16, 0, SC1);
      }
    }
#pragma unroll
    for (int u = 0; u < 2; ++u)
      __builtin_amdgcn_raw_buffer_store_b128(phi8(acc[2 * u][4], acc[2 * u + 1][4]), wsr, hbase + (28 + wn * 2 + u) * 1024, 0, SC1);
    ST(3);
    WAIT_VM(0);
    if (lane == 0) __hip_atomic_store((GAS u64*)(ws + OFF_HF + (tb1 * 8 + e) * 128) + wn, tag, RLX_AGENT);
    ST(4);
    tail();
    if (!(p & 1) && wave == REDW) {
      PRIO(3);
      const int h = 4 * e + (p >> 1);
      const int o = OFF_KVP + h * 8 * KVP_STR;
      f32x4 kv; f32x2 zs;
      for (unsigned spins = 0;; ++spins) {
        v4u a[8], b[8], zz[8];
#pragma unroll
        for (int q = 0; q < 8; ++q) {
          a[q] = __builtin_amdgcn_raw_buffer_load_b128(wsr, o + q * KVP_STR + lane * 16, 0, SC1);
          b[q] = __builtin_amdgcn_raw_buffer_load_b128(wsr, o + q * KVP_STR + 1024 + lane * 16, 0, SC1);
          zz[q] = __builtin_amdgcn_raw_buffer_load_b128(wsr, o + q * KVP_STR + 2048 + lg * 32 + (lc & 2) * 8, 0, SC1);
        }
        SCHED();
        bool ok = true; kv = (f32x4){0.f, 0.f, 0.f, 0.f}; zs = (f32x2){0.f, 0.f};
#pragma unroll
        for (int q = 0; q < 8; ++q) {
          ok &= (a[q][1] == tag32) & (a[q][3] == tag32) & (b[q][1] == tag32) & (b[q][3] == tag32) & (zz[q][1] == tag32) & (zz[q][3] == tag32);
          kv += (f32x4){u2f(a[q][0]), u2f(a[q][2]), u2f(b[q][0]), u2f(b[q][2])};
          zs += (f32x2){u2f(zz[q][0]), u2f(zz[q][2])};
        }
        if (__all(ok) || spins > SPIN_CAP) break;
        __builtin_amdgcn_s_sleep(1);
      }
      v4u f = {pk2(kv[0], kv[1]), pk2(kv[2], kv[3]), pk2(zs[0], zs[1]), tag32};
      __builtin_amdgcn_raw_buffer_store_b128(f, wsr, OFF_KVF + h * 1024 + lane * 16, 0, SC1);
      ST(26);
    }
  } else {
    gemm1(std::false_type{});
    ST(2);
    asm volatile("s_waitcnt vmcnt(6)" : "+v"(gg)::"memory");
    if (!__all(gg[1] == tag32)) {
      for (unsigned spins = 0;; ++spins) {
        u64 v = __hip_atomic_load((const GAS u64*)(ws + OFF_G) + tb1 * 64 + lane, RLX_AGENT);
        gg = (v2u){(unsigned)v, (unsigned)(v >> 32)};
        if (__all(gg[1] == tag32) || spins > SPIN_CAP) break;
        __builtin_amdgcn_s_sleep(1);
      }
    }
    const unsigned iol = f2u(__builtin_fminf(1.f + fexp(u2f(gg[0])), 1e30f));
    float io[4];
#pragma unroll
    for (int tt = 0; tt < 4; ++tt) io[tt] = u2f(__builtin_amdgcn_ds_bpermute((tt * 16 + lc) * 4, iol));
    head();
    const int slot = 2 * (ty - 1) + wn;
#pragma unroll
    for (int tt = 0; tt < 4; ++tt) {
      v4u hv = {swiglu2(lo2(acc[tt][0]), lo2(acc[tt][1]), io[tt]), swiglu2(hi2(acc[tt][0]), hi2(acc[tt][1]), io[tt]),
                swiglu2(lo2(acc[tt][2]), lo2(acc[tt][3]), io[tt]), swiglu2(hi2(acc[tt][2]), hi2(acc[tt][3]), io[tt])};
      __builtin_amdgcn_raw_buffer_store_b128(hv, wsr, hbase + (slot * 4 + tt) * 1024, 0, SC1);
    }
    if (ty >= 2) {
      v4u hv = {ffn_mixed(acc[0][4], io[0]), ffn_mixed(acc[1][4], io[1]), ffn_mixed(acc[2][4], io[2]), ffn_mixed(acc[3][4], io[3])};
      __builtin_amdgcn_raw_buffer_store_b128(hv, wsr, hbase + (24 + (ty - 2) * 2 + wn) * 1024, 0, SC1);
    } else {
#pragma unroll
      for (int u = 0; u < 2; ++u)
        __builtin_amdgcn_raw_buffer_store_b128(phi8(acc[2 * u][4], acc[2 * u + 1][4]), wsr, hbase + (28 + (2 + wn) * 2 + u) * 1024, 0, SC1);
    }
    SCHED();
    ST(3);
    WAIT_VM(0);
    if (lane == 0) __hip_atomic_store((GAS u64*)(ws + OFF_HF + (tb1 * 8 + e) * 128) + ty * 2 + wn, tag, RLX_AGENT);
    ST(4);
    SCHED();
    tail();
    if (bid == 8 * 4 && wave == 0) {
      for (unsigned spins = 0;; ++spins) {
        bool ok = true;
        v4u a[16];
#pragma unroll
        for (int q = 0; q < 16; ++q) a[q] = __builtin_amdgcn_raw_buffer_load_b128(wsr, OFF_G + q * 1024 + lane * 16, 0, SC1);
        SCHED();
#pragma unroll
        for (int q = 0; q < 16; ++q) ok &= (a[q][1] == tag32) & (a[q][3] == tag32);
        if (__all(ok) || spins > SPIN_CAP) break;
        __builtin_amdgcn_s_sleep(1);
      }
      if (lane == 0) __hip_atomic_store((GAS u64*)ws, tag, RLX_AGENT);
    }
  }
  ST(5);
  if (wn) PRIO(P2Y); else PRIO(P2O);
  const GAS u64* fl = (const GAS u64*)(ws + OFF_HF + (tb2 * 8 + e2) * 128) + (lane & 7);
  for (unsigned spins = 0;; ++spins) {
    u64 v = __hip_atomic_load(fl, RLX_AGENT);
    if (__all(v == tag || (lane & 1) || (lane & 7) == 0) || spins > SPIN_CAP) break;
    __builtin_amdgcn_s_sleep(1);
  }
  WAIT_VM(0);
  ST(6);
  f32x4 c2[4][4];
#pragma unroll
  for (int tt = 0; tt < 4; ++tt)
#pragma unroll
    for (int nt = 0; nt < 4; ++nt) c2[tt][nt] = (f32x4){0.f, 0.f, 0.f, 0.f};
  const int hb2 = OFF_H + (tb2 * 8 + e2) * 36864 + lane * 16;
  const int hs0 = par * 16384, hsd = sgn * 8192;
  v4u hq[9][4];
  auto ldq = [&](int qd) {
#pragma unroll
    for (int i = 0; i < 4; ++i)
      hq[qd][i] = qd < 6 ? __builtin_amdgcn_raw_buffer_load_b128(wsr, hb2 + i * 1024, hs0 + (qd % 3) * hsd + (qd < 3 ? 0 : 4096), SC1)
                         : __builtin_amdgcn_raw_buffer_load_b128(wsr, hb2 + (qd * 4 + i) * 1024, 0, SC1);
  };
  auto mmq = [&](const v4u (&w)[4], const v4u (&hf)[4]) {
#pragma unroll
    for (int tt = 0; tt < 4; ++tt)
#pragma unroll
      for (int nt = 0; nt < 4; ++nt) c2[tt][nt] = mma32(w[nt], hf[tt], c2[tt][nt]);
  };
  v4u kvf[4]; v2u g2;
  auto ldkv = [&]() {
#pragma unroll
    for (int i = 0; i < 4; ++i) kvf[i] = __builtin_amdgcn_raw_buffer_load_b128(wsr, OFF_KVF + (4 * e2 + i) * 1024 + lane * 16, 0, SC1);
    g2 = __builtin_amdgcn_raw_buffer_load_b64(wsr, OFF_G + (tb2 * 64 + lane) * 8, 0, SC1);
  };
  auto kvok = [&]() { return __all((kvf[0][3] == tag32) & (kvf[1][3] == tag32) & (kvf[2][3] == tag32) & (kvf[3][3] == tag32) & (g2[1] == tag32)); };
  auto mmf = [&](const v4u (&w)[4], const v4u (&hf)[4], auto fill) {
#pragma unroll
    for (int i = 0; i < 16; ++i) { c2[i >> 2][i & 3] = mma32(w[i & 3], hf[i >> 2], c2[i >> 2][i & 3]); fill(i); SCHED(); }
  };
  ldq(0);
  u64 kf = __hip_atomic_load(fl, RLX_AGENT);
  ldq(1); ldq(2); SCHED();
  mmq(W[0], hq[0]); SCHED(); ST(28);
  for (unsigned spins = 0; !__all(kf == tag) && spins <= SPIN_CAP; ++spins) {
    __builtin_amdgcn_s_sleep(1);
    kf = __hip_atomic_load(fl, RLX_AGENT);
  }
  ST(7);
  ldq(3); ldq(7); ldq(8); SCHED();
  ST(29); mmq(W[1], hq[1]); SCHED(); ST(30); ldkv(); ldq(4); SCHED();
  mmq(W[2], hq[2]); SCHED(); ST(31); ldq(5); SCHED();
  mmq(W[3], hq[3]); SCHED(); ldq(6); SCHED();
  ST(8);
  const bool early = kvok();
  if (!early) {
    for (unsigned spins = 0;; ++spins) {
      ldkv();
      if (kvok() || spins > SPIN_CAP) break;
      __builtin_amdgcn_s_sleep(1);
    }
  }
  ST(9);
  {
    const unsigned igl = f2u(1.f + fexp(-u2f(g2[0])));
    f32x2 ig[2];
#pragma unroll
    for (int tt = 0; tt < 4; ++tt) ig[tt >> 1][tt & 1] = u2f(__builtin_amdgcn_ds_bpermute((tt * 16 + lc) * 4, igl));
    const f32x4 zero = {0.f, 0.f, 0.f, 0.f}, eps = {1e-6f, 1e-6f, 0.f, 0.f};
    f32x4 den[4], n0[4], n1[4]; f32x2 rr[4];
    v4u of[2][4];
    auto small = [&](int so) {
#pragma unroll
      for (int hh = 0; hh < 2; ++hh) {
        const int i = 2 * so + hh;
        const unsigned zv = kvf[i][2];
        const v4u zf = {(lc & 3) == 0 ? zv : 0u, (lc & 3) == 2 ? zv : 0u, (lc & 3) == 1 ? zv : 0u, (lc & 3) == 3 ? zv : 0u};
#pragma unroll
        for (int u = 0; u < 2; ++u) den[hh * 2 + u] = mma32(zf, hq[7 + so][hh * 2 + u], eps);
      }
#pragma unroll
      for (int c = 0; c < 4; ++c) {
        const v2u kv = {kvf[2 * so + (c >> 1)][0], kvf[2 * so + (c >> 1)][1]};
        const v4u qq = hq[7 + so][c];
        n0[c] = mma16(kv, (v2u){qq[0], qq[1]}, zero); n1[c] = mma16(kv, (v2u){qq[2], qq[3]}, zero);
      }
      SCHED();
    };
    auto piece = [&](int so, int i) {
      const int c = i & 3, hh = c >> 1, u = c & 1;
      if (i < 4) { const f32x2 dd = (lo2(den[c]) + hi2(den[c])) * ig[u]; rr[c] = (f32x2){frcp(dd[0]), frcp(dd[1])}; }
      else if (i < 8) {
        of[so][2 * u][hh * 2] = __builtin_bit_cast(unsigned, __builtin_convertvector(lo2(n0[c]) * rr[c][0], bf16x2));
        of[so][2 * u][hh * 2 + 1] = __builtin_bit_cast(unsigned, __builtin_convertvector(hi2(n0[c]) * rr[c][0], bf16x2));
      } else if (i < 12) {
        of[so][2 * u + 1][hh * 2] = __builtin_bit_cast(unsigned, __builtin_convertvector(lo2(n1[c]) * rr[c][1], bf16x2));
        of[so][2 * u + 1][hh * 2 + 1] = __builtin_bit_cast(unsigned, __builtin_convertvector(hi2(n1[c]) * rr[c][1], bf16x2));
      }
    };
    small(0); ST(32);
    mmf(W[4], hq[4], [&](int i) { piece(0, i); });
    ST(33); read_unit(5, W[0]); read_unit(6, W[1]); SCHED();
    small(1); ST(34);
    mmf(W[0], of[0], [&](int i) { piece(1, i); });
    ST(35); read_unit(7, W[2]); SCHED();
    mmq(W[1], of[1]); SCHED();
    read_unit(8, W[3]); SCHED();
  }
  ST(25);
  mmq(W[2], hq[5]); SCHED(); ST(36);
  {
    v4u hf[4];
#pragma unroll
    for (int tt = 0; tt < 4; ++tt) hf[tt] = (v4u){hq[6][0][tt], hq[6][1][tt], hq[6][2][tt], hq[6][3][tt]};
    mmq(W[3], hf);
  }
  ST(10);
#pragma unroll
  for (int tt = 0; tt < 4; ++tt)
#pragma unroll
    for (int nt = 0; nt < 4; ++nt)
      *(LAS f32x4*)(wreg + chunk_off(tt * 4 + nt) + lg * 256 + ((lc ^ (4 * nt + 2 * (lg >> 1))) & 15) * 16) = c2[tt][nt];
  WAIT_LGKM0(); BAR(); SCHED();
  ST(11);
  {
    const int o = lane & 7, c = (wave & 1) * 8 + (lane >> 3), tt = wave >> 1;
    const int k = tt * 4 + (o >> 1);
    const int ro = (k < 6 ? 2 * SLOT + k * 1024 : (k < 12 ? (k - 6) * 1024 : SLOT + (k - 12) * 1024)) + (o & 1) * 512 + ((c ^ (2 * o)) & 15) * 16;
    f32x4 a = {0.f, 0.f, 0.f, 0.f}, b = {0.f, 0.f, 0.f, 0.f};
#pragma unroll
    for (int w = 0; w < 8; ++w) { a += *(LAS f32x4*)(smem + w * PIECE + ro); b += *(LAS f32x4*)(smem + w * PIECE + ro + 256); }
    v4u r = {pk2(a[0], a[1]), pk2(a[2], a[3]), pk2(b[0], b[1]), pk2(b[2], b[3])};
    *(GAS v4u*)(out + (size_t)(tb2 * 64 + tt * 16 + c) * 1024 + nb * 128 + o * 16) = r;
  }
}
}

extern "C" void kernel_launch(void* const* d_in, const int* in_sizes, int n_in, void* d_out, int out_size, void* d_ws, size_t ws_size,
                              hipStream_t stream) {
  fk::fused<<<256, 512, 0, stream>>>((const char*)d_in[0], (const char*)d_in[1], (const char*)d_in[2], (const char*)d_in[4],
                                     (const char*)d_in[5], (const char*)d_in[6], (const char*)d_in[7], (char*)d_out, (char*)d_ws);
}
```

```cpp
#define STAMP
#define DB 1
#include <hip/hip_runtime.h>
#include <hip/hip_bf16.h>
#include <stdint.h>
#include <type_traits>

#define GAS __attribute__((address_space(1)))
#define LAS __attribute__((address_space(3)))
typedef unsigned v4u __attribute__((ext_vector_type(4)));
typedef unsigned v2u __attribute__((ext_vector_type(2)));
typedef float f32x4 __attribute__((ext_vector_type(4)));
typedef float f32x2 __attribute__((ext_vector_type(2)));
typedef short s16x4 __attribute__((ext_vector_type(4)));
typedef __bf16 bf16x8 __attribute__((ext_vector_type(8)));
typedef __bf16 bf16x2 __attribute__((ext_vector_type(2)));
typedef unsigned long long u64;
#define RLX_AGENT __ATOMIC_RELAXED, __HIP_MEMORY_SCOPE_AGENT

namespace fk {
constexpr int XB = 32768, WB = 20480, SLOT = XB + WB;
constexpr int LDS_BYTES = 3 * SLOT + 1024;
constexpr int PIECE = SLOT / 8;
constexpr int OFF_G = 4096;
constexpr int OFF_HF = 32768;
constexpr int OFF_KVF = 69632;
constexpr int OFF_KVP = 131072;
constexpr int KVP_STR = 2304;
constexpr int OFF_H = 1 << 20;
constexpr int WS_USED = OFF_H + 256 * 36864;
constexpr int SC1 = 16;
constexpr unsigned SPIN_CAP = 1u << 15;

#define NSTAMP 38
#define STAMP_OFF (64u << 20)
#define CLK() __builtin_amdgcn_s_memrealtime()
#define REL 0
#define CY(t) do {} while (0)
#define ST(k) do { if ((k) == 0 || (k) == DB) { __builtin_amdgcn_sched_barrier(0); st[k] = (unsigned)CLK(); __builtin_amdgcn_sched_barrier(0); } } while (0)

__device__ __forceinline__ unsigned f2u(float f) { return __builtin_bit_cast(unsigned, f); }
__device__ __forceinline__ float u2f(unsigned u) { return __builtin_bit_cast(float, u); }
__device__ __forceinline__ unsigned pk2(float a, float b) {
  return __builtin_bit_cast(unsigned, __builtin_convertvector((f32x2){a, b}, bf16x2));
}
__device__ __forceinline__ f32x4 mma32(v4u a, v4u b, f32x4 c) {
  return __builtin_amdgcn_mfma_f32_16x16x32_bf16(__builtin_bit_cast(bf16x8, a), __builtin_bit_cast(bf16x8, b), c, 0, 0, 0);
}
__device__ __forceinline__ f32x4 mma16(v2u a, v2u b, f32x4 c) {
  return __builtin_amdgcn_mfma_f32_16x16x16bf16_1k(__builtin_bit_cast(s16x4, a), __builtin_bit_cast(s16x4, b), c, 0, 0, 0);
}
__device__ __forceinline__ v2u tr_read(LAS char* p) {
  return __builtin_bit_cast(v2u, __builtin_amdgcn_ds_read_tr16_b64_v4i16((LAS s16x4*)p));
}
__device__ __forceinline__ v4u cat(v2u a, v2u b) { return (v4u){a[0], a[1], b[0], b[1]}; }
__device__ __forceinline__ void glds16(const void* g, unsigned l) {
  asm volatile("s_mov_b32 m0, %1\n\ts_nop 0\n\tglobal_load_lds_dwordx4 %0, off" ::"v"(g), "s"(l) : "memory");
}
__device__ __forceinline__ void glds16s(unsigned voff, const void* sbase, unsigned l) {
  asm volatile("s_mov_b32 m0, %2\n\ts_nop 0\n\tglobal_load_lds_dwordx4 %0, %1" ::"v"(voff), "s"(sbase), "s"(l) : "memory");
}
__device__ __forceinline__ void glds16_lo(const void* g, unsigned l) {
  asm volatile("s_mov_b32 m0, %1\n\ts_mov_b32 exec_hi, 0\n\tglobal_load_lds_dwordx4 %0, off\n\ts_mov_b32 exec_hi, -1" ::"v"(g), "s"(l) : "memory");
}
__device__ __forceinline__ void ld8_sc1(v2u& d, unsigned voff, const void* sbase) {
  asm volatile("global_load_dwordx2 %0, %1, %2 sc1" : "=v"(d) : "v"(voff), "s"(sbase) : "memory");
}
__device__ __forceinline__ void touch4(unsigned& d, const void* g) {
  asm volatile("global_load_dword %0, %1, off" : "+v"(d) : "v"(g) : "memory");
}
__device__ __forceinline__ void glds4s(unsigned voff, const void* sbase, unsigned l) {
  asm volatile("s_mov_b32 m0, %2\n\ts_nop 0\n\tglobal_load_lds_dword %0, %1" ::"v"(voff), "s"(sbase), "s"(l) : "memory");
}
__device__ __forceinline__ void keep(unsigned& d) { asm volatile("" : "+v"(d)); }
__device__ __forceinline__ void st8_last(v2u d, const void* sbase) {
  asm volatile("s_mov_b32 exec_lo, 0\n\ts_brev_b32 exec_hi, 1\n\tglobal_store_dwordx2 %0, %1, %2 sc1\n\ts_mov_b64 exec, -1" ::"v"(0), "v"(d), "s"(sbase) : "memory");
}
__device__ __forceinline__ void ld16(v4u& d, unsigned voff, const void* sbase) {
  asm volatile("global_load_dwordx4 %0, %1, %2" : "=v"(d) : "v"(voff), "s"(sbase) : "memory");
}
__device__ __forceinline__ float fexp(float x) { return __builtin_amdgcn_exp2f(x * 1.44269504088896f); }
__device__ __forceinline__ float frcp(float x) { return __builtin_amdgcn_rcpf(x); }
__device__ __forceinline__ float bflo(unsigned u) { return u2f(u << 16); }
__device__ __forceinline__ float bfhi(unsigned u) { return u2f(u & 0xffff0000u); }
#define WAIT_VM(n) asm volatile("s_waitcnt vmcnt(" #n ")" ::: "memory")
#define WAIT_LGKM0() asm volatile("s_waitcnt lgkmcnt(0)" ::: "memory")
#define BAR() __builtin_amdgcn_s_barrier()
#define SCHED() __builtin_amdgcn_sched_barrier(0)
#define PRIO(n) __builtin_amdgcn_s_setprio(n)
#define PYOUNG 2
#define POLD 3
#define EARLY_OLD 1
#define P2Y 2
#define P2O 1
#define REDW 2

struct Frags { v4u x[4]; v2u wl[5], wh[5]; };
struct FragAddr { int xo, w0, w4; };
__device__ __forceinline__ void frag_read(Frags& f, int i, LAS char* sp, const FragAddr& a, int ks) {
  if (i >= 2 && i < 6) { f.x[i - 2] = *(LAS v4u*)(sp + (a.xo ^ (ks * 64)) + (i - 2) * 2048); return; }
  const int r = i < 2 ? i : i - 4, G = r >> 1;
  LAS char* p = G < 4 ? sp + (a.w0 ^ ((G >> 1) * 32)) + (G & 1) * 1024 + ks * 10240 + (r & 1) * 512 : sp + a.w4 + ks * 10240 + (r & 1) * 256;
  if (r & 1) f.wh[G] = tr_read(p); else f.wl[G] = tr_read(p);
}
struct NoFill { __device__ __forceinline__ void operator()(int) const {} };
template <bool KT, bool RD, int PER, class Act, class Fill = NoFill>
__device__ __forceinline__ void half_step(f32x4 (&acc)[4][5], const Frags& c, Frags& n, LAS char* sp, const FragAddr& a, int ks, Act act, Fill fill = Fill()) {
#pragma unroll
  for (int i = 0; i < 20; ++i) {
    const int G = i >> 2, tt = i & 3;
    const v4u w = cat(c.wl[G], c.wh[G]);
    acc[tt][G] = (KT && G < 4) ? mma32(c.x[tt], w, acc[tt][G]) : mma32(w, c.x[tt], acc[tt][G]);
    if (RD && i < 14) frag_read(n, i, sp, a, ks);
    if (i % PER == 1) act(i / PER);
    fill(i);
    SCHED();
  }
}
__device__ __forceinline__ unsigned swiglu2(f32x2 g, f32x2 u, float io) {
  const f32x2 t = g * -1.44269504088896f;
  const f32x2 e = {__builtin_amdgcn_exp2f(t[0]), __builtin_amdgcn_exp2f(t[1])};
  const f32x2 i2 = {io, io};
  const f32x2 d = __builtin_elementwise_fma(e, i2, i2);
  const f32x2 r = {frcp(d[0]), frcp(d[1])};
  return __builtin_bit_cast(unsigned, __builtin_convertvector((g * u) * r, bf16x2));
}
__device__ __forceinline__ f32x2 lo2(f32x4 a) { return (f32x2){a[0], a[1]}; }
__device__ __forceinline__ f32x2 hi2(f32x4 a) { return (f32x2){a[2], a[3]}; }
__device__ __forceinline__ unsigned ffn_mixed(f32x4 a, float io) {
  auto s0 = __builtin_amdgcn_permlane32_swap(f2u(a[0]), f2u(a[2]), false, false);
  auto s1 = __builtin_amdgcn_permlane32_swap(f2u(a[1]), f2u(a[3]), false, false);
  return swiglu2((f32x2){u2f(s0[0]), u2f(s1[0])}, (f32x2){u2f(s0[1]), u2f(s1[1])}, io);
}
__device__ __forceinline__ unsigned phi2(f32x2 t) {
  const f32x2 s = t * 1.44269504088896f;
  const f32x2 e = {__builtin_amdgcn_fmed3f(__builtin_amdgcn_exp2f(s[0]), 0.f, 1.f), __builtin_amdgcn_fmed3f(__builtin_amdgcn_exp2f(s[1]), 0.f, 1.f)};
  const f32x2 m = {__builtin_fmaxf(t[0], 0.f), __builtin_fmaxf(t[1], 0.f)};
  return __builtin_bit_cast(unsigned, __builtin_convertvector((m + e) + 1e-6f, bf16x2));
}
__device__ __forceinline__ v4u phi8(f32x4 a, f32x4 b) { return (v4u){phi2(lo2(a)), phi2(hi2(a)), phi2(lo2(b)), phi2(hi2(b))}; }
template <int CTRL, int RM> __device__ __forceinline__ float dpp_add(float v) {
  return v + u2f(__builtin_amdgcn_update_dpp(0u, f2u(v), CTRL, RM, 0xf, false));
}
__device__ __forceinline__ constexpr int chunk_off(int k) { return (k < 6 ? 2 * SLOT : (k < 12 ? 0 : SLOT)) + (k % 6) * 1024; }

#define SPREAD 1
#define PR(n) (SPREAD ? (n) : 3)
#define ROTMODE 0
typedef unsigned v16u __attribute__((ext_vector_type(16)));
__device__ __forceinline__ char* mkptr(unsigned lo, unsigned hi) { u64 a = ((u64)hi << 32) | lo; asm volatile("" : "+s"(a)); return (char*)a; }
__global__ __launch_bounds__(512, 2) void fused(const char* x_, const char* wqkv_, const char* wo_, const char* w1_, const char* w3_,
                                                const char* w2_, const char* gw_, char* out_, char* ws_) {
  v16u ka; v2u kb;
  asm volatile("s_load_dwordx16 %0, %2, 0x0\n\ts_load_dwordx2 %1, %2, 0x40" : "=&s"(ka), "=&s"(kb) : "s"(__builtin_amdgcn_kernarg_segment_ptr()));
  __shared__ __attribute__((aligned(16))) char smem_[LDS_BYTES];
  LAS char* smem = (LAS char*)smem_;
  const unsigned lds0 = (unsigned)(uintptr_t)smem;
  unsigned st[NSTAMP];
#pragma unroll
  for (int k = 0; k < NSTAMP; ++k) st[k] = 0;
  ST(0);
  int lane = threadIdx.x & 63, wave = __builtin_amdgcn_readfirstlane(threadIdx.x >> 6), bid = blockIdx.x;
  int p = (bid & 4) + ((bid >> 3) & 3);
  const int rot = ROTMODE == 0 ? 0 : ROTMODE == 1 ? (2 * bid + (bid & 4)) & 7 : ROTMODE == 2 ? ((bid >> 5) + 2 * (bid >> 3)) & 7 : (((bid >> 5) ^ (bid >> 3)) & 1) * 4;
  const unsigned xo0 = (lane >> 3) * 1024 + 16 * ((lane & 7) ^ (lane >> 4)), xo1 = (xo0 ^ 64) + 8192;
  unsigned oX = (p * 8 + wave) * 32768;
  asm volatile("s_waitcnt lgkmcnt(0)" : "+s"(ka), "+s"(kb), "+s"(oX));
  const char* x = mkptr(ka[0], ka[1]);
  ST(24);
  const char* xw = x + oX;
  auto stage_x = [&](int i, int kt, unsigned slot) {
    glds16s((i & 1) ? xo1 : xo0, xw + kt * 128 + (i >> 1) * 16384, lds0 + slot + wave * 4096 + i * 1024);
  };
#pragma unroll
  for (int i = 0; i < 4; ++i) stage_x(i, rot, 0);
  asm volatile("" : "+s"(wave), "+s"(bid));
  asm volatile("" : "+v"(lane));
  const char *wqkv = mkptr(ka[2], ka[3]), *w1 = mkptr(ka[6], ka[7]), *w3 = mkptr(ka[8], ka[9]);
  const int e = (bid & 3) * 2 + (bid >> 7), ty = (bid >> 5) & 3;
  const unsigned fo = 896 * (bid & 3);
  const unsigned sAB = ty == 0 ? 3072 : 3584, sC = ty < 2 ? 3072 : 3584;
  const unsigned oF = fo + 384 * (bid >> 7) + 128 * ty - 128, oK = 1024 + 128 * e;
  const unsigned oA = (ty == 0 ? oK : oF) + wave * 8 * sAB, oB = oA + (ty == 0 ? 1024 : 0);
  const unsigned voAB = (lane >> 3) * sAB + ((((lane >> 1) & 3) ^ (((lane >> 4) & 1) | ((wave & 1) << 1))) * 32) + (lane & 1) * 16;
  const char* bA = (ty == 0 ? wqkv : w1) + oA;
  const char* bB = (ty == 0 ? wqkv : w3) + oB;
  auto stage_ab = [&](int i, int kt, unsigned slot) {
    glds16s(voAB, (i ? bB : bA) + (unsigned)(kt * 64) * sAB, lds0 + slot + XB + wave * 2560 + i * 1024);
  };
  stage_ab(0, rot, 0); stage_ab(1, rot, 0);
  asm volatile("" : "+s"(wave));
  asm volatile("" : "+v"(lane));
  const unsigned oL = fo + 704 + 64 * (bid >> 7) + 32 * ty, oQ = 128 * e + 64 * ty;
  const char* bC0 = (ty < 2 ? wqkv : w1) + ((ty < 2 ? oQ : oL) + wave * 8 * sC);
  const char* bC1 = (ty < 2 ? wqkv : w3) + ((ty < 2 ? oQ + 16 : oL) + wave * 8 * sC);
  const char* pC = ((lane & 1) ? bC1 : bC0) + (unsigned)(((lane >> 2) & 7) * sC + ((((lane >> 1) ^ wave) & 1) << (ty < 2 ? 5 : 4)));
  auto stage_w = [&](int i, int kt, unsigned slot) {
    if (i < 2) stage_ab(i, kt, slot);
    else glds16_lo(pC + (unsigned)(kt * 64) * sC, lds0 + slot + XB + wave * 2560 + 2048);
  };
  stage_w(2, rot, 0);
  asm volatile("" : "+s"(wave), "+s"(xw), "+s"(bA), "+s"(bB));
  BAR();
#pragma unroll
  for (int i = 0; i < 4; ++i) stage_x(i, (rot + 1) & 7, SLOT);
#pragma unroll
  for (int i = 0; i < 3; ++i) stage_w(i, (rot + 1) & 7, SLOT);
  const char *gw = mkptr(ka[12], ka[13]), *wo = mkptr(ka[4], ka[5]), *w2 = mkptr(ka[10], ka[11]);
  char *ws = mkptr(kb[0], kb[1]), *out = mkptr(ka[14], ka[15]);
  asm volatile("" : "+s"(wave), "+s"(bid), "+s"(p));
  asm volatile("" : "+v"(lane));
  const int xg = bid & 7, idx = bid >> 3, lg = lane >> 4, lc = lane & 15;
  const int wm = wave & 3, wn = wave >> 2;
  const int gtok = p * 256 + (e * 4 + ty) * 8 + wave;
  const __amdgpu_buffer_rsrc_t wsr = __builtin_amdgcn_make_buffer_rsrc(ws, 0, WS_USED, 0x00020000);
  v4u xr, gr; v2u nn;
  auto load_gate = [&](int i) {
    if (i == 0) ld8_sc1(nn, 0, ws);
    if (i == 1) ld16(xr, lane * 16, x + gtok * 1024);
    if (i == 2) ld16(gr, lane * 16, gw);
  };
  const int tb2 = xg * 4 + (idx & 3), nb = idx >> 2, e2 = wave;
  const int jj = lane >> 3;
  const unsigned wbase2 = lds0 + wave * PIECE;
  unsigned w2o[2][2];
#pragma unroll
  for (int c = 0; c < 2; ++c) {
    unsigned col = ((((lane & 7) >> 1) ^ (((jj >> 1) & 1) | (c << 1))) * 32) + (lane & 1) * 16;
    w2o[0][c] = col + (16 * (jj >> 2) + (jj & 3)) * 1024;
    w2o[1][c] = col + (8 * (jj >> 1) + (jj & 1)) * 1024;
  }
  const int par = (idx ^ (idx >> 2)) & 1, sgn = 1 - 2 * par;
  const char* w2f = w2 + (size_t)(448 * (e2 >> 1) + 192 * (e2 & 1) + 128 * par) * 1024 + nb * 128;
  const char* w2l = w2 + (size_t)(448 * (e2 >> 1) + 384 + 32 * (e2 & 1)) * 1024 + nb * 128;
  const char* wob = wo + (size_t)(64 * e2) * 1024 + nb * 128;
  auto stage2 = [&](int n) {
    const int u = n >> 2, c = n & 3;
    const unsigned l = wbase2 + chunk_off(n % 18);
    if (u == 5 || u == 6) glds16s(w2o[0][c & 1], wob + (32 * (u - 5) + 4 * c) * 1024, l);
    else if (u < 8) glds16s(w2o[0][c & 1], w2f + (sgn * ((u < 5 ? u : 5) % 3) * 65536 + (u < 3 ? 0 : 32768) + 4096 * c), l);
    else glds16s(w2o[1][c & 1], w2l + (4 * (c & 1) + 2 * (c >> 1)) * 1024, l);
  };
  const int tb1 = p * 4 + wm;
  FragAddr fa;
  fa.xo = (wm * 64 + lc) * 128 + 16 * (lg ^ (lc >> 1));
  fa.w0 = XB + lg * 2560 + (lc >> 2) * 128 + (((2 * wn) ^ ((lc >> 3) | ((lg & 1) << 1))) * 32) + 8 * (lane & 3);
  fa.w4 = XB + lg * 2560 + 2048 + (lc >> 2) * 64 + ((wn ^ (lg & 1)) * 32) + 8 * (lane & 3);
  const int i16 = (idx & 3) + 4 * (ty & 1) + 8 * (bid >> 7);
  const char* pf = ty < 2 ? w2 + (size_t)(448 * (i16 >> 2) + ((i16 & 1) && wn ? 384 + 16 * (i16 & 2) : 96 * (i16 & 2) + 128 * (i16 & 1) + 64 * wn) + 8 * wm) * 1024
                          : wo + (size_t)(i16 * 8 + wave) * 4096;
  const unsigned pfo = (lane & (ty < 2 ? 15 : 7)) * 128, pfs = ty < 2 ? 2048 : 1024;
  const char* pf2 = w2 + (size_t)(448 * (idx >> 3) + 192 * ((idx >> 2) & 1) + 32 + (idx & 3) * 8 + wave) * 1024;
  const unsigned pfo2 = (lane & 7) * 128 + ((lane >> 3) % 3) * 65536;
  auto touch2 = [&]() { glds4s(pfo2, pf2, lds0 + 3 * SLOT + 768); };
  auto touch = [&](int s) { glds4s(pfo, pf + (unsigned)s * pfs, lds0 + 3 * SLOT + 768); };
  SCHED();
  WAIT_VM(7);
  ST(27);
  BAR();
  ST(1);

  f32x4 acc[4][5];
#pragma unroll
  for (int tt = 0; tt < 4; ++tt)
#pragma unroll
    for (int G = 0; G < 5; ++G) acc[tt][G] = (f32x4){0.f, 0.f, 0.f, 0.f};
  v2u gg = {0u, 0u};
  auto load_gg = [&]() { ld8_sc1(gg, lane * 8, ws + OFF_G + tb1 * 512); };
  u64 tag = 0; unsigned tag32 = 0; float gd = 0.f;
  auto wait_gate = [&]() {
    asm volatile("s_waitcnt vmcnt(7) lgkmcnt(0)" : "+v"(xr), "+v"(gr), "+v"(nn)::"memory");
    tag = (((u64)(unsigned)__builtin_amdgcn_readfirstlane(nn[1]) << 32) | (unsigned)__builtin_amdgcn_readfirstlane(nn[0])) + 0x9E3779B97F4A7C15ull;
    tag32 = (unsigned)tag;
  };
  auto gate_piece = [&](int i) {
    if (i < 8) gd += (i & 1) ? bfhi(xr[i >> 1]) * bfhi(gr[i >> 1]) : bflo(xr[i >> 1]) * bflo(gr[i >> 1]);
    if (i == 8) gd = dpp_add<0x111, 0xf>(gd);
    if (i == 9) gd = dpp_add<0x112, 0xf>(gd);
    if (i == 10) gd = dpp_add<0x114, 0xf>(gd);
    if (i == 11) gd = dpp_add<0x118, 0xf>(gd);
    if (i == 12) gd = dpp_add<0x142, 0xa>(gd);
    if (i == 13) gd = dpp_add<0x143, 0xc>(gd);
    if (i == 14) st8_last((v2u){f2u(gd), tag32}, ws + OFF_G + gtok * 8);
  };
  auto gemm1 = [&](auto ktc) {
    constexpr bool KT = decltype(ktc)::value;
    Frags f0, f1;
#pragma unroll
    for (int i = 0; i < 14; ++i) frag_read(f0, i, smem, fa, 0);
    SCHED();
    half_step<KT, true, PR(3)>(acc, f0, f1, smem, fa, 1, [&](int a) {
      if (a < 4) stage_x(a, (rot + 2) & 7, 2 * SLOT); else stage_w(a - 4, (rot + 2) & 7, 2 * SLOT); });
    asm volatile("s_waitcnt vmcnt(7) lgkmcnt(0)" ::: "memory"); BAR(); SCHED(); ST(14);
    half_step<KT, true, 3>(acc, f1, f0, smem + SLOT, fa, 0, [&](int a) { if (a < 3) load_gate(a); else stage_x(a - 3, (rot + 3) & 7, 0); });
    half_step<KT, true, 6>(acc, f0, f1, smem + SLOT, fa, 1, [&](int a) { if (a < 3) stage_w(a, (rot + 3) & 7, 0); });
    wait_gate(); BAR(); SCHED(); ST(15); CY(1);
    half_step<KT, true, 4>(acc, f1, f0, smem + 2 * SLOT, fa, 0, [&](int a) { if (a < 4) stage_x(a, (rot + 4) & 7, SLOT); else touch(0); }, gate_piece);
    unsigned sp = SLOT, so = 2 * SLOT, sn = 0;
#pragma unroll 1
    for (int t = 2; t < 5; ++t) {
      half_step<KT, true, 6>(acc, f0, f1, smem + so, fa, 1, [&](int a) { if (a < 3) stage_w(a, (rot + t + 2) & 7, sp); });
      asm volatile("s_waitcnt vmcnt(8) lgkmcnt(0)" ::: "memory"); BAR(); SCHED(); ST(14 + t); CY(t);
      half_step<KT, true, 4>(acc, f1, f0, smem + sn, fa, 0, [&](int a) { if (a < 4) stage_x(a, (rot + t + 3) & 7, so); else touch(t - 1); });
      const unsigned tmp = sp; sp = so; so = sn; sn = tmp;
    }
    half_step<KT, true, 6>(acc, f0, f1, smem + 2 * SLOT, fa, 1, [&](int a) { if (a < 3) stage_w(a, (rot + 7) & 7, SLOT); });
    asm volatile("s_waitcnt vmcnt(8) lgkmcnt(0)" ::: "memory"); BAR(); SCHED(); ST(19);
    half_step<KT, true, 5>(acc, f1, f0, smem, fa, 0, [&](int a) { if (a < 3) stage2(a); else touch2(); });
    half_step<KT, true, PR(5)>(acc, f0, f1, smem, fa, 1, [&](int a) {
      if (a < 3) stage2(3 + a);
      if (!KT && a == 3) load_gg(); });
    if (KT) asm volatile("s_waitcnt vmcnt(7) lgkmcnt(0)" ::: "memory"); else asm volatile("s_waitcnt vmcnt(8) lgkmcnt(0)" ::: "memory");
    BAR(); SCHED(); ST(20);
    half_step<KT, true, PR(6)>(acc, f1, f0, smem + SLOT, fa, 0, [&](int a) { if (a < 3) stage2(6 + a); });
    half_step<KT, true, PR(6)>(acc, f0, f1, smem + SLOT, fa, 1, [&](int a) { if (a < 3) stage2(9 + a); });
    WAIT_LGKM0(); BAR(); SCHED(); ST(21);
    half_step<KT, false, PR(3)>(acc, f1, f0, smem, fa, 0, [&](int a) { if (a < 6) stage2(12 + a); });
  };
  LAS char* wreg = smem + wave * PIECE;
  v4u W[5][4];
  int tro[4];
#pragma unroll
  for (int nt = 0; nt < 4; ++nt) tro[nt] = lg * 1024 + (lc >> 2) * 128 + ((nt ^ (((lc >> 3) & 1) | ((lg & 1) << 1))) * 32) + 8 * (lane & 3);
  auto read_unit = [&](int u, v4u (&d)[4]) {
    const int k0 = (4 * u) % 18, k2 = (4 * u + 2) % 18;
    const int delta = chunk_off(k2) - chunk_off(k0) - 2048;
    const int dl = delta ? (lg >= 2 ? delta : 0) : 0;
#pragma unroll
    for (int nt = 0; nt < 4; ++nt) {
      LAS char* q = wreg + chunk_off(k0) + tro[nt] + dl;
      d[nt] = cat(tr_read(q), tr_read(q + 512));
    }
  };
  const int hbase = OFF_H + (tb1 * 8 + e) * 36864 + lane * 16;
  auto stage_rng = [&](int a, int b) {
#pragma unroll
    for (int n = a; n < b; ++n) stage2(n);
  };
  auto head = [&]() {
    read_unit(0, W[0]); read_unit(1, W[1]); read_unit(2, W[2]);
    WAIT_LGKM0(); SCHED();
    if (EARLY_OLD || wn) stage_rng(18, 30);
    if (wn) PRIO(PYOUNG); else PRIO(POLD);
    SCHED();
  };
  auto tail = [&]() {
    PRIO(0);
    if (EARLY_OLD || wn) {
      read_unit(3, W[3]); read_unit(4, W[4]);
      WAIT_LGKM0(); SCHED();
      stage_rng(30, 36);
    } else {
      read_unit(3, W[3]);
      stage_rng(18, 30);
      WAIT_LGKM0(); SCHED();
      stage_rng(30, 34);
      WAIT_VM(14);
      read_unit(4, W[4]);
      WAIT_LGKM0(); SCHED();
      stage_rng(34, 36);
    }
  };
  if (ty == 0) {
    gemm1(std::true_type{});
    ST(2);
    WAIT_VM(6);
    head();
    const v4u ones = {0x3f803f80u, 0x3f803f80u, 0x3f803f80u, 0x3f803f80u};
    f32x4 okv = {0.f, 0.f, 0.f, 0.f}, oz = {0.f, 0.f, 0.f, 0.f};
    auto kv_slot = [&](int s) { return smem + (s % 3) * SLOT + ((s / 3) * 2 + (lane >> 5)) * PIECE + 6144 + (lane & 31) * 16; };
#pragma unroll
    for (int i2 = 0; i2 < 2; ++i2) {
      f32x4 kv = {0.f, 0.f, 0.f, 0.f}, z = {0.f, 0.f, 0.f, 0.f};
#pragma unroll
      for (int u = 0; u < 2; ++u) {
        f32x4 v0 = acc[2 * u][2 * i2 + 1], v1 = acc[2 * u + 1][2 * i2 + 1];
        v4u pa = phi8(acc[2 * u][2 * i2], acc[2 * u + 1][2 * i2]);
        v4u vb = {pk2(v0[0], v0[1]), pk2(v0[2], v0[3]), pk2(v1[0], v1[1]), pk2(v1[2], v1[3])};
        kv = mma32(pa, vb, kv);
        z = mma32(pa, ones, z);
      }
      if (wm == i2) { okv = kv; oz = z; }
      else {
        const int s = (wn * 2 + i2) * 3 + (wm > i2 ? wm - 1 : wm);
        *(LAS f32x4*)kv_slot(s) = kv;
        if (lc == 0) *(LAS f32x4*)(smem + 3 * SLOT + s * 64 + lg * 16) = z;
      }
    }
    WAIT_LGKM0(); BAR(); SCHED();
    if (wm < 2) {
#pragma unroll
      for (int m = 0; m < 3; ++m) {
        const int s = (wn * 2 + wm) * 3 + m;
        okv += *(LAS f32x4*)kv_slot(s); oz += *(LAS f32x4*)(smem + 3 * SLOT + s * 64 + lg * 16);
      }
      int o = OFF_KVP + ((4 * e + 2 * wn + wm) * 8 + p) * KVP_STR;
      v4u a = {f2u(okv[0]), tag32, f2u(okv[1]), tag32};
      v4u b = {f2u(okv[2]), tag32, f2u(okv[3]), tag32};
      __builtin_amdgcn_raw_buffer_store_b128(a, wsr, o + lane * 16, 0, SC1);
      __builtin_amdgcn_raw_buffer_store_b128(b, wsr, o + 1024 + lane * 16, 0, SC1);
      if (lc < 2) {
        v4u c = {f2u(lc ? oz[2] : oz[0]), tag32, f2u(lc ? oz[3] : oz[1]), tag32};
        __builtin_amdgcn_raw_buffer_store_b128(c, wsr, o + 2048 + lg * 32 + lc * 16, 0, SC1);
      }
    }
#pragma unroll
    for (int u = 0; u < 2; ++u)
      __builtin_amdgcn_raw_buffer_store_b128(phi8(acc[2 * u][4], acc[2 * u + 1][4]), wsr, hbase + (28 + wn * 2 + u) * 1024, 0, SC1);
    ST(3);
    WAIT_VM(0);
    if (lane == 0) __hip_atomic_store((GAS u64*)(ws + OFF_HF + (tb1 * 8 + e) * 128) + wn, tag, RLX_AGENT);
    ST(4);
    tail();
    if (!(p & 1) && wave == REDW) {
      PRIO(3);
      const int h = 4 * e + (p >> 1);
      const int o = OFF_KVP + h * 8 * KVP_STR;
      f32x4 kv; f32x2 zs;
      for (unsigned spins = 0;; ++spins) {
        v4u a[8], b[8], zz[8];
#pragma unroll
        for (int q = 0; q < 8; ++q) {
          a[q] = __builtin_amdgcn_raw_buffer_load_b128(wsr, o + q * KVP_STR + lane * 16, 0, SC1);
          b[q] = __builtin_amdgcn_raw_buffer_load_b128(wsr, o + q * KVP_STR + 1024 + lane * 16, 0, SC1);
          zz[q] = __builtin_amdgcn_raw_buffer_load_b128(wsr, o + q * KVP_STR + 2048 + lg * 32 + (lc & 2) * 8, 0, SC1);
        }
        SCHED();
        bool ok = true; kv = (f32x4){0.f, 0.f, 0.f, 0.f}; zs = (f32x2){0.f, 0.f};
#pragma unroll
        for (int q = 0; q < 8; ++q) {
          ok &= (a[q][1] == tag32) & (a[q][3] == tag32) & (b[q][1] == tag32) & (b[q][3] == tag32) & (zz[q][1] == tag32) & (zz[q][3] == tag32);
          kv += (f32x4){u2f(a[q][0]), u2f(a[q][2]), u2f(b[q][0]), u2f(b[q][2])};
          zs += (f32x2){u2f(zz[q][0]), u2f(zz[q][2])};
        }
        if (__all(ok) || spins > SPIN_CAP) break;
        __builtin_amdgcn_s_sleep(1);
      }
      v4u f = {pk2(kv[0], kv[1]), pk2(kv[2], kv[3]), pk2(zs[0], zs[1]), tag32};
      __builtin_amdgcn_raw_buffer_store_b128(f, wsr, OFF_KVF + h * 1024 + lane * 16, 0, SC1);
      ST(26);
    }
  } else {
    gemm1(std::false_type{});
    ST(2);
    asm volatile("s_waitcnt vmcnt(6)" : "+v"(gg)::"memory");
    if (!__all(gg[1] == tag32)) {
      for (unsigned spins = 0;; ++spins) {
        u64 v = __hip_atomic_load((const GAS u64*)(ws + OFF_G) + tb1 * 64 + lane, RLX_AGENT);
        gg = (v2u){(unsigned)v, (unsigned)(v >> 32)};
        if (__all(gg[1] == tag32) || spins > SPIN_CAP) break;
        __builtin_amdgcn_s_sleep(1);
      }
    }
    const unsigned iol = f2u(__builtin_fminf(1.f + fexp(u2f(gg[0])), 1e30f));
    float io[4];
#pragma unroll
    for (int tt = 0; tt < 4; ++tt) io[tt] = u2f(__builtin_amdgcn_ds_bpermute((tt * 16 + lc) * 4, iol));
    head();
    const int slot = 2 * (ty - 1) + wn;
#pragma unroll
    for (int tt = 0; tt < 4; ++tt) {
      v4u hv = {swiglu2(lo2(acc[tt][0]), lo2(acc[tt][1]), io[tt]), swiglu2(hi2(acc[tt][0]), hi2(acc[tt][1]), io[tt]),
                swiglu2(lo2(acc[tt][2]), lo2(acc[tt][3]), io[tt]), swiglu2(hi2(acc[tt][2]), hi2(acc[tt][3]), io[tt])};
      __builtin_amdgcn_raw_buffer_store_b128(hv, wsr, hbase + (slot * 4 + tt) * 1024, 0, SC1);
    }
    if (ty >= 2) {
      v4u hv = {ffn_mixed(acc[0][4], io[0]), ffn_mixed(acc[1][4], io[1]), ffn_mixed(acc[2][4], io[2]), ffn_mixed(acc[3][4], io[3])};
      __builtin_amdgcn_raw_buffer_store_b128(hv, wsr, hbase + (24 + (ty - 2) * 2 + wn) * 1024, 0, SC1);
    } else {
#pragma unroll
      for (int u = 0; u < 2; ++u)
        __builtin_amdgcn_raw_buffer_store_b128(phi8(acc[2 * u][4], acc[2 * u + 1][4]), wsr, hbase + (28 + (2 + wn) * 2 + u) * 1024, 0, SC1);
    }
    SCHED();
    ST(3);
    WAIT_VM(0);
    if (lane == 0) __hip_atomic_store((GAS u64*)(ws + OFF_HF + (tb1 * 8 + e) * 128) + ty * 2 + wn, tag, RLX_AGENT);
    ST(4);
    SCHED();
    tail();
    if (bid == 8 * 4 && wave == 0) {
      for (unsigned spins = 0;; ++spins) {
        bool ok = true;
        v4u a[16];
#pragma unroll
        for (int q = 0; q < 16; ++q) a[q] = __builtin_amdgcn_raw_buffer_load_b128(wsr, OFF_G + q * 1024 + lane * 16, 0, SC1);
        SCHED();
#pragma unroll
        for (int q = 0; q < 16; ++q) ok &= (a[q][1] == tag32) & (a[q][3] == tag32);
        if (__all(ok) || spins > SPIN_CAP) break;
        __builtin_amdgcn_s_sleep(1);
      }
      if (lane == 0) __hip_atomic_store((GAS u64*)ws, tag, RLX_AGENT);
    }
  }
  ST(5);
  if (wn) PRIO(P2Y); else PRIO(P2O);
  const GAS u64* fl = (const GAS u64*)(ws + OFF_HF + (tb2 * 8 + e2) * 128) + (lane & 7);
  for (unsigned spins = 0;; ++spins) {
    u64 v = __hip_atomic_load(fl, RLX_AGENT);
    if (__all(v == tag || (lane & 1) || (lane & 7) == 0) || spins > SPIN_CAP) break;
    __builtin_amdgcn_s_sleep(1);
  }
  WAIT_VM(0);
  ST(6);
  f32x4 c2[4][4];
#pragma unroll
  for (int tt = 0; tt < 4; ++tt)
#pragma unroll
    for (int nt = 0; nt < 4; ++nt) c2[tt][nt] = (f32x4){0.f, 0.f, 0.f, 0.f};
  const int hb2 = OFF_H + (tb2 * 8 + e2) * 36864 + lane * 16;
  const int hs0 = par * 16384, hsd = sgn * 8192;
  v4u hq[9][4];
  auto ldq = [&](int qd) {
#pragma unroll
    for (int i = 0; i < 4; ++i)
      hq[qd][i] = qd < 6 ? __builtin_amdgcn_raw_buffer_load_b128(wsr, hb2 + i * 1024, hs0 + (qd % 3) * hsd + (qd < 3 ? 0 : 4096), SC1)
                         : __builtin_amdgcn_raw_buffer_load_b128(wsr, hb2 + (qd * 4 + i) * 1024, 0, SC1);
  };
  auto mmq = [&](const v4u (&w)[4], const v4u (&hf)[4]) {
#pragma unroll
    for (int tt = 0; tt < 4; ++tt)
#pragma unroll
      for (int nt = 0; nt < 4; ++nt) c2[tt][nt] = mma32(w[nt], hf[tt], c2[tt][nt]);
  };
  v4u kvf[4]; v2u g2;
  auto ldkv = [&]() {
#pragma unroll
    for (int i = 0; i < 4; ++i) kvf[i] = __builtin_amdgcn_raw_buffer_load_b128(wsr, OFF_KVF + (4 * e2 + i) * 1024 + lane * 16, 0, SC1);
    g2 = __builtin_amdgcn_raw_buffer_load_b64(wsr, OFF_G + (tb2 * 64 + lane) * 8, 0, SC1);
  };
  auto kvok = [&]() { return __all((kvf[0][3] == tag32) & (kvf[1][3] == tag32) & (kvf[2][3] == tag32) & (kvf[3][3] == tag32) & (g2[1] == tag32)); };
  auto mmf = [&](const v4u (&w)[4], const v4u (&hf)[4], auto fill) {
#pragma unroll
    for (int i = 0; i < 16; ++i) { c2[i >> 2][i & 3] = mma32(w[i & 3], hf[i >> 2], c2[i >> 2][i & 3]); fill(i); SCHED(); }
  };
  ldq(0);
  u64 kf = __hip_atomic_load(fl, RLX_AGENT);
  ldq(1); ldq(2); SCHED();
  mmq(W[0], hq[0]); SCHED(); ST(28);
  for (unsigned spins = 0; !__all(kf == tag) && spins <= SPIN_CAP; ++spins) {
    __builtin_amdgcn_s_sleep(1);
    kf = __hip_atomic_load(fl, RLX_AGENT);
  }
  ST(7);
  ldq(3); ldq(7); ldq(8); SCHED();
  ST(29); mmq(W[1], hq[1]); SCHED(); ST(30); ldkv(); ldq(4); SCHED();
  mmq(W[2], hq[2]); SCHED(); ST(31); ldq(5); SCHED();
  mmq(W[3], hq[3]); SCHED(); ldq(6); SCHED();
  ST(8);
  const bool early = kvok();
  if (!early) {
    for (unsigned spins = 0;; ++spins) {
      ldkv();
      if (kvok() || spins > SPIN_CAP) break;
      __builtin_amdgcn_s_sleep(1);
    }
  }
  ST(9);
  {
    const unsigned igl = f2u(1.f + fexp(-u2f(g2[0])));
    f32x2 ig[2];
#pragma unroll
    for (int tt = 0; tt < 4; ++tt) ig[tt >> 1][tt & 1] = u2f(__builtin_amdgcn_ds_bpermute((tt * 16 + lc) * 4, igl));
    const f32x4 zero = {0.f, 0.f, 0.f, 0.f}, eps = {1e-6f, 1e-6f, 0.f, 0.f};
    f32x4 den[4], n0[4], n1[4]; f32x2 rr[4];
    v4u of[2][4];
    auto small = [&](int so) {
#pragma unroll
      for (int hh = 0; hh < 2; ++hh) {
        const int i = 2 * so + hh;
        const unsigned zv = kvf[i][2];
        const v4u zf = {(lc & 3) == 0 ? zv : 0u, (lc & 3) == 2 ? zv : 0u, (lc & 3) == 1 ? zv : 0u, (lc & 3) == 3 ? zv : 0u};
#pragma unroll
        for (int u = 0; u < 2; ++u) den[hh * 2 + u] = mma32(zf, hq[7 + so][hh * 2 + u], eps);
      }
#pragma unroll
      for (int c = 0; c < 4; ++c) {
        const v2u kv = {kvf[2 * so + (c >> 1)][0], kvf[2 * so + (c >> 1)][1]};
        const v4u qq = hq[7 + so][c];
        n0[c] = mma16(kv, (v2u){qq[0], qq[1]}, zero); n1[c] = mma16(kv, (v2u){qq[2], qq[3]}, zero);
      }
      SCHED();
    };
    auto piece = [&](int so, int i) {
      const int c = i & 3, hh = c >> 1, u = c & 1;
      if (i < 4) { const f32x2 dd = (lo2(den[c]) + hi2(den[c])) * ig[u]; rr[c] = (f32x2){frcp(dd[0]), frcp(dd[1])}; }
      else if (i < 8) {
        of[so][2 * u][hh * 2] = __builtin_bit_cast(unsigned, __builtin_convertvector(lo2(n0[c]) * rr[c][0], bf16x2));
        of[so][2 * u][hh * 2 + 1] = __builtin_bit_cast(unsigned, __builtin_convertvector(hi2(n0[c]) * rr[c][0], bf16x2));
      } else if (i < 12) {
        of[so][2 * u + 1][hh * 2] = __builtin_bit_cast(unsigned, __builtin_convertvector(lo2(n1[c]) * rr[c][1], bf16x2));
        of[so][2 * u + 1][hh * 2 + 1] = __builtin_bit_cast(unsigned, __builtin_convertvector(hi2(n1[c]) * rr[c][1], bf16x2));
      }
    };
    small(0); ST(32);
    mmf(W[4], hq[4], [&](int i) { piece(0, i); });
    ST(33); read_unit(5, W[0]); read_unit(6, W[1]); SCHED();
    small(1); ST(34);
    mmf(W[0], of[0], [&](int i) { piece(1, i); });
    ST(35); read_unit(7, W[2]); SCHED();
    mmq(W[1], of[1]); SCHED();
    read_unit(8, W[3]); SCHED();
  }
  ST(25);
  mmq(W[2], hq[5]); SCHED(); ST(36);
  {
    v4u hf[4];
#pragma unroll
    for (int tt = 0; tt < 4; ++tt) hf[tt] = (v4u){hq[6][0][tt], hq[6][1][tt], hq[6][2][tt], hq[6][3][tt]};
    mmq(W[3], hf);
  }
  ST(10);
#pragma unroll
  for (int tt = 0; tt < 4; ++tt)
#pragma unroll
    for (int nt = 0; nt < 4; ++nt)
      *(LAS f32x4*)(wreg + chunk_off(tt * 4 + nt) + lg * 256 + ((lc ^ (4 * nt + 2 * (lg >> 1))) & 15) * 16) = c2[tt][nt];
  WAIT_LGKM0(); BAR(); SCHED();
  ST(11);
  {
    const int o = lane & 7, c = (wave & 1) * 8 + (lane >> 3), tt = wave >> 1;
    const int k = tt * 4 + (o >> 1);
    const int ro = (k < 6 ? 2 * SLOT + k * 1024 : (k < 12 ? (k - 6) * 1024 : SLOT + (k - 12) * 1024)) + (o & 1) * 512 + ((c ^ (2 * o)) & 15) * 16;
    f32x4 a = {0.f, 0.f, 0.f, 0.f}, b = {0.f, 0.f, 0.f, 0.f};
#pragma unroll
    for (int w = 0; w < 8; ++w) { a += *(LAS f32x4*)(smem + w * PIECE + ro); b += *(LAS f32x4*)(smem + w * PIECE + ro + 256); }
    v4u r = {pk2(a[0], a[1]), pk2(a[2], a[3]), pk2(b[0], b[1]), pk2(b[2], b[3])};
    *(GAS v4u*)(out + (size_t)(tb2 * 64 + tt * 16 + c) * 1024 + nb * 128 + o * 16) = r;
  }
  ST(12);
  WAIT_VM(0);
  ST(13);
  { const unsigned dl = 6000 + 4 * (st[DB] - st[0]); while ((unsigned)__builtin_amdgcn_s_memrealtime() - st[0] < dl) __builtin_amdgcn_s_sleep(2); }
}
}

extern "C" void kernel_launch(void* const* d_in, const int* in_sizes, int n_in, void* d_out, int out_size, void* d_ws, size_t ws_size,
                              hipStream_t stream) {
  fk::fused<<<256, 512, 0, stream>>>((const char*)d_in[0], (const char*)d_in[1], (const char*)d_in[2], (const char*)d_in[4],
                                     (const char*)d_in[5], (const char*)d_in[6], (const char*)d_in[7], (char*)d_out, (char*)d_ws);
}
```

```cpp
#include <hip/hip_runtime.h>
#include <hip/hip_bf16.h>
#include <stdint.h>

#define GAS __attribute__((address_space(1)))
#define LAS __attribute__((address_space(3)))
typedef unsigned v4u __attribute__((ext_vector_type(4)));
typedef unsigned v2u __attribute__((ext_vector_type(2)));
typedef unsigned v16u __attribute__((ext_vector_type(16)));
typedef float f32x4 __attribute__((ext_vector_type(4)));
typedef float f32x2 __attribute__((ext_vector_type(2)));
typedef short s16x4 __attribute__((ext_vector_type(4)));
typedef __bf16 bf16x8 __attribute__((ext_vector_type(8)));
typedef __bf16 bf16x2 __attribute__((ext_vector_type(2)));
typedef unsigned long long u64;

namespace fk {
constexpr int SLOT = 32768, LDS_BYTES = 5 * SLOT;
constexpr int OFF_G = 4096;
constexpr int OFF_F = 32768;
constexpr int OFF_KVP = 131072;
constexpr int OFF_H = 1 << 20;
constexpr int WS_USED = OFF_H + 256 * 36864;
constexpr int SC1 = 16;

#define STH(k, p, a, b) do {} while (0)

#define DEV __device__ __forceinline__
DEV unsigned f2u(float f) { return __builtin_bit_cast(unsigned, f); }
DEV float u2f(unsigned u) { return __builtin_bit_cast(float, u); }
DEV unsigned pk2(float a, float b) { return __builtin_bit_cast(unsigned, __builtin_convertvector((f32x2){a, b}, bf16x2)); }
DEV f32x4 mma32(v4u a, v4u b, f32x4 c) {
  return __builtin_amdgcn_mfma_f32_16x16x32_bf16(__builtin_bit_cast(bf16x8, a), __builtin_bit_cast(bf16x8, b), c, 0, 0, 0);
}
DEV f32x4 mma16(v2u a, v2u b, f32x4 c) {
  return __builtin_amdgcn_mfma_f32_16x16x16bf16_1k(__builtin_bit_cast(s16x4, a), __builtin_bit_cast(s16x4, b), c, 0, 0, 0);
}
DEV v2u tr_read(LAS char* p) { return __builtin_bit_cast(v2u, __builtin_amdgcn_ds_read_tr16_b64_v4i16((LAS s16x4*)p)); }
DEV v4u cat(v2u a, v2u b) { return (v4u){a[0], a[1], b[0], b[1]}; }
template <int O> DEV void setm0(unsigned l) { asm volatile("s_add_u32 m0, %0, %1" ::"s"(l), "i"(O) : "scc"); }
DEV void dma(unsigned voff, const void* sbase) { asm volatile("global_load_lds_dwordx4 %0, %1" ::"v"(voff), "s"(sbase) : "memory"); }
DEV void dmaw(unsigned voff, const void* sbase) { asm volatile("global_load_lds_dwordx4 %0, %1" ::"v"(voff), "s"(sbase) : "memory"); }
DEV void dma(const void* g) { asm volatile("global_load_lds_dwordx4 %0, off" ::"v"(g) : "memory"); }
#define NOP() asm volatile("s_nop 0")
#define WAITC(vm, lg) __builtin_amdgcn_s_waitcnt(((vm) & 15) | 0x70 | ((lg) << 8) | (((vm) >> 4) << 14))
DEV int lane_id() {
  unsigned z = 0; asm volatile("" : "+s"(z)); return __builtin_amdgcn_mbcnt_hi(-1, __builtin_amdgcn_mbcnt_lo(-1, z));
}
#define FRESH() asm volatile("" : "+s"(wave), "+s"(bid), "+s"(rot))
DEV void ld4c(unsigned& d, unsigned voff, const void* sbase) {
  asm volatile("global_load_dword %0, %1, %2 sc1" : "=v"(d) : "v"(voff), "s"(sbase) : "memory");
}
DEV void ld4(unsigned& d, unsigned voff, const void* sbase) {
  asm volatile("global_load_dword %0, %1, %2" : "=v"(d) : "v"(voff), "s"(sbase) : "memory");
}
DEV void ld16(v4u& d, unsigned voff, const void* sbase) {
  asm volatile("global_load_dwordx4 %0, %1, %2" : "=v"(d) : "v"(voff), "s"(sbase) : "memory");
}
template <int O> DEV void ld16c(v4u& d, unsigned voff, const void* sbase) {
  asm volatile("global_load_dwordx4 %0, %1, %2 offset:%3 sc1" : "=v"(d) : "v"(voff), "s"(sbase), "i"(O) : "memory");
}
template <int O> DEV void st16c(v4u d, unsigned voff, void* sbase) {
  asm volatile("global_store_dwordx4 %0, %1, %2 offset:%3 sc1\n\ts_nop 1" ::"v"(voff), "v"(d), "s"(sbase), "i"(O) : "memory");
}
template <int O, int K> DEV void st16g(v4u d, unsigned voff, void* sbase, unsigned grp) {
  asm volatile("s_cmp_lg_u32 %4, %5\n\ts_cbranch_scc1 3f\n\tglobal_store_dwordx4 %0, %1, %2 offset:%3 sc1\n\ts_nop 1\n3:" ::"v"(voff), "v"(d), "s"(sbase), "i"(O), "s"(grp), "i"(K) : "scc", "memory");
}
DEV void ld8c(v2u& d, unsigned voff, const void* sbase) {
  asm volatile("global_load_dwordx2 %0, %1, %2 sc1" : "=v"(d) : "v"(voff), "s"(sbase) : "memory");
}
DEV void g_retry(v2u& d, unsigned voff, const void* src, unsigned tag) {
  unsigned t = d[1], n;
  asm volatile("v_cmp_ne_u32 vcc, %5, %1\n\ts_nop 1\n\ts_cmp_eq_u64 vcc, 0\n\ts_cbranch_scc1 3f\n\ts_mov_b32 %2, 0x3ffff\n"
               "0:\n\ts_sleep 1\n\tglobal_load_dword %1, %3, %4 offset:4 sc1\n\ts_waitcnt vmcnt(0)\n"
               "\tv_cmp_ne_u32 vcc, %5, %1\n\ts_nop 1\n\ts_cmp_eq_u64 vcc, 0\n\ts_cbranch_scc1 2f\n\ts_sub_u32 %2, %2, 1\n\ts_cbranch_scc0 0b\n"
               "2:\n\tglobal_load_dwordx2 %0, %3, %4 sc1\n\ts_waitcnt vmcnt(0)\n3:"
               : "+v"(d), "+v"(t), "=&s"(n) : "v"(voff), "s"(src), "s"(tag) : "vcc", "scc", "memory");
}
DEV void poll(unsigned& fv, unsigned voff, const void* fl, unsigned tag, unsigned mask) {
  unsigned t, n;
  asm volatile("s_mov_b32 %2, 0x3ffff\n\ts_branch 1f\n"
               "0:\n\ts_sleep 1\n\tglobal_load_dword %0, %3, %4 sc1\n\ts_waitcnt vmcnt(0)\n"
               "1:\n\tv_cmp_eq_u32 vcc, %5, %0\n\ts_nop 1\n\ts_and_b32 %1, vcc_lo, %6\n\ts_cmp_eq_u32 %1, %6\n\ts_cbranch_scc1 2f\n\t"
               "s_sub_u32 %2, %2, 1\n\ts_cbranch_scc0 0b\n2:"
               : "+v"(fv), "=&s"(t), "=&s"(n) : "v"(voff), "s"(fl), "s"(tag), "s"(mask) : "vcc", "scc", "memory");
}
DEV float ex2(float x) { return __builtin_amdgcn_exp2f(x); }
DEV float frcp(float x) { return __builtin_amdgcn_rcpf(x); }
DEV float bflo(unsigned u) { return u2f(u << 16); }
DEV float bfhi(unsigned u) { return u2f(u & 0xffff0000u); }
DEV float pin(float v) { asm volatile("" : "+v"(v)); return v; }
DEV float vmax(float a, float b) {
  int m = __builtin_elementwise_max(__builtin_bit_cast(int, a), __builtin_bit_cast(int, b)); asm volatile("" : "+v"(m)); return __builtin_bit_cast(float, m);
}
DEV LAS char* pinp(LAS char* p) { asm volatile("" : "+v"(p)); return p; }
DEV unsigned pinu(unsigned v) { asm volatile("" : "+v"(v)); return v; }
template <class T> DEV T pins(T v) { asm volatile("" : "+s"(v)); return v; }
DEV f32x2 lo2(f32x4 a) { return (f32x2){a[0], a[1]}; }
DEV f32x2 hi2(f32x4 a) { return (f32x2){a[2], a[3]}; }
DEV unsigned pkv(f32x2 v) { return __builtin_bit_cast(unsigned, __builtin_convertvector(v, bf16x2)); }
template <int CTRL, int RM> DEV float dpp_add(float v) { return v + u2f(__builtin_amdgcn_update_dpp(0u, f2u(v), CTRL, RM, 0xf, false)); }
DEV LAS char* xorp(LAS char* p, unsigned m) { return (LAS char*)((uintptr_t)p ^ m); }
DEV char* mkptr(unsigned lo, unsigned hi) { u64 a = ((u64)hi << 32) | lo; asm volatile("" : "+s"(a)); return (char*)a; }
#define BAR() __builtin_amdgcn_s_barrier()
#define SCHED() __builtin_amdgcn_sched_barrier(0)
#define PRIO(n) __builtin_amdgcn_s_setprio(n)
constexpr float L2E = 1.44269504088896f;
DEV unsigned swiglu2(f32x2 g, f32x2 u, float io) {
  const f32x2 t = g * -L2E, e = {ex2(t[0]), ex2(t[1])}, i2 = {io, io};
  const f32x2 d = __builtin_elementwise_fma(e, i2, i2), r = {frcp(d[0]), frcp(d[1])};
  return pkv((g * u) * r);
}
DEV unsigned phi2(f32x2 t) {
  const f32x2 s = __builtin_elementwise_fma(t, (f32x2){L2E, L2E}, (f32x2){-1e-6f * L2E, -1e-6f * L2E});
  const f32x2 e = {__builtin_amdgcn_fmed3f(ex2(s[0]), 0.f, 1.f), __builtin_amdgcn_fmed3f(ex2(s[1]), 0.f, 1.f)}, m = {vmax(t[0], 1e-6f), vmax(t[1], 1e-6f)};
  return pkv(e + m);
}
template <int C> DEV unsigned qp(unsigned v) { return __builtin_amdgcn_update_dpp(0u, v, C, 0xf, 0xf, true); }
DEV void kv_retry(v4u& a0, v4u& a1, v4u& a2, v4u& a3, v4u& b0, v4u& b1, v4u& b2, v4u& b3, unsigned voff, const char* src, unsigned tag) {
  unsigned bad = (a0[3] ^ tag) | (a1[3] ^ tag) | (a2[3] ^ tag) | (a3[3] ^ tag) | (b0[3] ^ tag) | (b1[3] ^ tag) | (b2[3] ^ tag) | (b3[3] ^ tag);
  unsigned t, n; const char* src2 = src + 16384;
  asm volatile("v_cmp_ne_u32 vcc, 0, %8\n\ts_nop 1\n\ts_cmp_eq_u64 vcc, 0\n\ts_cbranch_scc1 3f\n\ts_mov_b32 %10, 0x3ffff\n"
               "0:\n\tv_mov_b32 %8, 0\n"
#define KVT(B, O) "\tglobal_load_dword %9, %11, %" #B " offset:" #O " sc1\n\ts_waitcnt vmcnt(0)\n\tv_xor_b32 %9, %14, %9\n\tv_or_b32 %8, %8, %9\n"
               KVT(12, 12) KVT(12, 1036) KVT(12, 2060) KVT(12, 3084) KVT(13, 12) KVT(13, 1036) KVT(13, 2060) KVT(13, 3084)
               "\tv_cmp_ne_u32 vcc, 0, %8\n\ts_nop 1\n\ts_cmp_eq_u64 vcc, 0\n\ts_cbranch_scc1 2f\n\ts_sub_u32 %10, %10, 1\n\ts_cbranch_scc0 0b\n"
               "2:\n\tglobal_load_dwordx4 %0, %11, %12 sc1\n\tglobal_load_dwordx4 %1, %11, %12 offset:1024 sc1\n"
               "\tglobal_load_dwordx4 %2, %11, %12 offset:2048 sc1\n\tglobal_load_dwordx4 %3, %11, %12 offset:3072 sc1\n"
               "\tglobal_load_dwordx4 %4, %11, %13 sc1\n\tglobal_load_dwordx4 %5, %11, %13 offset:1024 sc1\n"
               "\tglobal_load_dwordx4 %6, %11, %13 offset:2048 sc1\n\tglobal_load_dwordx4 %7, %11, %13 offset:3072 sc1\n\ts_waitcnt vmcnt(0)\n3:"
               : "+v"(a0), "+v"(a1), "+v"(a2), "+v"(a3), "+v"(b0), "+v"(b1), "+v"(b2), "+v"(b3), "+v"(bad), "=&v"(t), "=&s"(n)
               : "v"(voff), "s"(src), "s"(src2), "s"(tag) : "vcc", "scc", "memory");
}

DEV unsigned max3u(unsigned a, unsigned b, unsigned c) { return __builtin_elementwise_max(__builtin_elementwise_max(a, b), c); }
DEV void h_check(v4u& a, v4u& b, v4u& c, v4u& d, unsigned voff, const char* src) {
  unsigned m = max3u(a[3], b[3], __builtin_elementwise_max(c[3], d[3])), t0, t1, t2, n;
  asm volatile("v_cmp_eq_u32 vcc, -1, %4\n\ts_nop 1\n\ts_cmp_eq_u64 vcc, 0\n\ts_cbranch_scc1 3f\n\ts_mov_b32 %8, 0x3ffff\n"
               "0:\n\ts_sleep 2\n\tglobal_load_dword %4, %9, %10 offset:12 sc1\n\tglobal_load_dword %5, %9, %10 offset:1036 sc1\n"
               "\tglobal_load_dword %6, %9, %10 offset:2060 sc1\n\tglobal_load_dword %7, %9, %10 offset:3084 sc1\n"
               "\ts_waitcnt vmcnt(0)\n\tv_max3_u32 %4, %4, %5, %6\n\tv_max_u32 %4, %4, %7\n"
               "\tv_cmp_eq_u32 vcc, -1, %4\n\ts_nop 1\n\ts_cmp_eq_u64 vcc, 0\n\ts_cbranch_scc1 2f\n\ts_sub_u32 %8, %8, 1\n\ts_cbranch_scc0 0b\n"
               "2:\n\tglobal_load_dwordx4 %0, %9, %10 sc1\n\tglobal_load_dwordx4 %1, %9, %10 offset:1024 sc1\n"
               "\tglobal_load_dwordx4 %2, %9, %10 offset:2048 sc1\n\tglobal_load_dwordx4 %3, %9, %10 offset:3072 sc1\n\ts_waitcnt vmcnt(0)\n3:"
               : "+v"(a), "+v"(b), "+v"(c), "+v"(d), "+v"(m), "=&v"(t0), "=&v"(t1), "=&v"(t2), "=&s"(n)
               : "v"(voff), "s"(src) : "vcc", "scc", "memory");
}

__global__ __launch_bounds__(512, 2) void fused(const char* x_, const char* wqkv_, const char* wo_, const char* w1_, const char* w3_,
                                                const char* w2_, const char* gw_, char* out_, char* ws_) {
  v16u ka; v2u kb;
  asm volatile("s_load_dwordx16 %0, %2, 0x0\n\ts_load_dwordx2 %1, %2, 0x40" : "=&s"(ka), "=&s"(kb) : "s"(__builtin_amdgcn_kernarg_segment_ptr()));
  const char* pc0; asm volatile("s_getpc_b64 %0" : "=s"(pc0));
  __shared__ __attribute__((aligned(16))) char smem_[LDS_BYTES];
  LAS char* smem = (LAS char*)smem_;
  const unsigned lds0 = (unsigned)(uintptr_t)smem;
  STH(0, 63, 96, 98);
  int lane = threadIdx.x & 63, wave = __builtin_amdgcn_readfirstlane(threadIdx.x >> 6), bid = blockIdx.x;
  unsigned pce; ld4(pce, __builtin_elementwise_min((unsigned)((bid >> 3) * 2048 + (threadIdx.x >> 5) * 128), 35584u), pc0);
#define TB ((bid & 4) * 2 + ((bid >> 3) & 7))
#define EG ((bid & 3) * 2 + (bid >> 7))
#define FB (896 * (bid & 3) + 384 * (bid >> 7))
#define WM (wave & 3)
#define WN (wave >> 2)
#define VC(l) (((((l) >> 1) & 3) ^ ((((l) >> 4) & 1) | ((wave & 1) << 1))) * 32 + ((l) & 1) * 16)
#define SZ(l) ((((l) >> 3) & 1) | (((l) >> 3) & 2))
#define FR(l) ((((l) >> 2) & 3) * 128 + 8 * ((l) & 3))
  unsigned rot = ((bid & 7) + 2 * ((bid >> 3) & 3)) & 7;
#define KT(s) ((rot + (s)) & 7u)
  const unsigned xo0 = (lane >> 3) * 1024 + 16 * ((lane & 7) ^ (lane >> 4)), xo1 = (xo0 ^ 64) + 8192;
  const unsigned lw2 = lds0 + wave * 2048, lw1 = lds0 + wave * 1024;
  unsigned oX = (TB * 8 + wave) * 16384;
  const bool tA = !(bid & 64);
  const unsigned st1 = tA ? 3072 : 3584;
  unsigned vo1 = (lane >> 3) * st1 + VC(lane), o1 = (tA ? 1024 : FB) + wave * 8 * st1, o1b = tA ? 1024 : 0, k0 = KT(0) * 64 * st1, em = tA ? 896 : 0, e0 = 128 * (EG + 4);
  asm volatile("s_waitcnt lgkmcnt(0)" : "+s"(ka), "+s"(kb), "+s"(oX), "+s"(o1), "+s"(o1b), "+s"(k0), "+s"(em), "+s"(e0), "+v"(vo1));
  STH(34, 0, 98, 96);
  const char* xw = mkptr(ka[0], ka[1]) + oX;
  setm0<0>(lw2); NOP(); dma(xo0, xw + KT(0) * 128); setm0<0 + 1024>(lw2); NOP(); dma(xo1, xw + KT(0) * 128);
  o1 += (e0 - (ka[2] & 896)) & em;
  const char* b1A = mkptr(tA ? ka[2] : ka[6], tA ? ka[3] : ka[7]) + o1;
  setm0<0 + 16384>(lw1); NOP(); dma(vo1, b1A + k0);
  const char* b1B = mkptr(tA ? ka[2] : ka[8], tA ? ka[3] : ka[9]) + (o1 + o1b);
  setm0<0 + 24576>(lw1); NOP(); dma(vo1, b1B + k0);
#define W1 mkptr(ka[6], ka[7])
#define W3 mkptr(ka[8], ka[9])
  const f32x4 zero4 = {0.f, 0.f, 0.f, 0.f}, eps4 = {1e-6f, 1e-6f, 1e-6f, 1e-6f};
  const v4u ones = {0x3f803f80u, 0x3f803f80u, 0x3f803f80u, 0x3f803f80u};
  auto finish = [&](LAS char* rr, unsigned oo, char* op_) {
    asm volatile("s_waitcnt lgkmcnt(0)" ::: "memory"); BAR(); SCHED();
    STH(28, 27, 96, 98);
    f32x4 a[8], b[8];
#pragma unroll
    for (int w = 0; w < 8; ++w) { a[w] = *(LAS f32x4*)(rr + w * 4096); b[w] = *(LAS f32x4*)(rr + w * 4096 + 256); }
    SCHED();
#pragma unroll
    for (int w = 1; w < 8; ++w) { a[0] += a[w]; b[0] += b[w]; }
    const v4u r = {pk2(a[0][0], a[0][1]), pk2(a[0][2], a[0][3]), pk2(b[0][0], b[0][1]), pk2(b[0][2], b[0][3])};
    asm volatile("global_store_dwordx4 %0, %1, %2 sc1\n\ts_nop 1" ::"v"(oo), "v"(r), "s"(op_) : "memory");
    __builtin_amdgcn_endpgm();
  };
  if (__builtin_expect(tA, 1)) {
    FRESH(); asm volatile("" : "+v"(lane));
    char* ws = mkptr(kb[0], kb[1]); const unsigned l16 = lane * 16;
    const unsigned gtok = TB * 128 + ((bid & 3) * 4 + (bid >> 6)) * 8 + wave;
    unsigned kX[8];
    for (int i = 0; i < 8; ++i) kX[i] = pins(KT(i) * 128);
    unsigned kQ[8];
    for (int i = 0; i < 8; ++i) kQ[i] = pins(KT(i) * (64 * 3072));
    const __amdgpu_buffer_rsrc_t wsr = __builtin_amdgcn_make_buffer_rsrc(ws, 0, WS_USED, 0x00020000);
    unsigned kF[8];
#pragma unroll
    for (int i = 0; i < 8; ++i) kF[i] = pins(KT(i) * (64 * 3584));
    const int r4 = bid >> 5, tb2 = (bid & 7) * 4 + ((bid >> 3) & 3);
    const char* w2b = pins(mkptr(ka[10], ka[11]) + (unsigned)((448 * (wave >> 1) + 192 * (wave & 1)) * 1024 + (bid >> 5) * 128));
    const char* wob = pins(mkptr(ka[4], ka[5]) + (unsigned)(((128 * (wave + 4) - (ka[2] & 896)) & 896) * 512 + (bid >> 5) * 128) - 7 * 32768);
    const unsigned whl = pins((384 - 160 * (wave & 1)) * 1024);
    const char* fl = pins(ws + (unsigned)(OFF_F + (tb2 * 8 + wave) * 128));
    const char* hb = pins(ws + (unsigned)(OFF_H + (tb2 * 8 + wave) * 36864));
    char* hdst = pins(ws + (unsigned)(OFF_H + ((2 * TB + (WM >> 1)) * 8 + EG) * 36864 + (WM & 1) * 2048 + WN * 4096));
    const unsigned fo = pins(OFF_F + ((2 * TB + (WM >> 1)) * 8 + EG) * 128 + 4 * (2 * WN + (WM & 1)));
    const unsigned lw4 = pins(lds0 + wave * 4096);
    v4u X[16][2]; v2u Wl[2][4], Wh[2][4];
    f32x4 ac[5][2][2], c2[4][4];
    unsigned fv, va, vp;
    LAS char *cw0, *cw1, *cx0, *cx1;
    const char* sp;
    const unsigned un[9] = {pins<unsigned>(0 + (0 ^ (r4 & 1))), pins<unsigned>(0 + (1 ^ (r4 & 1))), pins<unsigned>(2 + ((0 + r4) & 3)), pins<unsigned>(2 + ((1 + r4) & 3)), pins<unsigned>(2 + ((2 + r4) & 3)), pins<unsigned>(2 + ((3 + r4) & 3)), pins<unsigned>(6), pins<unsigned>(7 + (0 ^ (r4 & 1))), pins<unsigned>(7 + (1 ^ (r4 & 1)))};
    const char* b2A = pins(W1 + (unsigned)(FB + 128 + wave * (8 * 3584))); const char* b2B = pins(W3 + (unsigned)(FB + 128 + wave * (8 * 3584)));
    const char* b3 = pins(b1A - 1024);
    const char* kvsrc = pins(ws + (unsigned)(OFF_KVP + ((4 * EG + 2 * WN) * 16 + 4 * WM) * 1024));
    const unsigned kvdst = pins(OFF_KVP + ((4 * EG + 2 * WN + (WM & 1)) * 16 + TB) * 1024 + ((WM & 2) << 29));
    const unsigned vo2 = (lane >> 3) * 3584 + VC(lane);
    cw0 = pinp(smem + (16384 + (lane >> 4) * 1024 + FR(lane) + ((2 * WN) ^ SZ(lane)) * 32)); cw1 = pinp(xorp(cw0, 32));
    cx0 = pinp(smem + (0 + (WM * 32 + (lane & 15)) * 128 + 16 * ((lane >> 4) ^ ((lane & 15) >> 1)))); cx1 = pinp(xorp(cx0, 64));
    SCHED(); BAR();
    va = pinu(vo2 + kF[0]); setm0<32768>(lw1); NOP();
    dma(va, b2A);
    va = pinu(xo0 + kX[1]); setm0<40960>(lw2); NOP();
    dma(va, xw);
    va = pinu(xo1 + kX[1]); setm0<41984>(lw2); NOP();
    dma(va, xw);
    va = pinu(vo1 + kQ[1]); setm0<57344>(lw1); NOP();
    dma(va, b1A);
    setm0<65536>(lw1); NOP();
    dma(va, b1B);
    va = pinu(vo2 + kF[1]); setm0<73728>(lw1); NOP();
    dma(va, b2A);
    WAITC(6, 15);
    asm volatile("" :: "v"(pce));
    BAR(); SCHED();
    STH(1, 34, 96, 98);
    Wl[0][0] = tr_read(cw0 + 0);
    X[0][0] = *(LAS v4u*)(cx0 + 0);
    Wh[0][0] = tr_read(cw0 + 512);
    X[0][1] = *(LAS v4u*)(cx0 + 2048);
    Wl[0][1] = tr_read(cw1 + 0);
    Wh[0][1] = tr_read(cw1 + 512);
    Wl[0][2] = tr_read(cw0 + 4096);
    X[1][0] = *(LAS v4u*)(cx1 + 0);
    Wh[0][2] = tr_read(cw0 + 4608);
    X[1][1] = *(LAS v4u*)(cx1 + 2048);
    Wl[0][3] = tr_read(cw1 + 4096);
    Wh[0][3] = tr_read(cw1 + 4608);
    SCHED();
    ac[0][0][0] = mma32(X[0][0], cat(Wl[0][0], Wh[0][0]), eps4); SCHED();
    Wl[1][0] = tr_read(cw0 + 8192);
    SCHED();
    SCHED();
    ac[0][1][0] = mma32(X[0][1], cat(Wl[0][0], Wh[0][0]), eps4); SCHED();
    unsigned nn; ld4c(nn, 0, ws);
    Wh[1][0] = tr_read(cw0 + 8704);
    SCHED();
    PRIO(2);
    SCHED();
    ac[0][0][1] = mma32(X[0][0], cat(Wl[0][1], Wh[0][1]), eps4); SCHED();
    Wl[1][1] = tr_read(cw1 + 8192);
    SCHED();
    SCHED();
    ac[0][1][1] = mma32(X[0][1], cat(Wl[0][1], Wh[0][1]), eps4); SCHED();
    v4u gr; ld16(gr, l16, mkptr(ka[12], ka[13]));
    Wh[1][1] = tr_read(cw1 + 8704);
    SCHED();
    PRIO(1);
    SCHED();
    ac[0][0][0] = mma32(X[1][0], cat(Wl[0][2], Wh[0][2]), ac[0][0][0]); SCHED();
    Wl[1][2] = tr_read(cw0 + 12288);
    SCHED();
    SCHED();
    ac[0][1][0] = mma32(X[1][1], cat(Wl[0][2], Wh[0][2]), ac[0][1][0]); SCHED();
    v4u xr; ld16(xr, l16, mkptr(ka[0], ka[1]) + gtok * 1024);
    Wh[1][2] = tr_read(cw0 + 12800);
    SCHED();
    PRIO(0);
    SCHED();
    ac[0][0][1] = mma32(X[1][0], cat(Wl[0][3], Wh[0][3]), ac[0][0][1]); SCHED();
    Wl[1][3] = tr_read(cw1 + 12288);
    SCHED();
    SCHED();
    va = pinu(xo0 + kX[2]); setm0<81920>(lw2); SCHED();
    ac[0][1][1] = mma32(X[1][1], cat(Wl[0][3], Wh[0][3]), ac[0][1][1]); SCHED();
    dma(va, xw);
    Wh[1][3] = tr_read(cw1 + 12800);
    SCHED();
    PRIO(3);
    SCHED();
    WAITC(9, 15);
    BAR(); SCHED();
    ac[1][0][0] = mma32(X[0][0], cat(Wl[1][0], Wh[1][0]), zero4); SCHED();
    Wl[0][0] = tr_read(cw0 + 16384);
    SCHED();
    SCHED();
    va = pinu(xo1 + kX[2]); setm0<82944>(lw2); SCHED();
    ac[1][1][0] = mma32(X[0][1], cat(Wl[1][0], Wh[1][0]), zero4); SCHED();
    dma(va, xw);
    Wh[0][0] = tr_read(cw0 + 16896);
    SCHED();
    PRIO(2);
    SCHED();
    ac[1][0][1] = mma32(X[0][0], cat(Wl[1][1], Wh[1][1]), zero4); SCHED();
    Wl[0][1] = tr_read(cw1 + 16384);
    SCHED();
    SCHED();
    va = pinu(vo1 + kQ[2]); setm0<98304>(lw1); SCHED();
    ac[1][1][1] = mma32(X[0][1], cat(Wl[1][1], Wh[1][1]), zero4); SCHED();
    dma(va, b1A);
    Wh[0][1] = tr_read(cw1 + 16896);
    SCHED();
    PRIO(1);
    SCHED();
    ac[1][0][0] = mma32(X[1][0], cat(Wl[1][2], Wh[1][2]), ac[1][0][0]); SCHED();
    Wl[0][2] = tr_read(cw0 + 20480);
    SCHED();
    SCHED();
    setm0<106496>(lw1); SCHED();
    ac[1][1][0] = mma32(X[1][1], cat(Wl[1][2], Wh[1][2]), ac[1][1][0]); SCHED();
    dma(va, b1B);
    Wh[0][2] = tr_read(cw0 + 20992);
    SCHED();
    PRIO(0);
    SCHED();
    ac[1][0][1] = mma32(X[1][0], cat(Wl[1][3], Wh[1][3]), ac[1][0][1]); SCHED();
    Wl[0][3] = tr_read(cw1 + 20480);
    SCHED();
    SCHED();
    va = pinu(vo2 + kF[2]); setm0<114688>(lw1); SCHED();
    ac[1][1][1] = mma32(X[1][1], cat(Wl[1][3], Wh[1][3]), ac[1][1][1]); SCHED();
    dma(va, b2A);
    Wh[0][3] = tr_read(cw1 + 20992);
    SCHED();
    PRIO(3);
    SCHED();
    WAITC(8, 15);
    BAR(); SCHED();
    ac[3][0][0] = mma32(cat(Wl[0][0], Wh[0][0]), X[0][0], zero4); SCHED();
    Wl[1][0] = tr_read(cw0 + 40960);
    SCHED();
    X[2][0] = *(LAS v4u*)(cx0 + 40960);
    SCHED();
    SCHED();
    va = pinu(xo0 + kX[3]); setm0<122880>(lw2); SCHED();
    ac[3][1][0] = mma32(cat(Wl[0][0], Wh[0][0]), X[0][1], zero4); SCHED();
    dma(va, xw);
    Wh[1][0] = tr_read(cw0 + 41472);
    SCHED();
    X[2][1] = *(LAS v4u*)(cx0 + 43008);
    SCHED();
    SCHED();
    ac[3][0][1] = mma32(cat(Wl[0][1], Wh[0][1]), X[0][0], zero4); SCHED();
    Wl[1][1] = tr_read(cw1 + 40960);
    SCHED();
    SCHED();
    va = pinu(xo1 + kX[3]); setm0<123904>(lw2); SCHED();
    ac[3][1][1] = mma32(cat(Wl[0][1], Wh[0][1]), X[0][1], zero4); SCHED();
    dma(va, xw);
    Wh[1][1] = tr_read(cw1 + 41472);
    SCHED();
    SCHED();
    ac[3][0][0] = mma32(cat(Wl[0][2], Wh[0][2]), X[1][0], ac[3][0][0]); SCHED();
    Wl[1][2] = tr_read(cw0 + 45056);
    SCHED();
    X[3][0] = *(LAS v4u*)(cx1 + 40960);
    SCHED();
    SCHED();
    va = pinu(vo1 + kQ[3]); setm0<139264>(lw1); SCHED();
    ac[3][1][0] = mma32(cat(Wl[0][2], Wh[0][2]), X[1][1], ac[3][1][0]); SCHED();
    dma(va, b1A);
    Wh[1][2] = tr_read(cw0 + 45568);
    SCHED();
    X[3][1] = *(LAS v4u*)(cx1 + 43008);
    SCHED();
    PRIO(2);
    SCHED();
    ac[3][0][1] = mma32(cat(Wl[0][3], Wh[0][3]), X[1][0], ac[3][0][1]); SCHED();
    Wl[1][3] = tr_read(cw1 + 45056);
    SCHED();
    SCHED();
    setm0<147456>(lw1); SCHED();
    ac[3][1][1] = mma32(cat(Wl[0][3], Wh[0][3]), X[1][1], ac[3][1][1]); SCHED();
    dma(va, b1B);
    Wh[1][3] = tr_read(cw1 + 45568);
    SCHED();
    SCHED();
    ac[0][0][0] = mma32(X[2][0], cat(Wl[1][0], Wh[1][0]), ac[0][0][0]); SCHED();
    Wl[0][0] = tr_read(cw0 + 49152);
    SCHED();
    SCHED();
    ac[0][1][0] = mma32(X[2][1], cat(Wl[1][0], Wh[1][0]), ac[0][1][0]); SCHED();
    Wh[0][0] = tr_read(cw0 + 49664);
    SCHED();
    SCHED();
    va = pinu(vo2 + kF[3]); setm0<155648>(lw1); SCHED();
    ac[0][0][1] = mma32(X[2][0], cat(Wl[1][1], Wh[1][1]), ac[0][0][1]); SCHED();
    dma(va, b2A);
    Wl[0][1] = tr_read(cw1 + 49152);
    SCHED();
    SCHED();
    ac[0][1][1] = mma32(X[2][1], cat(Wl[1][1], Wh[1][1]), ac[0][1][1]); SCHED();
    Wh[0][1] = tr_read(cw1 + 49664);
    SCHED();
    PRIO(1);
    SCHED();
    ac[0][0][0] = mma32(X[3][0], cat(Wl[1][2], Wh[1][2]), ac[0][0][0]); SCHED();
    Wl[0][2] = tr_read(cw0 + 53248);
    SCHED();
    SCHED();
    ac[0][1][0] = mma32(X[3][1], cat(Wl[1][2], Wh[1][2]), ac[0][1][0]); SCHED();
    Wh[0][2] = tr_read(cw0 + 53760);
    SCHED();
    SCHED();
    va = pinu(xo0 + kX[4]); setm0<0>(lw2); SCHED();
    ac[0][0][1] = mma32(X[3][0], cat(Wl[1][3], Wh[1][3]), ac[0][0][1]); SCHED();
    dma(va, xw);
    Wl[0][3] = tr_read(cw1 + 53248);
    SCHED();
    SCHED();
    ac[0][1][1] = mma32(X[3][1], cat(Wl[1][3], Wh[1][3]), ac[0][1][1]); SCHED();
    Wh[0][3] = tr_read(cw1 + 53760);
    SCHED();
    SCHED();
    STH(2, 1, 98, 96);
    ac[1][0][0] = mma32(X[2][0], cat(Wl[0][0], Wh[0][0]), ac[1][0][0]); SCHED();
    Wl[1][0] = tr_read(cw0 + 57344);
    SCHED();
    SCHED();
    ac[1][1][0] = mma32(X[2][1], cat(Wl[0][0], Wh[0][0]), ac[1][1][0]); SCHED();
    Wh[1][0] = tr_read(cw0 + 57856);
    SCHED();
    PRIO(0);
    SCHED();
    va = pinu(xo1 + kX[4]); setm0<1024>(lw2); SCHED();
    ac[1][0][1] = mma32(X[2][0], cat(Wl[0][1], Wh[0][1]), ac[1][0][1]); SCHED();
    dma(va, xw);
    Wl[1][1] = tr_read(cw1 + 57344);
    SCHED();
    SCHED();
    ac[1][1][1] = mma32(X[2][1], cat(Wl[0][1], Wh[0][1]), ac[1][1][1]); SCHED();
    Wh[1][1] = tr_read(cw1 + 57856);
    SCHED();
    cx0 = pinp(cx0 + (81920));
    SCHED();
    SCHED();
    ac[1][0][0] = mma32(X[3][0], cat(Wl[0][2], Wh[0][2]), ac[1][0][0]); SCHED();
    Wl[1][2] = tr_read(cw0 + 61440);
    SCHED();
    SCHED();
    ac[1][1][0] = mma32(X[3][1], cat(Wl[0][2], Wh[0][2]), ac[1][1][0]); SCHED();
    Wh[1][2] = tr_read(cw0 + 61952);
    SCHED();
    cx1 = pinp(cx1 + (81920));
    SCHED();
    SCHED();
    va = pinu(vo1 + kQ[4]); setm0<16384>(lw1); SCHED();
    ac[1][0][1] = mma32(X[3][0], cat(Wl[0][3], Wh[0][3]), ac[1][0][1]); SCHED();
    dma(va, b1A);
    Wl[1][3] = tr_read(cw1 + 61440);
    SCHED();
    cw0 = pinp(cw0 + (81920));
    SCHED();
    SCHED();
    ac[1][1][1] = mma32(X[3][1], cat(Wl[0][3], Wh[0][3]), ac[1][1][1]); SCHED();
    Wh[1][3] = tr_read(cw1 + 61952);
    SCHED();
    PRIO(3);
    SCHED();
    WAITC(8, 15);
    BAR(); SCHED();
    ac[3][0][0] = mma32(cat(Wl[1][0], Wh[1][0]), X[2][0], ac[3][0][0]); SCHED();
    Wl[0][0] = tr_read(cw0 + 0);
    SCHED();
    X[4][0] = *(LAS v4u*)(cx0 + 0);
    SCHED();
    cw1 = pinp(cw1 + (81920));
    SCHED();
    asm volatile("" : "+v"(nn), "+v"(gr), "+v"(xr) :: "memory");
    SCHED();
    const unsigned tag = __builtin_amdgcn_readfirstlane(nn) + 0x9E3779B9u;
    SCHED();
    SCHED();
    ac[3][1][0] = mma32(cat(Wl[1][0], Wh[1][0]), X[2][1], ac[3][1][0]); SCHED();
    Wh[0][0] = tr_read(cw0 + 512);
    SCHED();
    X[4][1] = *(LAS v4u*)(cx0 + 2048);
    SCHED();
    float gd = 0.f;
    SCHED();
    float ga0 = pin(bflo(xr[0]));
    SCHED();
    SCHED();
    setm0<24576>(lw1); SCHED();
    ac[3][0][1] = mma32(cat(Wl[1][1], Wh[1][1]), X[2][0], ac[3][0][1]); SCHED();
    dma(va, b1B);
    Wl[0][1] = tr_read(cw1 + 0);
    SCHED();
    float gb0 = pin(bflo(gr[0]));
    SCHED();
    gd = pin(__builtin_fmaf(ga0, gb0, gd));
    SCHED();
    SCHED();
    ac[3][1][1] = mma32(cat(Wl[1][1], Wh[1][1]), X[2][1], ac[3][1][1]); SCHED();
    Wh[0][1] = tr_read(cw1 + 512);
    SCHED();
    float ga1 = pin(bfhi(xr[0]));
    SCHED();
    float gb1 = pin(bfhi(gr[0]));
    SCHED();
    SCHED();
    ac[3][0][0] = mma32(cat(Wl[1][2], Wh[1][2]), X[3][0], ac[3][0][0]); SCHED();
    Wl[0][2] = tr_read(cw0 + 4096);
    SCHED();
    X[5][0] = *(LAS v4u*)(cx1 + 0);
    SCHED();
    gd = pin(__builtin_fmaf(ga1, gb1, gd));
    SCHED();
    float ga2 = pin(bflo(xr[1]));
    SCHED();
    SCHED();
    ac[3][1][0] = mma32(cat(Wl[1][2], Wh[1][2]), X[3][1], ac[3][1][0]); SCHED();
    Wh[0][2] = tr_read(cw0 + 4608);
    SCHED();
    X[5][1] = *(LAS v4u*)(cx1 + 2048);
    SCHED();
    float gb2 = pin(bflo(gr[1]));
    SCHED();
    gd = pin(__builtin_fmaf(ga2, gb2, gd));
    SCHED();
    PRIO(2);
    SCHED();
    va = pinu(xo0 + kX[5]); setm0<32768>(lw2); SCHED();
    ac[3][0][1] = mma32(cat(Wl[1][3], Wh[1][3]), X[3][0], ac[3][0][1]); SCHED();
    dma(va, xw);
    Wl[0][3] = tr_read(cw1 + 4096);
    SCHED();
    float ga3 = pin(bfhi(xr[1]));
    SCHED();
    float gb3 = pin(bfhi(gr[1]));
    SCHED();
    SCHED();
    ac[3][1][1] = mma32(cat(Wl[1][3], Wh[1][3]), X[3][1], ac[3][1][1]); SCHED();
    Wh[0][3] = tr_read(cw1 + 4608);
    SCHED();
    gd = pin(__builtin_fmaf(ga3, gb3, gd));
    SCHED();
    float ga4 = pin(bflo(xr[2]));
    SCHED();
    SCHED();
    ac[0][0][0] = mma32(X[4][0], cat(Wl[0][0], Wh[0][0]), ac[0][0][0]); SCHED();
    Wl[1][0] = tr_read(cw0 + 8192);
    SCHED();
    float gb4 = pin(bflo(gr[2]));
    SCHED();
    gd = pin(__builtin_fmaf(ga4, gb4, gd));
    SCHED();
    lane = lane_id(); FRESH(); vp = pinu(((bid * 8 + wave) * 7 + (lane * 7 >> 6)) * 128);
    SCHED();
    SCHED();
    ac[0][1][0] = mma32(X[4][1], cat(Wl[0][0], Wh[0][0]), ac[0][1][0]); SCHED();
    Wh[1][0] = tr_read(cw0 + 8704);
    SCHED();
    float ga5 = pin(bfhi(xr[2]));
    SCHED();
    float gb5 = pin(bfhi(gr[2]));
    SCHED();
    unsigned pma; ld4(pma, vp, mkptr(ka[6], ka[7]));
    SCHED();
    SCHED();
    va = pinu(xo1 + kX[5]); setm0<33792>(lw2); SCHED();
    ac[0][0][1] = mma32(X[4][0], cat(Wl[0][1], Wh[0][1]), ac[0][0][1]); SCHED();
    dma(va, xw);
    Wl[1][1] = tr_read(cw1 + 8192);
    SCHED();
    gd = pin(__builtin_fmaf(ga5, gb5, gd));
    SCHED();
    float ga6 = pin(bflo(xr[3]));
    SCHED();
    SCHED();
    ac[0][1][1] = mma32(X[4][1], cat(Wl[0][1], Wh[0][1]), ac[0][1][1]); SCHED();
    Wh[1][1] = tr_read(cw1 + 8704);
    SCHED();
    float gb6 = pin(bflo(gr[3]));
    SCHED();
    gd = pin(__builtin_fmaf(ga6, gb6, gd));
    SCHED();
    PRIO(1);
    SCHED();
    ac[0][0][0] = mma32(X[5][0], cat(Wl[0][2], Wh[0][2]), ac[0][0][0]); SCHED();
    Wl[1][2] = tr_read(cw0 + 12288);
    SCHED();
    float ga7 = pin(bfhi(xr[3]));
    SCHED();
    float gb7 = pin(bfhi(gr[3]));
    SCHED();
    SCHED();
    ac[0][1][0] = mma32(X[5][1], cat(Wl[0][2], Wh[0][2]), ac[0][1][0]); SCHED();
    Wh[1][2] = tr_read(cw0 + 12800);
    SCHED();
    gd = pin(__builtin_fmaf(ga7, gb7, gd));
    SCHED();
    gd = pin(dpp_add<0x111, 0xf>(gd));
    SCHED();
    unsigned pmb; ld4(pmb, vp, mkptr(ka[8], ka[9]));
    SCHED();
    SCHED();
    va = pinu(vo1 + kQ[5]); setm0<49152>(lw1); SCHED();
    ac[0][0][1] = mma32(X[5][0], cat(Wl[0][3], Wh[0][3]), ac[0][0][1]); SCHED();
    dma(va, b1A);
    Wl[1][3] = tr_read(cw1 + 12288);
    SCHED();
    gd = pin(dpp_add<0x112, 0xf>(gd));
    SCHED();
    SCHED();
    ac[0][1][1] = mma32(X[5][1], cat(Wl[0][3], Wh[0][3]), ac[0][1][1]); SCHED();
    Wh[1][3] = tr_read(cw1 + 12800);
    SCHED();
    gd = pin(dpp_add<0x114, 0xf>(gd));
    SCHED();
    SCHED();
    ac[1][0][0] = mma32(X[4][0], cat(Wl[1][0], Wh[1][0]), ac[1][0][0]); SCHED();
    Wl[0][0] = tr_read(cw0 + 16384);
    SCHED();
    gd = pin(dpp_add<0x118, 0xf>(gd));
    SCHED();
    SCHED();
    ac[1][1][0] = mma32(X[4][1], cat(Wl[1][0], Wh[1][0]), ac[1][1][0]); SCHED();
    Wh[0][0] = tr_read(cw0 + 16896);
    SCHED();
    gd = pin(dpp_add<0x142, 0xa>(gd));
    SCHED();
    unsigned pmc; ld4(pmc, vp, mkptr(ka[10], ka[11]));
    SCHED();
    PRIO(0);
    SCHED();
    setm0<57344>(lw1); SCHED();
    ac[1][0][1] = mma32(X[4][0], cat(Wl[1][1], Wh[1][1]), ac[1][0][1]); SCHED();
    dma(va, b1B);
    Wl[0][1] = tr_read(cw1 + 16384);
    SCHED();
    gd = pin(dpp_add<0x143, 0xc>(gd));
    SCHED();
    SCHED();
    ac[1][1][1] = mma32(X[4][1], cat(Wl[1][1], Wh[1][1]), ac[1][1][1]); SCHED();
    Wh[0][1] = tr_read(cw1 + 16896);
    SCHED();
    __builtin_amdgcn_raw_buffer_store_b64((v2u){f2u(gd), tag}, wsr, (l16 ^ 1008) << 22, OFF_G + gtok * 8, SC1);
    SCHED();
    SCHED();
    ac[1][0][0] = mma32(X[5][0], cat(Wl[1][2], Wh[1][2]), ac[1][0][0]); SCHED();
    Wl[0][2] = tr_read(cw0 + 20480);
    SCHED();
    SCHED();
    ac[1][1][0] = mma32(X[5][1], cat(Wl[1][2], Wh[1][2]), ac[1][1][0]); SCHED();
    Wh[0][2] = tr_read(cw0 + 20992);
    SCHED();
    lane = lane_id(); FRESH(); vp = pinu(((bid * 8 + wave) * 2 + (lane >> 5)) * 128);
    SCHED();
    SCHED();
    ac[1][0][1] = mma32(X[5][0], cat(Wl[1][3], Wh[1][3]), ac[1][0][1]); SCHED();
    Wl[0][3] = tr_read(cw1 + 20480);
    SCHED();
    unsigned pmd; ld4(pmd, vp, mkptr(ka[4], ka[5]));
    SCHED();
    SCHED();
    ac[1][1][1] = mma32(X[5][1], cat(Wl[1][3], Wh[1][3]), ac[1][1][1]); SCHED();
    Wh[0][3] = tr_read(cw1 + 20992);
    SCHED();
    PRIO(3);
    SCHED();
    WAITC(13, 15);
    BAR(); SCHED();
    STH(3, 2, 96, 98);
    ac[3][0][0] = mma32(cat(Wl[0][0], Wh[0][0]), X[4][0], ac[3][0][0]); SCHED();
    Wl[1][0] = tr_read(cw0 + 40960);
    SCHED();
    X[6][0] = *(LAS v4u*)(cx0 + 40960);
    SCHED();
    SCHED();
    ac[3][1][0] = mma32(cat(Wl[0][0], Wh[0][0]), X[4][1], ac[3][1][0]); SCHED();
    Wh[1][0] = tr_read(cw0 + 41472);
    SCHED();
    X[6][1] = *(LAS v4u*)(cx0 + 43008);
    SCHED();
    SCHED();
    va = pinu(xo0 + kX[6]); setm0<65536>(lw2); SCHED();
    ac[3][0][1] = mma32(cat(Wl[0][1], Wh[0][1]), X[4][0], ac[3][0][1]); SCHED();
    dma(va, xw);
    Wl[1][1] = tr_read(cw1 + 40960);
    SCHED();
    lane = lane_id(); FRESH(); vp = pinu((bid * 2 + (wave >> 2)) * 3072 + ((wave & 3) * 2 + (lane >> 5)) * 128);
    SCHED();
    SCHED();
    ac[3][1][1] = mma32(cat(Wl[0][1], Wh[0][1]), X[4][1], ac[3][1][1]); SCHED();
    Wh[1][1] = tr_read(cw1 + 41472);
    SCHED();
    unsigned pme; ld4(pme, vp, mkptr(ka[2], ka[3]));
    SCHED();
    SCHED();
    ac[3][0][0] = mma32(cat(Wl[0][2], Wh[0][2]), X[5][0], ac[3][0][0]); SCHED();
    Wl[1][2] = tr_read(cw0 + 45056);
    SCHED();
    X[7][0] = *(LAS v4u*)(cx1 + 40960);
    SCHED();
    SCHED();
    ac[3][1][0] = mma32(cat(Wl[0][2], Wh[0][2]), X[5][1], ac[3][1][0]); SCHED();
    Wh[1][2] = tr_read(cw0 + 45568);
    SCHED();
    X[7][1] = *(LAS v4u*)(cx1 + 43008);
    SCHED();
    PRIO(2);
    SCHED();
    va = pinu(xo1 + kX[6]); setm0<66560>(lw2); SCHED();
    ac[3][0][1] = mma32(cat(Wl[0][3], Wh[0][3]), X[5][0], ac[3][0][1]); SCHED();
    dma(va, xw);
    Wl[1][3] = tr_read(cw1 + 45056);
    SCHED();
    SCHED();
    ac[3][1][1] = mma32(cat(Wl[0][3], Wh[0][3]), X[5][1], ac[3][1][1]); SCHED();
    Wh[1][3] = tr_read(cw1 + 45568);
    SCHED();
    SCHED();
    ac[0][0][0] = mma32(X[6][0], cat(Wl[1][0], Wh[1][0]), ac[0][0][0]); SCHED();
    Wl[0][0] = tr_read(cw0 + 49152);
    SCHED();
    SCHED();
    ac[0][1][0] = mma32(X[6][1], cat(Wl[1][0], Wh[1][0]), ac[0][1][0]); SCHED();
    Wh[0][0] = tr_read(cw0 + 49664);
    SCHED();
    SCHED();
    va = pinu(vo1 + kQ[6]); setm0<81920>(lw1); SCHED();
    ac[0][0][1] = mma32(X[6][0], cat(Wl[1][1], Wh[1][1]), ac[0][0][1]); SCHED();
    dma(va, b1A);
    Wl[0][1] = tr_read(cw1 + 49152);
    SCHED();
    FRESH(); const unsigned wg = pins<unsigned>(wave >> 1); char* h2d = pins(hdst + 2 * 4096); char* h3d = pins(hdst + 7 * 4096);
    SCHED();
    SCHED();
    ac[0][1][1] = mma32(X[6][1], cat(Wl[1][1], Wh[1][1]), ac[0][1][1]); SCHED();
    Wh[0][1] = tr_read(cw1 + 49664);
    SCHED();
    PRIO(1);
    SCHED();
    ac[0][0][0] = mma32(X[7][0], cat(Wl[1][2], Wh[1][2]), ac[0][0][0]); SCHED();
    Wl[0][2] = tr_read(cw0 + 53248);
    SCHED();
    SCHED();
    ac[0][1][0] = mma32(X[7][1], cat(Wl[1][2], Wh[1][2]), ac[0][1][0]); SCHED();
    Wh[0][2] = tr_read(cw0 + 53760);
    SCHED();
    SCHED();
    setm0<90112>(lw1); SCHED();
    ac[0][0][1] = mma32(X[7][0], cat(Wl[1][3], Wh[1][3]), ac[0][0][1]); SCHED();
    dma(va, b1B);
    Wl[0][3] = tr_read(cw1 + 53248);
    SCHED();
    SCHED();
    ac[0][1][1] = mma32(X[7][1], cat(Wl[1][3], Wh[1][3]), ac[0][1][1]); SCHED();
    Wh[0][3] = tr_read(cw1 + 53760);
    SCHED();
    SCHED();
    ac[1][0][0] = mma32(X[6][0], cat(Wl[0][0], Wh[0][0]), ac[1][0][0]); SCHED();
    Wl[1][0] = tr_read(cw0 + 57344);
    SCHED();
    SCHED();
    ac[1][1][0] = mma32(X[6][1], cat(Wl[0][0], Wh[0][0]), ac[1][1][0]); SCHED();
    Wh[1][0] = tr_read(cw0 + 57856);
    SCHED();
    PRIO(0);
    SCHED();
    va = pinu(xo0 + kX[7]); setm0<98304>(lw2); SCHED();
    ac[1][0][1] = mma32(X[6][0], cat(Wl[0][1], Wh[0][1]), ac[1][0][1]); SCHED();
    dma(va, xw);
    Wl[1][1] = tr_read(cw1 + 57344);
    SCHED();
    SCHED();
    ac[1][1][1] = mma32(X[6][1], cat(Wl[0][1], Wh[0][1]), ac[1][1][1]); SCHED();
    Wh[1][1] = tr_read(cw1 + 57856);
    SCHED();
    cx0 = pinp(cx0 + (-81920));
    SCHED();
    SCHED();
    ac[1][0][0] = mma32(X[7][0], cat(Wl[0][2], Wh[0][2]), ac[1][0][0]); SCHED();
    Wl[1][2] = tr_read(cw0 + 61440);
    SCHED();
    SCHED();
    ac[1][1][0] = mma32(X[7][1], cat(Wl[0][2], Wh[0][2]), ac[1][1][0]); SCHED();
    Wh[1][2] = tr_read(cw0 + 61952);
    SCHED();
    cx1 = pinp(cx1 + (-81920));
    SCHED();
    SCHED();
    va = pinu(xo1 + kX[7]); setm0<99328>(lw2); SCHED();
    ac[1][0][1] = mma32(X[7][0], cat(Wl[0][3], Wh[0][3]), ac[1][0][1]); SCHED();
    dma(va, xw);
    Wl[1][3] = tr_read(cw1 + 61440);
    SCHED();
    cw0 = pinp(cw0 + (-81920));
    SCHED();
    SCHED();
    ac[1][1][1] = mma32(X[7][1], cat(Wl[0][3], Wh[0][3]), ac[1][1][1]); SCHED();
    Wh[1][3] = tr_read(cw1 + 61952);
    SCHED();
    PRIO(3);
    SCHED();
    WAITC(11, 15);
    asm volatile("" :: "v"(pma));
    asm volatile("" :: "v"(pmb));
    BAR(); SCHED();
    ac[3][0][0] = mma32(cat(Wl[1][0], Wh[1][0]), X[6][0], ac[3][0][0]); SCHED();
    Wl[0][0] = tr_read(cw0 + 0);
    SCHED();
    X[8][0] = *(LAS v4u*)(cx0 + 0);
    SCHED();
    cw1 = pinp(cw1 + (-81920));
    SCHED();
    SCHED();
    ac[3][1][0] = mma32(cat(Wl[1][0], Wh[1][0]), X[6][1], ac[3][1][0]); SCHED();
    Wh[0][0] = tr_read(cw0 + 512);
    SCHED();
    X[8][1] = *(LAS v4u*)(cx0 + 2048);
    SCHED();
    SCHED();
    va = pinu(vo1 + kQ[7]); setm0<114688>(lw1); SCHED();
    ac[3][0][1] = mma32(cat(Wl[1][1], Wh[1][1]), X[6][0], ac[3][0][1]); SCHED();
    dma(va, b1A);
    Wl[0][1] = tr_read(cw1 + 0);
    SCHED();
    SCHED();
    ac[3][1][1] = mma32(cat(Wl[1][1], Wh[1][1]), X[6][1], ac[3][1][1]); SCHED();
    Wh[0][1] = tr_read(cw1 + 512);
    SCHED();
    SCHED();
    ac[3][0][0] = mma32(cat(Wl[1][2], Wh[1][2]), X[7][0], ac[3][0][0]); SCHED();
    Wl[0][2] = tr_read(cw0 + 4096);
    SCHED();
    X[9][0] = *(LAS v4u*)(cx1 + 0);
    SCHED();
    SCHED();
    ac[3][1][0] = mma32(cat(Wl[1][2], Wh[1][2]), X[7][1], ac[3][1][0]); SCHED();
    Wh[0][2] = tr_read(cw0 + 4608);
    SCHED();
    X[9][1] = *(LAS v4u*)(cx1 + 2048);
    SCHED();
    PRIO(2);
    SCHED();
    setm0<122880>(lw1); SCHED();
    ac[3][0][1] = mma32(cat(Wl[1][3], Wh[1][3]), X[7][0], ac[3][0][1]); SCHED();
    dma(va, b1B);
    Wl[0][3] = tr_read(cw1 + 4096);
    SCHED();
    SCHED();
    ac[3][1][1] = mma32(cat(Wl[1][3], Wh[1][3]), X[7][1], ac[3][1][1]); SCHED();
    Wh[0][3] = tr_read(cw1 + 4608);
    SCHED();
    SCHED();
    STH(4, 3, 98, 96);
    ac[0][0][0] = mma32(X[8][0], cat(Wl[0][0], Wh[0][0]), ac[0][0][0]); SCHED();
    Wl[1][0] = tr_read(cw0 + 8192);
    SCHED();
    SCHED();
    ac[0][1][0] = mma32(X[8][1], cat(Wl[0][0], Wh[0][0]), ac[0][1][0]); SCHED();
    Wh[1][0] = tr_read(cw0 + 8704);
    SCHED();
    SCHED();
    va = pinu(vo2 + kF[4]); setm0<131072>(lw1); SCHED();
    ac[0][0][1] = mma32(X[8][0], cat(Wl[0][1], Wh[0][1]), ac[0][0][1]); SCHED();
    dma(va, b2A);
    Wl[1][1] = tr_read(cw1 + 8192);
    SCHED();
    SCHED();
    ac[0][1][1] = mma32(X[8][1], cat(Wl[0][1], Wh[0][1]), ac[0][1][1]); SCHED();
    Wh[1][1] = tr_read(cw1 + 8704);
    SCHED();
    PRIO(1);
    SCHED();
    ac[0][0][0] = mma32(X[9][0], cat(Wl[0][2], Wh[0][2]), ac[0][0][0]); SCHED();
    Wl[1][2] = tr_read(cw0 + 12288);
    SCHED();
    SCHED();
    ac[0][1][0] = mma32(X[9][1], cat(Wl[0][2], Wh[0][2]), ac[0][1][0]); SCHED();
    Wh[1][2] = tr_read(cw0 + 12800);
    SCHED();
    SCHED();
    va = pinu(vo2 + kF[5]); setm0<139264>(lw1); SCHED();
    ac[0][0][1] = mma32(X[9][0], cat(Wl[0][3], Wh[0][3]), ac[0][0][1]); SCHED();
    dma(va, b2A);
    Wl[1][3] = tr_read(cw1 + 12288);
    SCHED();
    SCHED();
    ac[0][1][1] = mma32(X[9][1], cat(Wl[0][3], Wh[0][3]), ac[0][1][1]); SCHED();
    Wh[1][3] = tr_read(cw1 + 12800);
    SCHED();
    SCHED();
    ac[1][0][0] = mma32(X[8][0], cat(Wl[1][0], Wh[1][0]), ac[1][0][0]); SCHED();
    Wl[0][0] = tr_read(cw0 + 32768);
    SCHED();
    X[10][0] = *(LAS v4u*)(cx0 + 32768);
    SCHED();
    SCHED();
    ac[1][1][0] = mma32(X[8][1], cat(Wl[1][0], Wh[1][0]), ac[1][1][0]); SCHED();
    Wh[0][0] = tr_read(cw0 + 33280);
    SCHED();
    X[10][1] = *(LAS v4u*)(cx0 + 34816);
    SCHED();
    lane = lane_id(); FRESH(); const unsigned go = ((lane >> 5) * 16 + (lane & 15)) * 8; const char* gsrc = ws + (unsigned)(OFF_G + (TB * 128 + WM * 32) * 8);
    SCHED();
    PRIO(0);
    SCHED();
    va = pinu(vo2 + kF[6]); setm0<147456>(lw1); SCHED();
    ac[1][0][1] = mma32(X[8][0], cat(Wl[1][1], Wh[1][1]), ac[1][0][1]); SCHED();
    dma(va, b2A);
    Wl[0][1] = tr_read(cw1 + 32768);
    SCHED();
    v2u gg; ld8c(gg, go, gsrc);
    SCHED();
    SCHED();
    ac[1][1][1] = mma32(X[8][1], cat(Wl[1][1], Wh[1][1]), ac[1][1][1]); SCHED();
    Wh[0][1] = tr_read(cw1 + 33280);
    SCHED();
    SCHED();
    ac[1][0][0] = mma32(X[9][0], cat(Wl[1][2], Wh[1][2]), ac[1][0][0]); SCHED();
    Wl[0][2] = tr_read(cw0 + 36864);
    SCHED();
    X[11][0] = *(LAS v4u*)(cx1 + 32768);
    SCHED();
    SCHED();
    ac[1][1][0] = mma32(X[9][1], cat(Wl[1][2], Wh[1][2]), ac[1][1][0]); SCHED();
    Wh[0][2] = tr_read(cw0 + 37376);
    SCHED();
    X[11][1] = *(LAS v4u*)(cx1 + 34816);
    SCHED();
    SCHED();
    ac[1][0][1] = mma32(X[9][0], cat(Wl[1][3], Wh[1][3]), ac[1][0][1]); SCHED();
    Wl[0][3] = tr_read(cw1 + 36864);
    SCHED();
    SCHED();
    ac[1][1][1] = mma32(X[9][1], cat(Wl[1][3], Wh[1][3]), ac[1][1][1]); SCHED();
    Wh[0][3] = tr_read(cw1 + 37376);
    SCHED();
    PRIO(3);
    SCHED();
    WAITC(9, 15);
    asm volatile("" :: "v"(pmc));
    asm volatile("" :: "v"(pmd));
    asm volatile("" :: "v"(pme));
    BAR(); SCHED();
    ac[0][0][0] = mma32(X[10][0], cat(Wl[0][0], Wh[0][0]), ac[0][0][0]); SCHED();
    Wl[1][0] = tr_read(cw0 + 40960);
    SCHED();
    SCHED();
    ac[0][1][0] = mma32(X[10][1], cat(Wl[0][0], Wh[0][0]), ac[0][1][0]); SCHED();
    Wh[1][0] = tr_read(cw0 + 41472);
    SCHED();
    SCHED();
    va = pinu(vo2 + kF[7]); setm0<155648>(lw1); SCHED();
    ac[0][0][1] = mma32(X[10][0], cat(Wl[0][1], Wh[0][1]), ac[0][0][1]); SCHED();
    dma(va, b2A);
    Wl[1][1] = tr_read(cw1 + 40960);
    SCHED();
    SCHED();
    ac[0][1][1] = mma32(X[10][1], cat(Wl[0][1], Wh[0][1]), ac[0][1][1]); SCHED();
    Wh[1][1] = tr_read(cw1 + 41472);
    SCHED();
    cx0 = pinp(cx0 + (65536));
    SCHED();
    PRIO(2);
    SCHED();
    ac[0][0][0] = mma32(X[11][0], cat(Wl[0][2], Wh[0][2]), ac[0][0][0]); SCHED();
    Wl[1][2] = tr_read(cw0 + 45056);
    SCHED();
    SCHED();
    ac[0][1][0] = mma32(X[11][1], cat(Wl[0][2], Wh[0][2]), ac[0][1][0]); SCHED();
    Wh[1][2] = tr_read(cw0 + 45568);
    SCHED();
    cx1 = pinp(cx1 + (65536));
    SCHED();
    SCHED();
    va = pinu(vo2 + kF[0]); setm0<0>(lw1); SCHED();
    ac[0][0][1] = mma32(X[11][0], cat(Wl[0][3], Wh[0][3]), ac[0][0][1]); SCHED();
    dma(va, b2B);
    Wl[1][3] = tr_read(cw1 + 45056);
    SCHED();
    cw0 = pinp(cw0 + (65536));
    SCHED();
    SCHED();
    ac[0][1][1] = mma32(X[11][1], cat(Wl[0][3], Wh[0][3]), ac[0][1][1]); SCHED();
    Wh[1][3] = tr_read(cw1 + 45568);
    SCHED();
    PRIO(1);
    SCHED();
    ac[1][0][0] = mma32(X[10][0], cat(Wl[1][0], Wh[1][0]), ac[1][0][0]); SCHED();
    Wl[0][0] = tr_read(cw0 + 0);
    SCHED();
    X[12][0] = *(LAS v4u*)(cx0 + 0);
    SCHED();
    cw1 = pinp(cw1 + (65536));
    SCHED();
    SCHED();
    ac[1][1][0] = mma32(X[10][1], cat(Wl[1][0], Wh[1][0]), ac[1][1][0]); SCHED();
    Wh[0][0] = tr_read(cw0 + 512);
    SCHED();
    X[12][1] = *(LAS v4u*)(cx0 + 2048);
    SCHED();
    SCHED();
    va = pinu(vo2 + kF[1]); setm0<8192>(lw1); SCHED();
    ac[1][0][1] = mma32(X[10][0], cat(Wl[1][1], Wh[1][1]), ac[1][0][1]); SCHED();
    dma(va, b2B);
    Wl[0][1] = tr_read(cw1 + 0);
    SCHED();
    SCHED();
    ac[1][1][1] = mma32(X[10][1], cat(Wl[1][1], Wh[1][1]), ac[1][1][1]); SCHED();
    Wh[0][1] = tr_read(cw1 + 512);
    SCHED();
    PRIO(0);
    SCHED();
    ac[1][0][0] = mma32(X[11][0], cat(Wl[1][2], Wh[1][2]), ac[1][0][0]); SCHED();
    Wl[0][2] = tr_read(cw0 + 4096);
    SCHED();
    X[13][0] = *(LAS v4u*)(cx1 + 0);
    SCHED();
    SCHED();
    ac[1][1][0] = mma32(X[11][1], cat(Wl[1][2], Wh[1][2]), ac[1][1][0]); SCHED();
    Wh[0][2] = tr_read(cw0 + 4608);
    SCHED();
    X[13][1] = *(LAS v4u*)(cx1 + 2048);
    SCHED();
    SCHED();
    va = pinu(vo2 + kF[2]); setm0<16384>(lw1); SCHED();
    ac[1][0][1] = mma32(X[11][0], cat(Wl[1][3], Wh[1][3]), ac[1][0][1]); SCHED();
    dma(va, b2B);
    Wl[0][3] = tr_read(cw1 + 4096);
    SCHED();
    SCHED();
    ac[1][1][1] = mma32(X[11][1], cat(Wl[1][3], Wh[1][3]), ac[1][1][1]); SCHED();
    Wh[0][3] = tr_read(cw1 + 4608);
    SCHED();
    PRIO(3);
    SCHED();
    WAITC(9, 15);
    BAR(); SCHED();
    STH(5, 4, 96, 98);
    ac[0][0][0] = mma32(X[12][0], cat(Wl[0][0], Wh[0][0]), ac[0][0][0]); SCHED();
    Wl[1][0] = tr_read(cw0 + 8192);
    SCHED();
    lane = lane_id(); const unsigned w2o0 = ((((lane & 7) >> 1) ^ ((lane >> 4) & 1)) * 32) + (lane & 1) * 16 + (16 * (lane >> 5) + ((lane >> 3) & 3)) * 1024, w2o1 = w2o0 ^ 64;
    SCHED();
    SCHED();
    ac[0][1][0] = mma32(X[12][1], cat(Wl[0][0], Wh[0][0]), ac[0][1][0]); SCHED();
    Wh[1][0] = tr_read(cw0 + 8704);
    SCHED();
    SCHED();
    SCHED();
    va = pinu(vo2 + kF[3]); setm0<24576>(lw1); SCHED();
    ac[0][0][1] = mma32(X[12][0], cat(Wl[0][1], Wh[0][1]), ac[0][0][1]); SCHED();
    dma(va, b2B);
    Wl[1][1] = tr_read(cw1 + 8192);
    SCHED();
    SCHED();
    ac[0][1][1] = mma32(X[12][1], cat(Wl[0][1], Wh[0][1]), ac[0][1][1]); SCHED();
    Wh[1][1] = tr_read(cw1 + 8704);
    SCHED();
    PRIO(2);
    SCHED();
    ac[0][0][0] = mma32(X[13][0], cat(Wl[0][2], Wh[0][2]), ac[0][0][0]); SCHED();
    Wl[1][2] = tr_read(cw0 + 12288);
    SCHED();
    SCHED();
    ac[0][1][0] = mma32(X[13][1], cat(Wl[0][2], Wh[0][2]), ac[0][1][0]); SCHED();
    Wh[1][2] = tr_read(cw0 + 12800);
    SCHED();
    SCHED();
    va = pinu(vo2 + kF[4]); setm0<32768>(lw1); SCHED();
    ac[0][0][1] = mma32(X[13][0], cat(Wl[0][3], Wh[0][3]), ac[0][0][1]); SCHED();
    dma(va, b2B);
    Wl[1][3] = tr_read(cw1 + 12288);
    SCHED();
    SCHED();
    ac[0][1][1] = mma32(X[13][1], cat(Wl[0][3], Wh[0][3]), ac[0][1][1]); SCHED();
    Wh[1][3] = tr_read(cw1 + 12800);
    SCHED();
    PRIO(1);
    SCHED();
    ac[1][0][0] = mma32(X[12][0], cat(Wl[1][0], Wh[1][0]), ac[1][0][0]); SCHED();
    Wl[0][0] = tr_read(cw0 + 32768);
    SCHED();
    X[14][0] = *(LAS v4u*)(cx0 + 32768);
    SCHED();
    SCHED();
    ac[1][1][0] = mma32(X[12][1], cat(Wl[1][0], Wh[1][0]), ac[1][1][0]); SCHED();
    Wh[0][0] = tr_read(cw0 + 33280);
    SCHED();
    X[14][1] = *(LAS v4u*)(cx0 + 34816);
    SCHED();
    SCHED();
    va = pinu(vo2 + kF[5]); setm0<40960>(lw1); SCHED();
    ac[1][0][1] = mma32(X[12][0], cat(Wl[1][1], Wh[1][1]), ac[1][0][1]); SCHED();
    dma(va, b2B);
    Wl[0][1] = tr_read(cw1 + 32768);
    SCHED();
    SCHED();
    ac[1][1][1] = mma32(X[12][1], cat(Wl[1][1], Wh[1][1]), ac[1][1][1]); SCHED();
    Wh[0][1] = tr_read(cw1 + 33280);
    SCHED();
    PRIO(0);
    SCHED();
    ac[1][0][0] = mma32(X[13][0], cat(Wl[1][2], Wh[1][2]), ac[1][0][0]); SCHED();
    Wl[0][2] = tr_read(cw0 + 36864);
    SCHED();
    X[15][0] = *(LAS v4u*)(cx1 + 32768);
    SCHED();
    WAITC(7, 15);
    asm volatile("" : "+v"(gg) :: "memory");
    SCHED();
    g_retry(gg, go, gsrc, tag);
    SCHED();
    SCHED();
    SCHED();
    ac[1][1][0] = mma32(X[13][1], cat(Wl[1][2], Wh[1][2]), ac[1][1][0]); SCHED();
    Wh[0][2] = tr_read(cw0 + 37376);
    SCHED();
    X[15][1] = *(LAS v4u*)(cx1 + 34816);
    SCHED();
    float gx = pin(u2f(gg[0]) * L2E);
    SCHED();
    SCHED();
    va = pinu(vo2 + kF[6]); setm0<49152>(lw1); SCHED();
    ac[1][0][1] = mma32(X[13][0], cat(Wl[1][3], Wh[1][3]), ac[1][0][1]); SCHED();
    dma(va, b2B);
    Wl[0][3] = tr_read(cw1 + 36864);
    SCHED();
    SCHED();
    ac[1][1][1] = mma32(X[13][1], cat(Wl[1][3], Wh[1][3]), ac[1][1][1]); SCHED();
    Wh[0][3] = tr_read(cw1 + 37376);
    SCHED();
    PRIO(3);
    SCHED();
    BAR(); SCHED();
    ac[0][0][0] = mma32(X[14][0], cat(Wl[0][0], Wh[0][0]), ac[0][0][0]); SCHED();
    Wl[1][0] = tr_read(cw0 + 40960);
    SCHED();
    SCHED();
    ac[0][1][0] = mma32(X[14][1], cat(Wl[0][0], Wh[0][0]), ac[0][1][0]); SCHED();
    Wh[1][0] = tr_read(cw0 + 41472);
    SCHED();
    SCHED();
    va = pinu(vo2 + kF[7]); setm0<57344>(lw1); SCHED();
    ac[0][0][1] = mma32(X[14][0], cat(Wl[0][1], Wh[0][1]), ac[0][0][1]); SCHED();
    dma(va, b2B);
    Wl[1][1] = tr_read(cw1 + 40960);
    SCHED();
    SCHED();
    ac[0][1][1] = mma32(X[14][1], cat(Wl[0][1], Wh[0][1]), ac[0][1][1]); SCHED();
    Wh[1][1] = tr_read(cw1 + 41472);
    SCHED();
    PRIO(2);
    SCHED();
    ac[0][0][0] = mma32(X[15][0], cat(Wl[0][2], Wh[0][2]), ac[0][0][0]); SCHED();
    Wl[1][2] = tr_read(cw0 + 45056);
    SCHED();
    SCHED();
    ac[0][1][0] = mma32(X[15][1], cat(Wl[0][2], Wh[0][2]), ac[0][1][0]); SCHED();
    Wh[1][2] = tr_read(cw0 + 45568);
    SCHED();
    SCHED();
    const char* ws0 = pins(w2b + (un[0] << 15)); setm0<65536>(lw4); SCHED();
    ac[0][0][1] = mma32(X[15][0], cat(Wl[0][3], Wh[0][3]), ac[0][0][1]); SCHED();
    dmaw(w2o0, ws0);
    Wl[1][3] = tr_read(cw1 + 45056);
    SCHED();
    SCHED();
    ac[0][1][1] = mma32(X[15][1], cat(Wl[0][3], Wh[0][3]), ac[0][1][1]); SCHED();
    Wh[1][3] = tr_read(cw1 + 45568);
    SCHED();
    v4u pa0, pa1, vb0, vb1; const float l2e = pin(L2E);
    SCHED();
    PRIO(1);
    SCHED();
    ac[1][0][0] = mma32(X[14][0], cat(Wl[1][0], Wh[1][0]), ac[1][0][0]); SCHED();
    Wl[0][0] = tr_read(cw0 + 49152);
    SCHED();
    SCHED();
    ac[1][1][0] = mma32(X[14][1], cat(Wl[1][0], Wh[1][0]), ac[1][1][0]); SCHED();
    Wh[0][0] = tr_read(cw0 + 49664);
    SCHED();
    float fkt0 = pin(__builtin_fmaf(ac[0][0][0][0], l2e, -1e-6f * L2E));
    SCHED();
    SCHED();
    ws0 = pins(ws0 + 4096); setm0<66560>(lw4); SCHED();
    ac[1][0][1] = mma32(X[14][0], cat(Wl[1][1], Wh[1][1]), ac[1][0][1]); SCHED();
    dmaw(w2o1, ws0);
    Wl[0][1] = tr_read(cw1 + 49152);
    SCHED();
    SCHED();
    ac[1][1][1] = mma32(X[14][1], cat(Wl[1][1], Wh[1][1]), ac[1][1][1]); SCHED();
    Wh[0][1] = tr_read(cw1 + 49664);
    SCHED();
    fkt0 = pin(__builtin_amdgcn_fmed3f(ex2(fkt0), 0.f, 1.f));
    SCHED();
    float fkt1 = pin(__builtin_fmaf(ac[0][0][0][1], l2e, -1e-6f * L2E));
    SCHED();
    PRIO(0);
    SCHED();
    ac[1][0][0] = mma32(X[15][0], cat(Wl[1][2], Wh[1][2]), ac[1][0][0]); SCHED();
    Wl[0][2] = tr_read(cw0 + 53248);
    SCHED();
    float fkp0 = vmax(ac[0][0][0][0], 1e-6f);
    SCHED();
    SCHED();
    ac[1][1][0] = mma32(X[15][1], cat(Wl[1][2], Wh[1][2]), ac[1][1][0]); SCHED();
    Wh[0][2] = tr_read(cw0 + 53760);
    SCHED();
    fkt1 = pin(__builtin_amdgcn_fmed3f(ex2(fkt1), 0.f, 1.f));
    SCHED();
    float fkt2 = pin(__builtin_fmaf(ac[0][0][0][2], l2e, -1e-6f * L2E));
    SCHED();
    SCHED();
    ws0 = pins(ws0 + 4096); setm0<67584>(lw4); SCHED();
    ac[1][0][1] = mma32(X[15][0], cat(Wl[1][3], Wh[1][3]), ac[1][0][1]); SCHED();
    dmaw(w2o0, ws0);
    Wl[0][3] = tr_read(cw1 + 53248);
    SCHED();
    fkp0 = pin(fkp0 + fkt0);
    SCHED();
    float fkp1 = vmax(ac[0][0][0][1], 1e-6f);
    SCHED();
    SCHED();
    ac[1][1][1] = mma32(X[15][1], cat(Wl[1][3], Wh[1][3]), ac[1][1][1]); SCHED();
    Wh[0][3] = tr_read(cw1 + 53760);
    SCHED();
    fkt2 = pin(__builtin_amdgcn_fmed3f(ex2(fkt2), 0.f, 1.f));
    SCHED();
    float fkt3 = pin(__builtin_fmaf(ac[0][0][0][3], l2e, -1e-6f * L2E));
    SCHED();
    PRIO(3);
    SCHED();
    WAITC(10, 15);
    BAR(); SCHED();
    STH(6, 5, 98, 96);
    ac[3][0][0] = mma32(cat(Wl[0][0], Wh[0][0]), X[8][0], ac[3][0][0]); SCHED();
    Wl[1][0] = tr_read(cw0 + 57344);
    SCHED();
    fkp1 = pin(fkp1 + fkt1);
    SCHED();
    float fkp2 = vmax(ac[0][0][0][2], 1e-6f);
    SCHED();
    SCHED();
    ac[3][1][0] = mma32(cat(Wl[0][0], Wh[0][0]), X[8][1], ac[3][1][0]); SCHED();
    Wh[1][0] = tr_read(cw0 + 57856);
    SCHED();
    fkt3 = pin(__builtin_amdgcn_fmed3f(ex2(fkt3), 0.f, 1.f));
    SCHED();
    pa0[0] = pinu(pk2(fkp0, fkp1));
    SCHED();
    float fkt4 = pin(__builtin_fmaf(ac[0][1][0][0], l2e, -1e-6f * L2E));
    SCHED();
    SCHED();
    ws0 = pins(ws0 + 4096); setm0<68608>(lw4); SCHED();
    ac[3][0][1] = mma32(cat(Wl[0][1], Wh[0][1]), X[8][0], ac[3][0][1]); SCHED();
    dmaw(w2o1, ws0);
    Wl[1][1] = tr_read(cw1 + 57344);
    SCHED();
    fkp2 = pin(fkp2 + fkt2);
    SCHED();
    float fkp3 = vmax(ac[0][0][0][3], 1e-6f);
    SCHED();
    SCHED();
    ac[3][1][1] = mma32(cat(Wl[0][1], Wh[0][1]), X[8][1], ac[3][1][1]); SCHED();
    Wh[1][1] = tr_read(cw1 + 57856);
    SCHED();
    fkt4 = pin(__builtin_amdgcn_fmed3f(ex2(fkt4), 0.f, 1.f));
    SCHED();
    float fkt5 = pin(__builtin_fmaf(ac[0][1][0][1], l2e, -1e-6f * L2E));
    SCHED();
    SCHED();
    ac[3][0][0] = mma32(cat(Wl[0][2], Wh[0][2]), X[9][0], ac[3][0][0]); SCHED();
    Wl[1][2] = tr_read(cw0 + 61440);
    SCHED();
    fkp3 = pin(fkp3 + fkt3);
    SCHED();
    float fkp4 = vmax(ac[0][1][0][0], 1e-6f);
    SCHED();
    SCHED();
    ac[3][1][0] = mma32(cat(Wl[0][2], Wh[0][2]), X[9][1], ac[3][1][0]); SCHED();
    Wh[1][2] = tr_read(cw0 + 61952);
    SCHED();
    fkt5 = pin(__builtin_amdgcn_fmed3f(ex2(fkt5), 0.f, 1.f));
    SCHED();
    pa0[1] = pinu(pk2(fkp2, fkp3));
    SCHED();
    float fkt6 = pin(__builtin_fmaf(ac[0][1][0][2], l2e, -1e-6f * L2E));
    SCHED();
    SCHED();
    va = pinu(vo1 + kQ[0]); setm0<98304>(lw1); SCHED();
    ac[3][0][1] = mma32(cat(Wl[0][3], Wh[0][3]), X[9][0], ac[3][0][1]); SCHED();
    dma(va, b3);
    Wl[1][3] = tr_read(cw1 + 61440);
    SCHED();
    cw0 = pinp(cw0 + (65536));
    SCHED();
    fkp4 = pin(fkp4 + fkt4);
    SCHED();
    float fkp5 = vmax(ac[0][1][0][1], 1e-6f);
    SCHED();
    SCHED();
    ac[3][1][1] = mma32(cat(Wl[0][3], Wh[0][3]), X[9][1], ac[3][1][1]); SCHED();
    Wh[1][3] = tr_read(cw1 + 61952);
    SCHED();
    fkt6 = pin(__builtin_amdgcn_fmed3f(ex2(fkt6), 0.f, 1.f));
    SCHED();
    float fkt7 = pin(__builtin_fmaf(ac[0][1][0][3], l2e, -1e-6f * L2E));
    SCHED();
    PRIO(2);
    SCHED();
    ac[3][0][0] = mma32(cat(Wl[1][0], Wh[1][0]), X[10][0], ac[3][0][0]); SCHED();
    Wl[0][0] = tr_read(cw0 + 0);
    SCHED();
    cw1 = pinp(cw1 + (65536));
    SCHED();
    fkp5 = pin(fkp5 + fkt5);
    SCHED();
    float fkp6 = vmax(ac[0][1][0][2], 1e-6f);
    SCHED();
    SCHED();
    ac[3][1][0] = mma32(cat(Wl[1][0], Wh[1][0]), X[10][1], ac[3][1][0]); SCHED();
    Wh[0][0] = tr_read(cw0 + 512);
    SCHED();
    fkt7 = pin(__builtin_amdgcn_fmed3f(ex2(fkt7), 0.f, 1.f));
    SCHED();
    pa0[2] = pinu(pk2(fkp4, fkp5));
    SCHED();
    float fkt8 = pin(__builtin_fmaf(ac[0][0][1][0], l2e, -1e-6f * L2E));
    SCHED();
    SCHED();
    va = pinu(vo1 + kQ[1]); setm0<106496>(lw1); SCHED();
    ac[3][0][1] = mma32(cat(Wl[1][1], Wh[1][1]), X[10][0], ac[3][0][1]); SCHED();
    dma(va, b3);
    Wl[0][1] = tr_read(cw1 + 0);
    SCHED();
    fkp6 = pin(fkp6 + fkt6);
    SCHED();
    float fkp7 = vmax(ac[0][1][0][3], 1e-6f);
    SCHED();
    SCHED();
    ac[3][1][1] = mma32(cat(Wl[1][1], Wh[1][1]), X[10][1], ac[3][1][1]); SCHED();
    Wh[0][1] = tr_read(cw1 + 512);
    SCHED();
    fkt8 = pin(__builtin_amdgcn_fmed3f(ex2(fkt8), 0.f, 1.f));
    SCHED();
    float fkt9 = pin(__builtin_fmaf(ac[0][0][1][1], l2e, -1e-6f * L2E));
    SCHED();
    SCHED();
    ac[3][0][0] = mma32(cat(Wl[1][2], Wh[1][2]), X[11][0], ac[3][0][0]); SCHED();
    Wl[0][2] = tr_read(cw0 + 4096);
    SCHED();
    fkp7 = pin(fkp7 + fkt7);
    SCHED();
    float fkp8 = vmax(ac[0][0][1][0], 1e-6f);
    SCHED();
    SCHED();
    ac[3][1][0] = mma32(cat(Wl[1][2], Wh[1][2]), X[11][1], ac[3][1][0]); SCHED();
    Wh[0][2] = tr_read(cw0 + 4608);
    SCHED();
    fkt9 = pin(__builtin_amdgcn_fmed3f(ex2(fkt9), 0.f, 1.f));
    SCHED();
    pa0[3] = pinu(pk2(fkp6, fkp7));
    SCHED();
    float fkt10 = pin(__builtin_fmaf(ac[0][0][1][2], l2e, -1e-6f * L2E));
    SCHED();
    SCHED();
    va = pinu(vo1 + kQ[2]); setm0<114688>(lw1); SCHED();
    ac[3][0][1] = mma32(cat(Wl[1][3], Wh[1][3]), X[11][0], ac[3][0][1]); SCHED();
    dma(va, b3);
    Wl[0][3] = tr_read(cw1 + 4096);
    SCHED();
    fkp8 = pin(fkp8 + fkt8);
    SCHED();
    float fkp9 = vmax(ac[0][0][1][1], 1e-6f);
    SCHED();
    SCHED();
    ac[3][1][1] = mma32(cat(Wl[1][3], Wh[1][3]), X[11][1], ac[3][1][1]); SCHED();
    Wh[0][3] = tr_read(cw1 + 4608);
    SCHED();
    fkt10 = pin(__builtin_amdgcn_fmed3f(ex2(fkt10), 0.f, 1.f));
    SCHED();
    float fkt11 = pin(__builtin_fmaf(ac[0][0][1][3], l2e, -1e-6f * L2E));
    SCHED();
    PRIO(1);
    SCHED();
    ac[3][0][0] = mma32(cat(Wl[0][0], Wh[0][0]), X[12][0], ac[3][0][0]); SCHED();
    Wl[1][0] = tr_read(cw0 + 8192);
    SCHED();
    lane = lane_id(); LAS char* pt0 = pinp(smem + (wave * 4096 + (lane >> 4) * 1024 + FR(lane) + SZ(lane) * 32)); LAS char *tp0, *tp1;
    SCHED();
    fkp9 = pin(fkp9 + fkt9);
    SCHED();
    float fkp10 = vmax(ac[0][0][1][2], 1e-6f);
    SCHED();
    SCHED();
    ac[3][1][0] = mma32(cat(Wl[0][0], Wh[0][0]), X[12][1], ac[3][1][0]); SCHED();
    Wh[1][0] = tr_read(cw0 + 8704);
    SCHED();
    fkt11 = pin(__builtin_amdgcn_fmed3f(ex2(fkt11), 0.f, 1.f));
    SCHED();
    pa1[0] = pinu(pk2(fkp8, fkp9));
    SCHED();
    float fkt12 = pin(__builtin_fmaf(ac[0][1][1][0], l2e, -1e-6f * L2E));
    SCHED();
    SCHED();
    va = pinu(vo1 + kQ[3]); setm0<122880>(lw1); SCHED();
    ac[3][0][1] = mma32(cat(Wl[0][1], Wh[0][1]), X[12][0], ac[3][0][1]); SCHED();
    dma(va, b3);
    Wl[1][1] = tr_read(cw1 + 8192);
    SCHED();
    fkp10 = pin(fkp10 + fkt10);
    SCHED();
    float fkp11 = vmax(ac[0][0][1][3], 1e-6f);
    SCHED();
    SCHED();
    ac[3][1][1] = mma32(cat(Wl[0][1], Wh[0][1]), X[12][1], ac[3][1][1]); SCHED();
    Wh[1][1] = tr_read(cw1 + 8704);
    SCHED();
    fkt12 = pin(__builtin_amdgcn_fmed3f(ex2(fkt12), 0.f, 1.f));
    SCHED();
    float fkt13 = pin(__builtin_fmaf(ac[0][1][1][1], l2e, -1e-6f * L2E));
    SCHED();
    SCHED();
    ac[3][0][0] = mma32(cat(Wl[0][2], Wh[0][2]), X[13][0], ac[3][0][0]); SCHED();
    Wl[1][2] = tr_read(cw0 + 12288);
    SCHED();
    fkp11 = pin(fkp11 + fkt11);
    SCHED();
    float fkp12 = vmax(ac[0][1][1][0], 1e-6f);
    SCHED();
    SCHED();
    ac[3][1][0] = mma32(cat(Wl[0][2], Wh[0][2]), X[13][1], ac[3][1][0]); SCHED();
    Wh[1][2] = tr_read(cw0 + 12800);
    SCHED();
    fkt13 = pin(__builtin_amdgcn_fmed3f(ex2(fkt13), 0.f, 1.f));
    SCHED();
    pa1[1] = pinu(pk2(fkp10, fkp11));
    SCHED();
    float fkt14 = pin(__builtin_fmaf(ac[0][1][1][2], l2e, -1e-6f * L2E));
    SCHED();
    SCHED();
    ac[3][0][1] = mma32(cat(Wl[0][3], Wh[0][3]), X[13][0], ac[3][0][1]); SCHED();
    Wl[1][3] = tr_read(cw1 + 12288);
    SCHED();
    cw0 = pinp(cw0 + (-147456));
    SCHED();
    fkp12 = pin(fkp12 + fkt12);
    SCHED();
    float fkp13 = vmax(ac[0][1][1][1], 1e-6f);
    SCHED();
    v4u fil0 = {~0u, ~0u, ~0u, ~0u}; asm volatile("" : "+v"(fil0));
    SCHED();
    SCHED();
    ac[3][1][1] = mma32(cat(Wl[0][3], Wh[0][3]), X[13][1], ac[3][1][1]); SCHED();
    Wh[1][3] = tr_read(cw1 + 12800);
    SCHED();
    st16c<0>(fil0, l16, h3d);
    SCHED();
    fkt14 = pin(__builtin_amdgcn_fmed3f(ex2(fkt14), 0.f, 1.f));
    SCHED();
    float fkt15 = pin(__builtin_fmaf(ac[0][1][1][3], l2e, -1e-6f * L2E));
    SCHED();
    PRIO(0);
    SCHED();
    ac[3][0][0] = mma32(cat(Wl[1][0], Wh[1][0]), X[14][0], ac[3][0][0]); SCHED();
    Wl[0][0] = tr_read(cw0 + 0);
    SCHED();
    cw1 = pinp(cw1 + (-147456));
    SCHED();
    fkp13 = pin(fkp13 + fkt13);
    SCHED();
    float fkp14 = vmax(ac[0][1][1][2], 1e-6f);
    SCHED();
    SCHED();
    ac[3][1][0] = mma32(cat(Wl[1][0], Wh[1][0]), X[14][1], ac[3][1][0]); SCHED();
    Wh[0][0] = tr_read(cw0 + 512);
    SCHED();
    fkt15 = pin(__builtin_amdgcn_fmed3f(ex2(fkt15), 0.f, 1.f));
    SCHED();
    pa1[2] = pinu(pk2(fkp12, fkp13));
    SCHED();
    SCHED();
    ac[3][0][1] = mma32(cat(Wl[1][1], Wh[1][1]), X[14][0], ac[3][0][1]); SCHED();
    Wl[0][1] = tr_read(cw1 + 0);
    SCHED();
    fkp14 = pin(fkp14 + fkt14);
    SCHED();
    float fkp15 = vmax(ac[0][1][1][3], 1e-6f);
    SCHED();
    SCHED();
    ac[3][1][1] = mma32(cat(Wl[1][1], Wh[1][1]), X[14][1], ac[3][1][1]); SCHED();
    Wh[0][1] = tr_read(cw1 + 512);
    SCHED();
    vb0[0] = pinu(pk2(ac[1][0][0][0], ac[1][0][0][1]));
    SCHED();
    vb0[1] = pinu(pk2(ac[1][0][0][2], ac[1][0][0][3]));
    SCHED();
    SCHED();
    ac[3][0][0] = mma32(cat(Wl[1][2], Wh[1][2]), X[15][0], ac[3][0][0]); SCHED();
    Wl[0][2] = tr_read(cw0 + 4096);
    SCHED();
    WAITC(5, 15);
    SCHED();
    tp0 = pinp(xorp(pt0, 65536));
    SCHED();
    fkp15 = pin(fkp15 + fkt15);
    SCHED();
    vb0[2] = pinu(pk2(ac[1][1][0][0], ac[1][1][0][1]));
    SCHED();
    vb0[3] = pinu(pk2(ac[1][1][0][2], ac[1][1][0][3]));
    SCHED();
    vb1[0] = pinu(pk2(ac[1][0][1][0], ac[1][0][1][1]));
    SCHED();
    SCHED();
    ac[3][1][0] = mma32(cat(Wl[1][2], Wh[1][2]), X[15][1], ac[3][1][0]); SCHED();
    Wh[0][2] = tr_read(cw0 + 4608);
    SCHED();
    const v2u Wa0_0 = tr_read(tp0);
    SCHED();
    pa1[3] = pinu(pk2(fkp14, fkp15));
    SCHED();
    vb1[1] = pinu(pk2(ac[1][0][1][2], ac[1][0][1][3]));
    SCHED();
    vb1[2] = pinu(pk2(ac[1][1][1][0], ac[1][1][1][1]));
    SCHED();
    SCHED();
    ac[3][0][1] = mma32(cat(Wl[1][3], Wh[1][3]), X[15][0], ac[3][0][1]); SCHED();
    Wl[0][3] = tr_read(cw1 + 4096);
    SCHED();
    const v4u Wf0_0 = cat(Wa0_0, tr_read(tp0 + 512));
    SCHED();
    tp1 = pinp(xorp(pt0, 65568));
    SCHED();
    vb1[3] = pinu(pk2(ac[1][1][1][2], ac[1][1][1][3]));
    SCHED();
    f32x4 kv0 = mma32(pa0, vb0, zero4);
    SCHED();
    SCHED();
    ac[3][1][1] = mma32(cat(Wl[1][3], Wh[1][3]), X[15][1], ac[3][1][1]); SCHED();
    Wh[0][3] = tr_read(cw1 + 4608);
    SCHED();
    const v2u Wa0_1 = tr_read(tp1);
    SCHED();
    f32x4 z0 = mma32(pa0, ones, zero4);
    SCHED();
    PRIO(3);
    SCHED();
    BAR(); SCHED();
    STH(7, 6, 96, 98);
    ac[4][0][0] = mma32(cat(Wl[0][0], Wh[0][0]), X[0][0], zero4); SCHED();
    Wl[1][0] = tr_read(cw0 + 8192);
    SCHED();
    const v4u Wf0_1 = cat(Wa0_1, tr_read(tp1 + 512));
    SCHED();
    tp0 = pinp(xorp(pt0, 65600));
    SCHED();
    f32x4 kv1 = mma32(pa1, vb1, zero4);
    SCHED();
    SCHED();
    ac[4][1][0] = mma32(cat(Wl[0][0], Wh[0][0]), X[0][1], zero4); SCHED();
    Wh[1][0] = tr_read(cw0 + 8704);
    SCHED();
    const v2u Wa0_2 = tr_read(tp0);
    SCHED();
    f32x4 z1 = mma32(pa1, ones, zero4);
    SCHED();
    SCHED();
    ac[4][0][1] = mma32(cat(Wl[0][1], Wh[0][1]), X[0][0], zero4); SCHED();
    Wl[1][1] = tr_read(cw1 + 8192);
    SCHED();
    const v4u Wf0_2 = cat(Wa0_2, tr_read(tp0 + 512));
    SCHED();
    tp1 = pinp(xorp(pt0, 65632));
    SCHED();
    lane = lane_id(); LAS char* sx = pinp(smem + (131072 + wave * 4096 + lane * 16)); const bool odd = lane & 1;
    SCHED();
    SCHED();
    v4u fil1 = {~0u, ~0u, ~0u, ~0u}; asm volatile("" : "+v"(fil1));
    SCHED();
    SCHED();
    ac[4][1][1] = mma32(cat(Wl[0][1], Wh[0][1]), X[0][1], zero4); SCHED();
    Wh[1][1] = tr_read(cw1 + 8704);
    SCHED();
    const v2u Wa0_3 = tr_read(tp1);
    SCHED();
    st16c<1024>(fil1, l16, h3d);
    SCHED();
    *(LAS f32x4*)sx = kv0;
    SCHED();
    PRIO(2);
    SCHED();
    ac[4][0][0] = mma32(cat(Wl[0][2], Wh[0][2]), X[1][0], ac[4][0][0]); SCHED();
    Wl[1][2] = tr_read(cw0 + 12288);
    SCHED();
    const v4u Wf0_3 = cat(Wa0_3, tr_read(tp1 + 512));
    SCHED();
    SCHED();
    SCHED();
    f32x4 zz; zz[0] = odd ? z0[2] : z0[0]; zz[1] = odd ? z0[3] : z0[1];
    SCHED();
    *(LAS f32x4*)(sx + 1024) = kv1;
    SCHED();
    SCHED();
    ac[4][1][0] = mma32(cat(Wl[0][2], Wh[0][2]), X[1][1], ac[4][1][0]); SCHED();
    Wh[1][2] = tr_read(cw0 + 12800);
    SCHED();
    zz[2] = odd ? z1[2] : z1[0]; zz[3] = odd ? z1[3] : z1[1];
    SCHED();
    *(LAS f32x4*)(sx + 2048) = zz;
    SCHED();
    SCHED();
    SCHED();
    SCHED();
    ac[4][0][1] = mma32(cat(Wl[0][3], Wh[0][3]), X[1][0], ac[4][0][1]); SCHED();
    Wl[1][3] = tr_read(cw1 + 12288);
    SCHED();
    LAS char* sr = pinp(smem + (131072 + WN * 16384 + (WM & 1) * 1024 + lane * 16)); LAS char* sy = pinp(smem + (131072 + WN * 16384 + (WM & 1) * 8 + lane * 16));
    SCHED();
    SCHED();
    ac[4][1][1] = mma32(cat(Wl[0][3], Wh[0][3]), X[1][1], ac[4][1][1]); SCHED();
    Wh[1][3] = tr_read(cw1 + 12800);
    SCHED();
    PRIO(1);
    SCHED();
    ac[4][0][0] = mma32(cat(Wl[1][0], Wh[1][0]), X[2][0], ac[4][0][0]); SCHED();
    Wl[0][0] = tr_read(cw0 + 16384);
    SCHED();
    SCHED();
    ac[4][1][0] = mma32(cat(Wl[1][0], Wh[1][0]), X[2][1], ac[4][1][0]); SCHED();
    Wh[0][0] = tr_read(cw0 + 16896);
    SCHED();
    SCHED();
    ac[4][0][1] = mma32(cat(Wl[1][1], Wh[1][1]), X[2][0], ac[4][0][1]); SCHED();
    Wl[0][1] = tr_read(cw1 + 16384);
    SCHED();
    SCHED();
    ac[4][1][1] = mma32(cat(Wl[1][1], Wh[1][1]), X[2][1], ac[4][1][1]); SCHED();
    Wh[0][1] = tr_read(cw1 + 16896);
    SCHED();
    WAITC(63, 7);
    SCHED();
    PRIO(0);
    SCHED();
    ac[4][0][0] = mma32(cat(Wl[1][2], Wh[1][2]), X[3][0], ac[4][0][0]); SCHED();
    Wl[0][2] = tr_read(cw0 + 20480);
    SCHED();
    SCHED();
    ac[4][1][0] = mma32(cat(Wl[1][2], Wh[1][2]), X[3][1], ac[4][1][0]); SCHED();
    Wh[0][2] = tr_read(cw0 + 20992);
    SCHED();
    SCHED();
    const char* ws1 = pins(w2b + (un[1] << 15)); setm0<65536>(lw4); SCHED();
    ac[4][0][1] = mma32(cat(Wl[1][3], Wh[1][3]), X[3][0], ac[4][0][1]); SCHED();
    dmaw(w2o0, ws1);
    Wl[0][3] = tr_read(cw1 + 20480);
    SCHED();
    SCHED();
    ac[4][1][1] = mma32(cat(Wl[1][3], Wh[1][3]), X[3][1], ac[4][1][1]); SCHED();
    Wh[0][3] = tr_read(cw1 + 20992);
    SCHED();
    PRIO(3);
    SCHED();
    WAITC(63, 10);
    BAR(); SCHED();
    ac[4][0][0] = mma32(cat(Wl[0][0], Wh[0][0]), X[4][0], ac[4][0][0]); SCHED();
    Wl[1][0] = tr_read(cw0 + 24576);
    SCHED();
    f32x4 r0 = *(LAS f32x4*)(sr + 0); f32x2 y0 = *(LAS f32x2*)(sy + 2048);
    SCHED();
    SCHED();
    ac[4][1][0] = mma32(cat(Wl[0][0], Wh[0][0]), X[4][1], ac[4][1][0]); SCHED();
    Wh[1][0] = tr_read(cw0 + 25088);
    SCHED();
    f32x4 r1 = *(LAS f32x4*)(sr + 4096); f32x2 y1 = *(LAS f32x2*)(sy + 6144);
    SCHED();
    SCHED();
    ws1 = pins(ws1 + 4096); setm0<66560>(lw4); SCHED();
    ac[4][0][1] = mma32(cat(Wl[0][1], Wh[0][1]), X[4][0], ac[4][0][1]); SCHED();
    dmaw(w2o1, ws1);
    Wl[1][1] = tr_read(cw1 + 24576);
    SCHED();
    f32x4 r2 = *(LAS f32x4*)(sr + 8192); f32x2 y2 = *(LAS f32x2*)(sy + 10240);
    SCHED();
    SCHED();
    ac[4][1][1] = mma32(cat(Wl[0][1], Wh[0][1]), X[4][1], ac[4][1][1]); SCHED();
    Wh[1][1] = tr_read(cw1 + 25088);
    SCHED();
    f32x4 r3 = *(LAS f32x4*)(sr + 12288); f32x2 y3 = *(LAS f32x2*)(sy + 14336);
    SCHED();
    PRIO(2);
    SCHED();
    ac[4][0][0] = mma32(cat(Wl[0][2], Wh[0][2]), X[5][0], ac[4][0][0]); SCHED();
    Wl[1][2] = tr_read(cw0 + 28672);
    SCHED();
    SCHED();
    ac[4][1][0] = mma32(cat(Wl[0][2], Wh[0][2]), X[5][1], ac[4][1][0]); SCHED();
    Wh[1][2] = tr_read(cw0 + 29184);
    SCHED();
    r0[0] = pin(r0[0] + r1[0]); r2[0] = pin(r2[0] + r3[0]);
    SCHED();
    SCHED();
    ws1 = pins(ws1 + 4096); setm0<67584>(lw4); SCHED();
    ac[4][0][1] = mma32(cat(Wl[0][3], Wh[0][3]), X[5][0], ac[4][0][1]); SCHED();
    dmaw(w2o0, ws1);
    Wl[1][3] = tr_read(cw1 + 28672);
    SCHED();
    r0[1] = pin(r0[1] + r1[1]); r2[1] = pin(r2[1] + r3[1]);
    SCHED();
    r0[2] = pin(r0[2] + r1[2]); r2[2] = pin(r2[2] + r3[2]);
    SCHED();
    SCHED();
    ac[4][1][1] = mma32(cat(Wl[0][3], Wh[0][3]), X[5][1], ac[4][1][1]); SCHED();
    Wh[1][3] = tr_read(cw1 + 29184);
    SCHED();
    r0[3] = pin(r0[3] + r1[3]); r2[3] = pin(r2[3] + r3[3]);
    SCHED();
    PRIO(1);
    SCHED();
    ac[4][0][0] = mma32(cat(Wl[1][0], Wh[1][0]), X[6][0], ac[4][0][0]); SCHED();
    Wl[0][0] = tr_read(cw0 + 32768);
    SCHED();
    float ya0 = pin(y0[0] + pin(y1[0])); float yb0 = pin(y2[0] + pin(y3[0]));
    SCHED();
    float ya1 = pin(y0[1] + pin(y1[1])); float yb1 = pin(y2[1] + pin(y3[1]));
    SCHED();
    SCHED();
    ac[4][1][0] = mma32(cat(Wl[1][0], Wh[1][0]), X[6][1], ac[4][1][0]); SCHED();
    Wh[0][0] = tr_read(cw0 + 33280);
    SCHED();
    r0[0] = pin(r0[0] + r2[0]); r0[1] = pin(r0[1] + r2[1]);
    SCHED();
    SCHED();
    ws1 = pins(ws1 + 4096); setm0<68608>(lw4); SCHED();
    ac[4][0][1] = mma32(cat(Wl[1][1], Wh[1][1]), X[6][0], ac[4][0][1]); SCHED();
    dmaw(w2o1, ws1);
    Wl[0][1] = tr_read(cw1 + 32768);
    SCHED();
    r0[2] = pin(r0[2] + r2[2]); r0[3] = pin(r0[3] + r2[3]);
    SCHED();
    ya0 = pin(ya0 + yb0); ya1 = pin(ya1 + yb1);
    SCHED();
    SCHED();
    ac[4][1][1] = mma32(cat(Wl[1][1], Wh[1][1]), X[6][1], ac[4][1][1]); SCHED();
    Wh[0][1] = tr_read(cw1 + 33280);
    SCHED();
    v4u kg = {pk2(r0[0], r0[1]), pk2(r0[2], r0[3]), pk2(ya0, ya1), tag};;;;
    SCHED();
    __builtin_amdgcn_raw_buffer_store_b128(kg, wsr, l16 + kvdst, 0, SC1);
    SCHED();
    PRIO(0);
    SCHED();
    ac[4][0][0] = mma32(cat(Wl[1][2], Wh[1][2]), X[7][0], ac[4][0][0]); SCHED();
    Wl[0][2] = tr_read(cw0 + 36864);
    SCHED();
    SCHED();
    ac[4][1][0] = mma32(cat(Wl[1][2], Wh[1][2]), X[7][1], ac[4][1][0]); SCHED();
    Wh[0][2] = tr_read(cw0 + 37376);
    SCHED();
    float gy = pin(ex2(gx));
    SCHED();
    gy = pin(1.f + gy);
    SCHED();
    SCHED();
    va = pinu(vo1 + kQ[4]); setm0<0>(lw1); SCHED();
    ac[4][0][1] = mma32(cat(Wl[1][3], Wh[1][3]), X[7][0], ac[4][0][1]); SCHED();
    dma(va, b3);
    Wl[0][3] = tr_read(cw1 + 36864);
    SCHED();
    gy = pin(__builtin_fminf(gy, 1e30f));
    SCHED();
    SCHED();
    ac[4][1][1] = mma32(cat(Wl[1][3], Wh[1][3]), X[7][1], ac[4][1][1]); SCHED();
    Wh[0][3] = tr_read(cw1 + 37376);
    SCHED();
    PRIO(3);
    SCHED();
    WAITC(11, 15);
    BAR(); SCHED();
    STH(8, 7, 98, 96);
    ac[4][0][0] = mma32(cat(Wl[0][0], Wh[0][0]), X[8][0], ac[4][0][0]); SCHED();
    Wl[1][0] = tr_read(cw0 + 40960);
    SCHED();
    SCHED();
    ac[4][1][0] = mma32(cat(Wl[0][0], Wh[0][0]), X[8][1], ac[4][1][0]); SCHED();
    Wh[1][0] = tr_read(cw0 + 41472);
    SCHED();
    SCHED();
    va = pinu(vo1 + kQ[5]); setm0<8192>(lw1); SCHED();
    ac[4][0][1] = mma32(cat(Wl[0][1], Wh[0][1]), X[8][0], ac[4][0][1]); SCHED();
    dma(va, b3);
    Wl[1][1] = tr_read(cw1 + 40960);
    SCHED();
    float io0, io1; { auto s = __builtin_amdgcn_permlane32_swap(f2u(gy), f2u(gy), false, false); io0 = u2f(s[0]); io1 = u2f(s[1]); }
    SCHED();
    SCHED();
    ac[4][1][1] = mma32(cat(Wl[0][1], Wh[0][1]), X[8][1], ac[4][1][1]); SCHED();
    Wh[1][1] = tr_read(cw1 + 41472);
    SCHED();
    gx = pin(ex2(-gx));
    SCHED();
    SCHED();
    ac[4][0][0] = mma32(cat(Wl[0][2], Wh[0][2]), X[9][0], ac[4][0][0]); SCHED();
    Wl[1][2] = tr_read(cw0 + 45056);
    SCHED();
    gx = pin(1.f + gx);
    SCHED();
    SCHED();
    ac[4][1][0] = mma32(cat(Wl[0][2], Wh[0][2]), X[9][1], ac[4][1][0]); SCHED();
    Wh[1][2] = tr_read(cw0 + 45568);
    SCHED();
    SCHED();
    va = pinu(vo1 + kQ[6]); setm0<16384>(lw1); SCHED();
    ac[4][0][1] = mma32(cat(Wl[0][3], Wh[0][3]), X[9][0], ac[4][0][1]); SCHED();
    dma(va, b3);
    Wl[1][3] = tr_read(cw1 + 45056);
    SCHED();
    SCHED();
    ac[4][1][1] = mma32(cat(Wl[0][3], Wh[0][3]), X[9][1], ac[4][1][1]); SCHED();
    Wh[1][3] = tr_read(cw1 + 45568);
    SCHED();
    PRIO(2);
    SCHED();
    ac[4][0][0] = mma32(cat(Wl[1][0], Wh[1][0]), X[10][0], ac[4][0][0]); SCHED();
    Wl[0][0] = tr_read(cw0 + 49152);
    SCHED();
    float ig0, ig1; { auto s = __builtin_amdgcn_permlane32_swap(f2u(gx), f2u(gx), false, false); ig0 = u2f(s[0]); ig1 = u2f(s[1]); }
    SCHED();
    SCHED();
    ac[4][1][0] = mma32(cat(Wl[1][0], Wh[1][0]), X[10][1], ac[4][1][0]); SCHED();
    Wh[0][0] = tr_read(cw0 + 49664);
    SCHED();
    SCHED();
    va = pinu(vo1 + kQ[7]); setm0<24576>(lw1); SCHED();
    ac[4][0][1] = mma32(cat(Wl[1][1], Wh[1][1]), X[10][0], ac[4][0][1]); SCHED();
    dma(va, b3);
    Wl[0][1] = tr_read(cw1 + 49152);
    SCHED();
    SCHED();
    ac[4][1][1] = mma32(cat(Wl[1][1], Wh[1][1]), X[10][1], ac[4][1][1]); SCHED();
    Wh[0][1] = tr_read(cw1 + 49664);
    SCHED();
    SCHED();
    ac[4][0][0] = mma32(cat(Wl[1][2], Wh[1][2]), X[11][0], ac[4][0][0]); SCHED();
    Wl[0][2] = tr_read(cw0 + 53248);
    SCHED();
    SCHED();
    ac[4][1][0] = mma32(cat(Wl[1][2], Wh[1][2]), X[11][1], ac[4][1][0]); SCHED();
    Wh[0][2] = tr_read(cw0 + 53760);
    SCHED();
    SCHED();
    const char* ws2 = pins(w2b + (un[2] << 15)); setm0<131072>(lw4); SCHED();
    ac[4][0][1] = mma32(cat(Wl[1][3], Wh[1][3]), X[11][0], ac[4][0][1]); SCHED();
    dmaw(w2o0, ws2);
    Wl[0][3] = tr_read(cw1 + 53248);
    SCHED();
    SCHED();
    ac[4][1][1] = mma32(cat(Wl[1][3], Wh[1][3]), X[11][1], ac[4][1][1]); SCHED();
    Wh[0][3] = tr_read(cw1 + 53760);
    SCHED();
    PRIO(1);
    SCHED();
    ac[4][0][0] = mma32(cat(Wl[0][0], Wh[0][0]), X[12][0], ac[4][0][0]); SCHED();
    Wl[1][0] = tr_read(cw0 + 57344);
    SCHED();
    SCHED();
    ac[4][1][0] = mma32(cat(Wl[0][0], Wh[0][0]), X[12][1], ac[4][1][0]); SCHED();
    Wh[1][0] = tr_read(cw0 + 57856);
    SCHED();
    SCHED();
    ws2 = pins(ws2 + 4096); setm0<132096>(lw4); SCHED();
    ac[4][0][1] = mma32(cat(Wl[0][1], Wh[0][1]), X[12][0], ac[4][0][1]); SCHED();
    dmaw(w2o1, ws2);
    Wl[1][1] = tr_read(cw1 + 57344);
    SCHED();
    SCHED();
    ac[4][1][1] = mma32(cat(Wl[0][1], Wh[0][1]), X[12][1], ac[4][1][1]); SCHED();
    Wh[1][1] = tr_read(cw1 + 57856);
    SCHED();
    SCHED();
    ac[4][0][0] = mma32(cat(Wl[0][2], Wh[0][2]), X[13][0], ac[4][0][0]); SCHED();
    Wl[1][2] = tr_read(cw0 + 61440);
    SCHED();
    SCHED();
    ac[4][1][0] = mma32(cat(Wl[0][2], Wh[0][2]), X[13][1], ac[4][1][0]); SCHED();
    Wh[1][2] = tr_read(cw0 + 61952);
    SCHED();
    SCHED();
    ws2 = pins(ws2 + 4096); setm0<133120>(lw4); SCHED();
    ac[4][0][1] = mma32(cat(Wl[0][3], Wh[0][3]), X[13][0], ac[4][0][1]); SCHED();
    dmaw(w2o0, ws2);
    Wl[1][3] = tr_read(cw1 + 61440);
    SCHED();
    cw0 = pinp(cw0 + (98304));
    SCHED();
    SCHED();
    ac[4][1][1] = mma32(cat(Wl[0][3], Wh[0][3]), X[13][1], ac[4][1][1]); SCHED();
    Wh[1][3] = tr_read(cw1 + 61952);
    SCHED();
    PRIO(0);
    SCHED();
    ac[4][0][0] = mma32(cat(Wl[1][0], Wh[1][0]), X[14][0], ac[4][0][0]); SCHED();
    Wl[0][0] = tr_read(cw0 + 0);
    SCHED();
    cw1 = pinp(cw1 + (98304));
    SCHED();
    SCHED();
    ac[4][1][0] = mma32(cat(Wl[1][0], Wh[1][0]), X[14][1], ac[4][1][0]); SCHED();
    Wh[0][0] = tr_read(cw0 + 512);
    SCHED();
    SCHED();
    ws2 = pins(ws2 + 4096); setm0<134144>(lw4); SCHED();
    ac[4][0][1] = mma32(cat(Wl[1][1], Wh[1][1]), X[14][0], ac[4][0][1]); SCHED();
    dmaw(w2o1, ws2);
    Wl[0][1] = tr_read(cw1 + 0);
    SCHED();
    SCHED();
    ac[4][1][1] = mma32(cat(Wl[1][1], Wh[1][1]), X[14][1], ac[4][1][1]); SCHED();
    Wh[0][1] = tr_read(cw1 + 512);
    SCHED();
    SCHED();
    ac[4][0][0] = mma32(cat(Wl[1][2], Wh[1][2]), X[15][0], ac[4][0][0]); SCHED();
    Wl[0][2] = tr_read(cw0 + 4096);
    SCHED();
    SCHED();
    ac[4][1][0] = mma32(cat(Wl[1][2], Wh[1][2]), X[15][1], ac[4][1][0]); SCHED();
    Wh[0][2] = tr_read(cw0 + 4608);
    SCHED();
    SCHED();
    ac[4][0][1] = mma32(cat(Wl[1][3], Wh[1][3]), X[15][0], ac[4][0][1]); SCHED();
    Wl[0][3] = tr_read(cw1 + 4096);
    SCHED();
    SCHED();
    ac[4][1][1] = mma32(cat(Wl[1][3], Wh[1][3]), X[15][1], ac[4][1][1]); SCHED();
    Wh[0][3] = tr_read(cw1 + 4608);
    SCHED();
    PRIO(3);
    SCHED();
    WAITC(7, 15);
    BAR(); SCHED();
    STH(9, 8, 96, 98);
    ac[2][0][0] = mma32(cat(Wl[0][0], Wh[0][0]), X[0][0], eps4); SCHED();
    Wl[1][0] = tr_read(cw0 + 8192);
    SCHED();
    v4u hv2[2];
    SCHED();
    SCHED();
    const char* ws3 = pins(w2b + (un[3] << 15)); setm0<32768>(lw4); SCHED();
    ac[2][1][0] = mma32(cat(Wl[0][0], Wh[0][0]), X[0][1], eps4); SCHED();
    dmaw(w2o0, ws3);
    Wh[1][0] = tr_read(cw0 + 8704);
    SCHED();
    float hv2_t0 = pin(ac[3][0][0][0] * -L2E);
    SCHED();
    SCHED();
    ac[2][0][1] = mma32(cat(Wl[0][1], Wh[0][1]), X[0][0], eps4); SCHED();
    Wl[1][1] = tr_read(cw1 + 8192);
    SCHED();
    SCHED();
    ac[2][1][1] = mma32(cat(Wl[0][1], Wh[0][1]), X[0][1], eps4); SCHED();
    Wh[1][1] = tr_read(cw1 + 8704);
    SCHED();
    hv2_t0 = pin(ex2(hv2_t0));
    SCHED();
    float hv2_t1 = pin(ac[3][0][0][1] * -L2E);
    SCHED();
    SCHED();
    ws3 = pins(ws3 + 4096); setm0<33792>(lw4); SCHED();
    ac[2][0][0] = mma32(cat(Wl[0][2], Wh[0][2]), X[1][0], ac[2][0][0]); SCHED();
    dmaw(w2o1, ws3);
    Wl[1][2] = tr_read(cw0 + 12288);
    SCHED();
    float hv2_p0 = pin(ac[3][0][0][0] * ac[4][0][0][0]);
    SCHED();
    SCHED();
    ac[2][1][0] = mma32(cat(Wl[0][2], Wh[0][2]), X[1][1], ac[2][1][0]); SCHED();
    Wh[1][2] = tr_read(cw0 + 12800);
    SCHED();
    hv2_t1 = pin(ex2(hv2_t1));
    SCHED();
    hv2_t0 = pin(__builtin_fmaf(hv2_t0, io0, io0));
    SCHED();
    float hv2_t2 = pin(ac[3][0][0][2] * -L2E);
    SCHED();
    SCHED();
    ac[2][0][1] = mma32(cat(Wl[0][3], Wh[0][3]), X[1][0], ac[2][0][1]); SCHED();
    Wl[1][3] = tr_read(cw1 + 12288);
    SCHED();
    hv2_t0 = pin(frcp(hv2_t0));
    SCHED();
    float hv2_p1 = pin(ac[3][0][0][1] * ac[4][0][0][1]);
    SCHED();
    SCHED();
    ws3 = pins(ws3 + 4096); setm0<34816>(lw4); SCHED();
    ac[2][1][1] = mma32(cat(Wl[0][3], Wh[0][3]), X[1][1], ac[2][1][1]); SCHED();
    dmaw(w2o0, ws3);
    Wh[1][3] = tr_read(cw1 + 12800);
    SCHED();
    hv2_t2 = pin(ex2(hv2_t2));
    SCHED();
    hv2_p0 = pin(hv2_p0 * hv2_t0);
    SCHED();
    hv2_t1 = pin(__builtin_fmaf(hv2_t1, io0, io0));
    SCHED();
    float hv2_t3 = pin(ac[3][0][0][3] * -L2E);
    SCHED();
    PRIO(2);
    SCHED();
    ac[2][0][0] = mma32(cat(Wl[1][0], Wh[1][0]), X[2][0], ac[2][0][0]); SCHED();
    Wl[0][0] = tr_read(cw0 + 16384);
    SCHED();
    hv2_t1 = pin(frcp(hv2_t1));
    SCHED();
    float hv2_p2 = pin(ac[3][0][0][2] * ac[4][0][0][2]);
    SCHED();
    SCHED();
    ws3 = pins(ws3 + 4096); setm0<35840>(lw4); SCHED();
    ac[2][1][0] = mma32(cat(Wl[1][0], Wh[1][0]), X[2][1], ac[2][1][0]); SCHED();
    dmaw(w2o1, ws3);
    Wh[0][0] = tr_read(cw0 + 16896);
    SCHED();
    hv2_t3 = pin(ex2(hv2_t3));
    SCHED();
    hv2_p1 = pin(hv2_p1 * hv2_t1);
    SCHED();
    hv2_t2 = pin(__builtin_fmaf(hv2_t2, io0, io0));
    SCHED();
    float hv2_t4 = pin(ac[3][0][1][0] * -L2E);
    SCHED();
    SCHED();
    ac[2][0][1] = mma32(cat(Wl[1][1], Wh[1][1]), X[2][0], ac[2][0][1]); SCHED();
    Wl[0][1] = tr_read(cw1 + 16384);
    SCHED();
    hv2_t2 = pin(frcp(hv2_t2));
    SCHED();
    SCHED();
    tp0 = pinp(xorp(pt0, 65536));
    SCHED();
    hv2[0][0] = pinu(pk2(hv2_p0, hv2_p1));
    SCHED();
    float hv2_p3 = pin(ac[3][0][0][3] * ac[4][0][0][3]);
    SCHED();
    SCHED();
    ac[2][1][1] = mma32(cat(Wl[1][1], Wh[1][1]), X[2][1], ac[2][1][1]); SCHED();
    Wh[0][1] = tr_read(cw1 + 16896);
    SCHED();
    const v2u Wa1_0 = tr_read(tp0);
    SCHED();
    hv2_t4 = pin(ex2(hv2_t4));
    SCHED();
    hv2_p2 = pin(hv2_p2 * hv2_t2);
    SCHED();
    hv2_t3 = pin(__builtin_fmaf(hv2_t3, io0, io0));
    SCHED();
    float hv2_t5 = pin(ac[3][0][1][1] * -L2E);
    SCHED();
    SCHED();
    ac[2][0][0] = mma32(cat(Wl[1][2], Wh[1][2]), X[3][0], ac[2][0][0]); SCHED();
    v4u q00; ld16c<0>(q00, l16, kvsrc + 0);
    Wl[0][2] = tr_read(cw0 + 20480);
    SCHED();
    const v4u Wf1_0 = cat(Wa1_0, tr_read(tp0 + 512));
    SCHED();
    hv2_t3 = pin(frcp(hv2_t3));
    SCHED();
    tp1 = pinp(xorp(pt0, 65568));
    SCHED();
    float hv2_p4 = pin(ac[3][0][1][0] * ac[4][0][1][0]);
    SCHED();
    SCHED();
    ac[2][1][0] = mma32(cat(Wl[1][2], Wh[1][2]), X[3][1], ac[2][1][0]); SCHED();
    Wh[0][2] = tr_read(cw0 + 20992);
    SCHED();
    const v2u Wa1_1 = tr_read(tp1);
    SCHED();
    hv2_t5 = pin(ex2(hv2_t5));
    SCHED();
    hv2_p3 = pin(hv2_p3 * hv2_t3);
    SCHED();
    hv2_t4 = pin(__builtin_fmaf(hv2_t4, io0, io0));
    SCHED();
    float hv2_t6 = pin(ac[3][0][1][2] * -L2E);
    SCHED();
    SCHED();
    ac[2][0][1] = mma32(cat(Wl[1][3], Wh[1][3]), X[3][0], ac[2][0][1]); SCHED();
    Wl[0][3] = tr_read(cw1 + 20480);
    SCHED();
    const v4u Wf1_1 = cat(Wa1_1, tr_read(tp1 + 512));
    SCHED();
    hv2_t4 = pin(frcp(hv2_t4));
    SCHED();
    tp0 = pinp(xorp(pt0, 65600));
    SCHED();
    hv2[0][1] = pinu(pk2(hv2_p2, hv2_p3));
    SCHED();
    float hv2_p5 = pin(ac[3][0][1][1] * ac[4][0][1][1]);
    SCHED();
    SCHED();
    ac[2][1][1] = mma32(cat(Wl[1][3], Wh[1][3]), X[3][1], ac[2][1][1]); SCHED();
    v4u q01; ld16c<1024>(q01, l16, kvsrc + 0);
    Wh[0][3] = tr_read(cw1 + 20992);
    SCHED();
    const v2u Wa1_2 = tr_read(tp0);
    SCHED();
    hv2_t6 = pin(ex2(hv2_t6));
    SCHED();
    hv2_p4 = pin(hv2_p4 * hv2_t4);
    SCHED();
    hv2_t5 = pin(__builtin_fmaf(hv2_t5, io0, io0));
    SCHED();
    float hv2_t7 = pin(ac[3][0][1][3] * -L2E);
    SCHED();
    PRIO(1);
    SCHED();
    ac[2][0][0] = mma32(cat(Wl[0][0], Wh[0][0]), X[4][0], ac[2][0][0]); SCHED();
    Wl[1][0] = tr_read(cw0 + 24576);
    SCHED();
    const v4u Wf1_2 = cat(Wa1_2, tr_read(tp0 + 512));
    SCHED();
    hv2_t5 = pin(frcp(hv2_t5));
    SCHED();
    tp1 = pinp(xorp(pt0, 65632));
    SCHED();
    float hv2_p6 = pin(ac[3][0][1][2] * ac[4][0][1][2]);
    SCHED();
    SCHED();
    ac[2][1][0] = mma32(cat(Wl[0][0], Wh[0][0]), X[4][1], ac[2][1][0]); SCHED();
    v4u q02; ld16c<2048>(q02, l16, kvsrc + 0);
    Wh[1][0] = tr_read(cw0 + 25088);
    SCHED();
    const v2u Wa1_3 = tr_read(tp1);
    SCHED();
    hv2_t7 = pin(ex2(hv2_t7));
    SCHED();
    hv2_p5 = pin(hv2_p5 * hv2_t5);
    SCHED();
    hv2_t6 = pin(__builtin_fmaf(hv2_t6, io0, io0));
    SCHED();
    float hv2_t8 = pin(ac[3][1][0][0] * -L2E);
    SCHED();
    SCHED();
    ac[2][0][1] = mma32(cat(Wl[0][1], Wh[0][1]), X[4][0], ac[2][0][1]); SCHED();
    Wl[1][1] = tr_read(cw1 + 24576);
    SCHED();
    const v4u Wf1_3 = cat(Wa1_3, tr_read(tp1 + 512));
    SCHED();
    hv2_t6 = pin(frcp(hv2_t6));
    SCHED();
    SCHED();
    SCHED();
    hv2[0][2] = pinu(pk2(hv2_p4, hv2_p5));
    SCHED();
    float hv2_p7 = pin(ac[3][0][1][3] * ac[4][0][1][3]);
    SCHED();
    SCHED();
    ac[2][1][1] = mma32(cat(Wl[0][1], Wh[0][1]), X[4][1], ac[2][1][1]); SCHED();
    Wh[1][1] = tr_read(cw1 + 25088);
    SCHED();
    hv2_t8 = pin(ex2(hv2_t8));
    SCHED();
    hv2_p6 = pin(hv2_p6 * hv2_t6);
    SCHED();
    hv2_t7 = pin(__builtin_fmaf(hv2_t7, io0, io0));
    SCHED();
    float hv2_t9 = pin(ac[3][1][0][1] * -L2E);
    SCHED();
    SCHED();
    ac[2][0][0] = mma32(cat(Wl[0][2], Wh[0][2]), X[5][0], ac[2][0][0]); SCHED();
    v4u q03; ld16c<3072>(q03, l16, kvsrc + 0);
    Wl[1][2] = tr_read(cw0 + 28672);
    SCHED();
    hv2_t7 = pin(frcp(hv2_t7));
    SCHED();
    WAITC(8, 15);
    SCHED();
    tp0 = pinp(xorp(pt0, 131072));
    SCHED();
    float hv2_p8 = pin(ac[3][1][0][0] * ac[4][1][0][0]);
    SCHED();
    SCHED();
    ac[2][1][0] = mma32(cat(Wl[0][2], Wh[0][2]), X[5][1], ac[2][1][0]); SCHED();
    Wh[1][2] = tr_read(cw0 + 29184);
    SCHED();
    const v2u Wa2_0 = tr_read(tp0);
    SCHED();
    hv2_t9 = pin(ex2(hv2_t9));
    SCHED();
    hv2_p7 = pin(hv2_p7 * hv2_t7);
    SCHED();
    hv2_t8 = pin(__builtin_fmaf(hv2_t8, io1, io1));
    SCHED();
    float hv2_t10 = pin(ac[3][1][0][2] * -L2E);
    SCHED();
    SCHED();
    ac[2][0][1] = mma32(cat(Wl[0][3], Wh[0][3]), X[5][0], ac[2][0][1]); SCHED();
    Wl[1][3] = tr_read(cw1 + 28672);
    SCHED();
    const v4u Wf2_0 = cat(Wa2_0, tr_read(tp0 + 512));
    SCHED();
    hv2_t8 = pin(frcp(hv2_t8));
    SCHED();
    cw0 = pinp(cw0 + (-98304));
    SCHED();
    tp1 = pinp(xorp(pt0, 131104));
    SCHED();
    hv2[0][3] = pinu(pk2(hv2_p6, hv2_p7));
    SCHED();
    float hv2_p9 = pin(ac[3][1][0][1] * ac[4][1][0][1]);
    SCHED();
    SCHED();
    ac[2][1][1] = mma32(cat(Wl[0][3], Wh[0][3]), X[5][1], ac[2][1][1]); SCHED();
    v4u q10; ld16c<0>(q10, l16, kvsrc + 16384);
    Wh[1][3] = tr_read(cw1 + 29184);
    SCHED();
    const v2u Wa2_1 = tr_read(tp1);
    SCHED();
    st16c<0>(hv2[0], l16, h2d);
    SCHED();
    hv2_t10 = pin(ex2(hv2_t10));
    SCHED();
    hv2_p8 = pin(hv2_p8 * hv2_t8);
    SCHED();
    hv2_t9 = pin(__builtin_fmaf(hv2_t9, io1, io1));
    SCHED();
    float hv2_t11 = pin(ac[3][1][0][3] * -L2E);
    SCHED();
    PRIO(0);
    SCHED();
    ac[2][0][0] = mma32(cat(Wl[1][0], Wh[1][0]), X[6][0], ac[2][0][0]); SCHED();
    Wl[0][0] = tr_read(cw0 + 0);
    SCHED();
    const v4u Wf2_1 = cat(Wa2_1, tr_read(tp1 + 512));
    SCHED();
    hv2_t9 = pin(frcp(hv2_t9));
    SCHED();
    cw1 = pinp(cw1 + (-98304));
    SCHED();
    tp0 = pinp(xorp(pt0, 131136));
    SCHED();
    float hv2_p10 = pin(ac[3][1][0][2] * ac[4][1][0][2]);
    SCHED();
    SCHED();
    ac[2][1][0] = mma32(cat(Wl[1][0], Wh[1][0]), X[6][1], ac[2][1][0]); SCHED();
    v4u q11; ld16c<1024>(q11, l16, kvsrc + 16384);
    Wh[0][0] = tr_read(cw0 + 512);
    SCHED();
    const v2u Wa2_2 = tr_read(tp0);
    SCHED();
    hv2_t11 = pin(ex2(hv2_t11));
    SCHED();
    WAITC(63, 12);
    SCHED();
    hv2_p9 = pin(hv2_p9 * hv2_t9);
    SCHED();
    hv2_t10 = pin(__builtin_fmaf(hv2_t10, io1, io1));
    SCHED();
    float hv2_t12 = pin(ac[3][1][1][0] * -L2E);
    SCHED();
    SCHED();
    ac[2][0][1] = mma32(cat(Wl[1][1], Wh[1][1]), X[6][0], ac[2][0][1]); SCHED();
    Wl[0][1] = tr_read(cw1 + 0);
    SCHED();
    const v4u Wf2_2 = cat(Wa2_2, tr_read(tp0 + 512));
    SCHED();
    hv2_t10 = pin(frcp(hv2_t10));
    SCHED();
    tp1 = pinp(xorp(pt0, 131168));
    SCHED();
    hv2[1][0] = pinu(pk2(hv2_p8, hv2_p9));
    SCHED();
    float hv2_p11 = pin(ac[3][1][0][3] * ac[4][1][0][3]);
    SCHED();
    SCHED();
    ac[2][1][1] = mma32(cat(Wl[1][1], Wh[1][1]), X[6][1], ac[2][1][1]); SCHED();
    Wh[0][1] = tr_read(cw1 + 512);
    SCHED();
    const v2u Wa2_3 = tr_read(tp1);
    SCHED();
    hv2_t12 = pin(ex2(hv2_t12));
    SCHED();
    hv2_p10 = pin(hv2_p10 * hv2_t10);
    SCHED();
    hv2_t11 = pin(__builtin_fmaf(hv2_t11, io1, io1));
    SCHED();
    float hv2_t13 = pin(ac[3][1][1][1] * -L2E);
    SCHED();
    SCHED();
    ac[2][0][0] = mma32(cat(Wl[1][2], Wh[1][2]), X[7][0], ac[2][0][0]); SCHED();
    v4u q12; ld16c<2048>(q12, l16, kvsrc + 16384);
    Wl[0][2] = tr_read(cw0 + 4096);
    SCHED();
    const v4u Wf2_3 = cat(Wa2_3, tr_read(tp1 + 512));
    SCHED();
    hv2_t11 = pin(frcp(hv2_t11));
    SCHED();
    SCHED();
    SCHED();
    float hv2_p12 = pin(ac[3][1][1][0] * ac[4][1][1][0]);
    SCHED();
    SCHED();
    ac[2][1][0] = mma32(cat(Wl[1][2], Wh[1][2]), X[7][1], ac[2][1][0]); SCHED();
    Wh[0][2] = tr_read(cw0 + 4608);
    SCHED();
    hv2_t13 = pin(ex2(hv2_t13));
    SCHED();
    hv2_p11 = pin(hv2_p11 * hv2_t11);
    SCHED();
    hv2_t12 = pin(__builtin_fmaf(hv2_t12, io1, io1));
    SCHED();
    float hv2_t14 = pin(ac[3][1][1][2] * -L2E);
    SCHED();
    SCHED();
    ac[2][0][1] = mma32(cat(Wl[1][3], Wh[1][3]), X[7][0], ac[2][0][1]); SCHED();
    Wl[0][3] = tr_read(cw1 + 4096);
    SCHED();
    hv2_t12 = pin(frcp(hv2_t12));
    SCHED();
    hv2[1][1] = pinu(pk2(hv2_p10, hv2_p11));
    SCHED();
    float hv2_p13 = pin(ac[3][1][1][1] * ac[4][1][1][1]);
    SCHED();
    SCHED();
    ac[2][1][1] = mma32(cat(Wl[1][3], Wh[1][3]), X[7][1], ac[2][1][1]); SCHED();
    v4u q13; ld16c<3072>(q13, l16, kvsrc + 16384);
    Wh[0][3] = tr_read(cw1 + 4608);
    SCHED();
    hv2_t14 = pin(ex2(hv2_t14));
    SCHED();
    hv2_p12 = pin(hv2_p12 * hv2_t12);
    SCHED();
    hv2_t13 = pin(__builtin_fmaf(hv2_t13, io1, io1));
    SCHED();
    float hv2_t15 = pin(ac[3][1][1][3] * -L2E);
    SCHED();
    PRIO(3);
    SCHED();
    BAR(); SCHED();
    STH(10, 9, 98, 96);
    ac[2][0][0] = mma32(cat(Wl[0][0], Wh[0][0]), X[8][0], ac[2][0][0]); SCHED();
    Wl[1][0] = tr_read(cw0 + 8192);
    SCHED();
    hv2_t13 = pin(frcp(hv2_t13));
    SCHED();
    float hv2_p14 = pin(ac[3][1][1][2] * ac[4][1][1][2]);
    SCHED();
    SCHED();
    const char* ws4 = pins(w2b + (un[4] << 15)); setm0<65536>(lw4); SCHED();
    ac[2][1][0] = mma32(cat(Wl[0][0], Wh[0][0]), X[8][1], ac[2][1][0]); SCHED();
    dmaw(w2o0, ws4);
    Wh[1][0] = tr_read(cw0 + 8704);
    SCHED();
    hv2_t15 = pin(ex2(hv2_t15));
    SCHED();
    hv2_p13 = pin(hv2_p13 * hv2_t13);
    SCHED();
    hv2_t14 = pin(__builtin_fmaf(hv2_t14, io1, io1));
    SCHED();
    SCHED();
    ac[2][0][1] = mma32(cat(Wl[0][1], Wh[0][1]), X[8][0], ac[2][0][1]); SCHED();
    Wl[1][1] = tr_read(cw1 + 8192);
    SCHED();
    hv2_t14 = pin(frcp(hv2_t14));
    SCHED();
    hv2[1][2] = pinu(pk2(hv2_p12, hv2_p13));
    SCHED();
    float hv2_p15 = pin(ac[3][1][1][3] * ac[4][1][1][3]);
    SCHED();
    lane = lane_id(); const unsigned l4 = pinu((lane & 31) * 4);
    SCHED();
    SCHED();
    ac[2][1][1] = mma32(cat(Wl[0][1], Wh[0][1]), X[8][1], ac[2][1][1]); SCHED();
    Wh[1][1] = tr_read(cw1 + 8704);
    SCHED();
    WAITC(63, 7);
    SCHED();
    hv2_p14 = pin(hv2_p14 * hv2_t14);
    SCHED();
    hv2_t15 = pin(__builtin_fmaf(hv2_t15, io1, io1));
    SCHED();
    ld4c(fv, l4, fl);
    SCHED();
    SCHED();
    ws4 = pins(ws4 + 4096); setm0<66560>(lw4); SCHED();
    ac[2][0][0] = mma32(cat(Wl[0][2], Wh[0][2]), X[9][0], ac[2][0][0]); SCHED();
    dmaw(w2o1, ws4);
    Wl[1][2] = tr_read(cw0 + 12288);
    SCHED();
    hv2_t15 = pin(frcp(hv2_t15));
    SCHED();
    WAITC(12, 15);
    SCHED();
    tp0 = pinp(xorp(pt0, 32768));
    SCHED();
    SCHED();
    ac[2][1][0] = mma32(cat(Wl[0][2], Wh[0][2]), X[9][1], ac[2][1][0]); SCHED();
    Wh[1][2] = tr_read(cw0 + 12800);
    SCHED();
    const v2u Wa3_0 = tr_read(tp0);
    SCHED();
    hv2_p15 = pin(hv2_p15 * hv2_t15);
    SCHED();
    SCHED();
    ac[2][0][1] = mma32(cat(Wl[0][3], Wh[0][3]), X[9][0], ac[2][0][1]); SCHED();
    Wl[1][3] = tr_read(cw1 + 12288);
    SCHED();
    const v4u Wf3_0 = cat(Wa3_0, tr_read(tp0 + 512));
    SCHED();
    tp1 = pinp(xorp(pt0, 32800));
    SCHED();
    hv2[1][3] = pinu(pk2(hv2_p14, hv2_p15));
    SCHED();
    SCHED();
    ws4 = pins(ws4 + 4096); setm0<67584>(lw4); SCHED();
    ac[2][1][1] = mma32(cat(Wl[0][3], Wh[0][3]), X[9][1], ac[2][1][1]); SCHED();
    dmaw(w2o0, ws4);
    Wh[1][3] = tr_read(cw1 + 12800);
    SCHED();
    const v2u Wa3_1 = tr_read(tp1);
    SCHED();
    st16c<1024>(hv2[1], l16, h2d);
    SCHED();
    PRIO(3);
    SCHED();
    ac[2][0][0] = mma32(cat(Wl[1][0], Wh[1][0]), X[10][0], ac[2][0][0]); SCHED();
    Wl[0][0] = tr_read(cw0 + 16384);
    SCHED();
    const v4u Wf3_1 = cat(Wa3_1, tr_read(tp1 + 512));
    SCHED();
    tp0 = pinp(xorp(pt0, 32832));
    SCHED();
    SCHED();
    ws4 = pins(ws4 + 4096); setm0<68608>(lw4); SCHED();
    ac[2][1][0] = mma32(cat(Wl[1][0], Wh[1][0]), X[10][1], ac[2][1][0]); SCHED();
    dmaw(w2o1, ws4);
    Wh[0][0] = tr_read(cw0 + 16896);
    SCHED();
    const v2u Wa3_2 = tr_read(tp0);
    SCHED();
    SCHED();
    ac[2][0][1] = mma32(cat(Wl[1][1], Wh[1][1]), X[10][0], ac[2][0][1]); SCHED();
    Wl[0][1] = tr_read(cw1 + 16384);
    SCHED();
    const v4u Wf3_2 = cat(Wa3_2, tr_read(tp0 + 512));
    SCHED();
    tp1 = pinp(xorp(pt0, 32864));
    SCHED();
    SCHED();
    ac[2][1][1] = mma32(cat(Wl[1][1], Wh[1][1]), X[10][1], ac[2][1][1]); SCHED();
    Wh[0][1] = tr_read(cw1 + 16896);
    SCHED();
    const v2u Wa3_3 = tr_read(tp1);
    SCHED();
    SCHED();
    const char* ws5 = pins(w2b + (un[5] << 15)); setm0<98304>(lw4); SCHED();
    ac[2][0][0] = mma32(cat(Wl[1][2], Wh[1][2]), X[11][0], ac[2][0][0]); SCHED();
    dmaw(w2o0, ws5);
    Wl[0][2] = tr_read(cw0 + 20480);
    SCHED();
    const v4u Wf3_3 = cat(Wa3_3, tr_read(tp1 + 512));
    SCHED();
    SCHED();
    SCHED();
    SCHED();
    ac[2][1][0] = mma32(cat(Wl[1][2], Wh[1][2]), X[11][1], ac[2][1][0]); SCHED();
    Wh[0][2] = tr_read(cw0 + 20992);
    SCHED();
    SCHED();
    ac[2][0][1] = mma32(cat(Wl[1][3], Wh[1][3]), X[11][0], ac[2][0][1]); SCHED();
    Wl[0][3] = tr_read(cw1 + 20480);
    SCHED();
    SCHED();
    ws5 = pins(ws5 + 4096); setm0<99328>(lw4); SCHED();
    ac[2][1][1] = mma32(cat(Wl[1][3], Wh[1][3]), X[11][1], ac[2][1][1]); SCHED();
    dmaw(w2o1, ws5);
    Wh[0][3] = tr_read(cw1 + 20992);
    SCHED();
    WAITC(6, 15);
    asm volatile("" : "+v"(fv) :: "memory");
    poll(fv, l4, fl, tag, 0xfu);
    SCHED();
    PRIO(2);
    SCHED();
    ac[2][0][0] = mma32(cat(Wl[0][0], Wh[0][0]), X[12][0], ac[2][0][0]); SCHED();
    Wl[1][0] = tr_read(cw0 + 24576);
    SCHED();
    SCHED();
    const char* hs0 = pins(hb + (un[0] << 12)); SCHED();
    ac[2][1][0] = mma32(cat(Wl[0][0], Wh[0][0]), X[12][1], ac[2][1][0]); SCHED();
    v4u h0_0; ld16c<0>(h0_0, l16, hs0);
    Wh[1][0] = tr_read(cw0 + 25088);
    SCHED();
    SCHED();
    ac[2][0][1] = mma32(cat(Wl[0][1], Wh[0][1]), X[12][0], ac[2][0][1]); SCHED();
    Wl[1][1] = tr_read(cw1 + 24576);
    SCHED();
    SCHED();
    ac[2][1][1] = mma32(cat(Wl[0][1], Wh[0][1]), X[12][1], ac[2][1][1]); SCHED();
    Wh[1][1] = tr_read(cw1 + 25088);
    SCHED();
    WAITC(63, 7);
    SCHED();
    SCHED();
    ac[2][0][0] = mma32(cat(Wl[0][2], Wh[0][2]), X[13][0], ac[2][0][0]); SCHED();
    v4u h0_1; ld16c<1024>(h0_1, l16, hs0);
    Wl[1][2] = tr_read(cw0 + 28672);
    SCHED();
    SCHED();
    ac[2][1][0] = mma32(cat(Wl[0][2], Wh[0][2]), X[13][1], ac[2][1][0]); SCHED();
    Wh[1][2] = tr_read(cw0 + 29184);
    SCHED();
    lane = lane_id(); const int dg = (lane & 15) - 4 * (lane >> 4); LAS char* sx2 = pinp(smem + (131072 + wave * 4096 + lane * 16)); LAS char* sr2 = pinp(smem + (131072 + WN * 16384 + lane * 16));
    SCHED();
    const v2u idm = {dg == 0 ? 0x3f80u : dg == 1 ? 0x3f800000u : 0u, dg == 2 ? 0x3f80u : dg == 3 ? 0x3f800000u : 0u};;;;;
    SCHED();
    SCHED();
    ac[2][0][1] = mma32(cat(Wl[0][3], Wh[0][3]), X[13][0], ac[2][0][1]); SCHED();
    Wl[1][3] = tr_read(cw1 + 28672);
    SCHED();
    SCHED();
    ac[2][1][1] = mma32(cat(Wl[0][3], Wh[0][3]), X[13][1], ac[2][1][1]); SCHED();
    v4u h0_2; ld16c<2048>(h0_2, l16, hs0);
    Wh[1][3] = tr_read(cw1 + 29184);
    SCHED();
    PRIO(2);
    SCHED();
    ac[2][0][0] = mma32(cat(Wl[1][0], Wh[1][0]), X[14][0], ac[2][0][0]); SCHED();
    SCHED();
    ac[2][1][0] = mma32(cat(Wl[1][0], Wh[1][0]), X[14][1], ac[2][1][0]); SCHED();
    v4u h0_3; ld16c<3072>(h0_3, l16, hs0);
    SCHED();
    ac[2][0][1] = mma32(cat(Wl[1][1], Wh[1][1]), X[14][0], ac[2][0][1]); SCHED();
    WAITC(7, 15);
    __builtin_amdgcn_raw_buffer_store_b32(tag, wsr, l16 << 22, fo + 32, SC1);
    SCHED();
    SCHED();
    ac[2][1][1] = mma32(cat(Wl[1][1], Wh[1][1]), X[14][1], ac[2][1][1]); SCHED();
    SCHED();
    const char* hs1 = pins(hb + (un[1] << 12)); SCHED();
    ac[2][0][0] = mma32(cat(Wl[1][2], Wh[1][2]), X[15][0], ac[2][0][0]); SCHED();
    v4u h1_0; ld16c<0>(h1_0, l16, hs1);
    SCHED();
    ac[2][1][0] = mma32(cat(Wl[1][2], Wh[1][2]), X[15][1], ac[2][1][0]); SCHED();
    SCHED();
    ac[2][0][1] = mma32(cat(Wl[1][3], Wh[1][3]), X[15][0], ac[2][0][1]); SCHED();
    SCHED();
    ac[2][1][1] = mma32(cat(Wl[1][3], Wh[1][3]), X[15][1], ac[2][1][1]); SCHED();
    v4u h1_1; ld16c<1024>(h1_1, l16, hs1);
    PRIO(1);
    SCHED();
    STH(16, 10, 96, 98);
    STH(15, 16, 98, 96);
    PRIO(1);
    v4u h1_2; ld16c<2048>(h1_2, l16, hs1);
    v4u h1_3; ld16c<3072>(h1_3, l16, hs1);
    asm volatile("" : "+v"(q00), "+v"(q01), "+v"(q02), "+v"(q03), "+v"(q10), "+v"(q11), "+v"(q12), "+v"(q13) :: "memory");
    kv_retry(q00, q01, q02, q03, q10, q11, q12, q13, l16, kvsrc, tag);
    f32x4 k0 = mma16(idm, (v2u){q00[0], q00[1]}, zero4);
    f32x4 k1 = mma16(idm, (v2u){q10[0], q10[1]}, zero4);
    f32x4 kz = mma16(idm, (v2u){q00[2], q10[2]}, zero4);
    k0 = mma16(idm, (v2u){q01[0], q01[1]}, k0);
    k1 = mma16(idm, (v2u){q11[0], q11[1]}, k1);
    kz = mma16(idm, (v2u){q01[2], q11[2]}, kz);
    k0 = mma16(idm, (v2u){q02[0], q02[1]}, k0);
    k1 = mma16(idm, (v2u){q12[0], q12[1]}, k1);
    kz = mma16(idm, (v2u){q02[2], q12[2]}, kz);
    k0 = mma16(idm, (v2u){q03[0], q03[1]}, k0);
    k1 = mma16(idm, (v2u){q13[0], q13[1]}, k1);
    kz = mma16(idm, (v2u){q03[2], q13[2]}, kz);
    const v2u pq00 = {phi2(lo2(ac[2][0][0])), phi2(hi2(ac[2][0][0]))};
    const v2u pq01 = {phi2(lo2(ac[2][0][1])), phi2(hi2(ac[2][0][1]))};
    *(LAS f32x4*)sx2 = k0;;
    *(LAS f32x4*)(sx2 + 1024) = k1;;
    *(LAS f32x4*)(sx2 + 2048) = kz;
    SCHED();
    STH(31, 15, 96, 98);
    WAITC(63, 0);
    BAR(); SCHED();
    STH(32, 31, 98, 96);
    SCHED();
    PRIO(1);
    ws5 = pins(ws5 + 4096); setm0<100352>(lw4); NOP();
    dmaw(w2o0, ws5);
    ws5 = pins(ws5 + 4096); setm0<101376>(lw4); NOP();
    dmaw(w2o1, ws5);
    f32x4 xu0 = *(LAS f32x4*)(sr2 + 0);
    f32x4 xu1 = *(LAS f32x4*)(sr2 + 4096);
    f32x4 xu2 = *(LAS f32x4*)(sr2 + 8192);
    f32x4 xu3 = *(LAS f32x4*)(sr2 + 12288);
    f32x4 xv0 = *(LAS f32x4*)(sr2 + 1024);
    f32x4 xv1 = *(LAS f32x4*)(sr2 + 5120);
    f32x4 xv2 = *(LAS f32x4*)(sr2 + 9216);
    f32x4 xv3 = *(LAS f32x4*)(sr2 + 13312);
    f32x4 xz0 = *(LAS f32x4*)(sr2 + 2048);
    f32x4 xz1 = *(LAS f32x4*)(sr2 + 6144);
    f32x4 xz2 = *(LAS f32x4*)(sr2 + 10240);
    f32x4 xz3 = *(LAS f32x4*)(sr2 + 14336);
    SCHED();
    const v2u pq10 = {phi2(lo2(ac[2][1][0])), phi2(hi2(ac[2][1][0]))};
    const v2u pq11 = {phi2(lo2(ac[2][1][1])), phi2(hi2(ac[2][1][1]))};
    SCHED();
    PRIO(1);
    const char* ws6 = pins(w2b + whl); setm0<32768>(lw4); NOP();
    dmaw(w2o0, ws6);
    ws6 = pins(ws6 + 4096); setm0<33792>(lw4); NOP();
    dmaw(w2o1, ws6);
    xu0 += xu1; xu2 += xu3;
    xu0 += xu2;
    xv0 += xv1; xv2 += xv3;
    xv0 += xv2;
    xz0 += xz1; xz2 += xz3;
    xz0 += xz2;
    const v2u kA0 = {pk2(xu0[0], xu0[1]), pk2(xu0[2], xu0[3])}, kA1 = {pk2(xv0[0], xv0[1]), pk2(xv0[2], xv0[3])};
    const unsigned zp0 = pk2(xz0[0], xz0[1]), zp1 = pk2(xz0[2], xz0[3]);
    const v2u zA0 = {qp<0xA0>(zp0), qp<0xF5>(zp0)}, zA1 = {qp<0xA0>(zp1), qp<0xF5>(zp1)};
    v4u ov[2];
    const f32x4 dn00 = mma16(zA0, pq00, eps4), nm00 = mma16(kA0, pq00, zero4);
    const f32x4 dn01 = mma16(zA1, pq01, eps4), nm01 = mma16(kA1, pq01, zero4);
    const f32x4 dn10 = mma16(zA0, pq10, eps4), nm10 = mma16(kA0, pq10, zero4);
    const f32x4 dn11 = mma16(zA1, pq11, eps4), nm11 = mma16(kA1, pq11, zero4);
    { const float r = frcp(dn00[0] * ig0); ov[0][0] = pkv(lo2(nm00) * r); ov[0][1] = pkv(hi2(nm00) * r); }
    { const float r = frcp(dn01[0] * ig0); ov[0][2] = pkv(lo2(nm01) * r); ov[0][3] = pkv(hi2(nm01) * r); }
    { const float r = frcp(dn10[0] * ig1); ov[1][0] = pkv(lo2(nm10) * r); ov[1][1] = pkv(hi2(nm10) * r); }
    { const float r = frcp(dn11[0] * ig1); ov[1][2] = pkv(lo2(nm11) * r); ov[1][3] = pkv(hi2(nm11) * r); }
    ld4c(fv, l4, fl);
    st16c<0>(ov[0], l16, h3d);
    st16c<1024>(ov[1], l16, h3d);
    SCHED();
    STH(18, 32, 96, 98);
    PRIO(0);
    STH(17, 18, 98, 96);
    SCHED();
    WAITC(12, 15);
    asm volatile("" : "+v"(h0_0), "+v"(h0_1), "+v"(h0_2), "+v"(h0_3) :: "memory");
    c2[0][0] = mma32(Wf0_0, h0_0, zero4); SCHED();
    SCHED();
    ws6 = pins(ws6 + 4096); setm0<34816>(lw4); SCHED();
    c2[0][1] = mma32(Wf0_1, h0_0, zero4); SCHED();
    dmaw(w2o0, ws6);
    SCHED();
    c2[0][2] = mma32(Wf0_2, h0_0, zero4); SCHED();
    SCHED();
    ws6 = pins(ws6 + 4096); setm0<35840>(lw4); SCHED();
    c2[0][3] = mma32(Wf0_3, h0_0, zero4); SCHED();
    dmaw(w2o1, ws6);
    SCHED();
    c2[1][0] = mma32(Wf0_0, h0_1, zero4); SCHED();
    SCHED();
    const char* ws7 = pins(wob + (un[7] << 15)); setm0<0>(lw4); SCHED();
    c2[1][1] = mma32(Wf0_1, h0_1, zero4); SCHED();
    dmaw(w2o0, ws7);
    SCHED();
    c2[1][2] = mma32(Wf0_2, h0_1, zero4); SCHED();
    SCHED();
    ws7 = pins(ws7 + 4096); setm0<1024>(lw4); SCHED();
    c2[1][3] = mma32(Wf0_3, h0_1, zero4); SCHED();
    dmaw(w2o1, ws7);
    SCHED();
    c2[2][0] = mma32(Wf0_0, h0_2, zero4); SCHED();
    SCHED();
    ws7 = pins(ws7 + 4096); setm0<2048>(lw4); SCHED();
    c2[2][1] = mma32(Wf0_1, h0_2, zero4); SCHED();
    dmaw(w2o0, ws7);
    SCHED();
    c2[2][2] = mma32(Wf0_2, h0_2, zero4); SCHED();
    SCHED();
    ws7 = pins(ws7 + 4096); setm0<3072>(lw4); SCHED();
    c2[2][3] = mma32(Wf0_3, h0_2, zero4); SCHED();
    dmaw(w2o1, ws7);
    SCHED();
    c2[3][0] = mma32(Wf0_0, h0_3, zero4); SCHED();
    SCHED();
    c2[3][1] = mma32(Wf0_1, h0_3, zero4); SCHED();
    SCHED();
    c2[3][2] = mma32(Wf0_2, h0_3, zero4); SCHED();
    SCHED();
    c2[3][3] = mma32(Wf0_3, h0_3, zero4); SCHED();
    SCHED();
    STH(19, 17, 96, 98);
    WAITC(8, 15);
    asm volatile("" : "+v"(fv) :: "memory");
    poll(fv, l4, fl, tag, 0xff0u);
    STH(30, 19, 98, 96);
    const char* hs2 = pins(hb + (un[2] << 12));
    v4u h2_0; ld16c<0>(h2_0, l16, hs2);
    v4u h2_1; ld16c<1024>(h2_1, l16, hs2);
    v4u h2_2; ld16c<2048>(h2_2, l16, hs2);
    v4u h2_3; ld16c<3072>(h2_3, l16, hs2);
    const char* hs3 = pins(hb + (un[3] << 12));
    v4u h3_0; ld16c<0>(h3_0, l16, hs3);
    v4u h3_1; ld16c<1024>(h3_1, l16, hs3);
    v4u h3_2; ld16c<2048>(h3_2, l16, hs3);
    v4u h3_3; ld16c<3072>(h3_3, l16, hs3);
    SCHED();
    asm volatile("" : "+v"(h1_0), "+v"(h1_1), "+v"(h1_2), "+v"(h1_3) :: "memory");
    c2[0][0] = mma32(Wf1_0, h1_0, c2[0][0]); SCHED();
    SCHED();
    tp0 = pinp(xorp(pt0, 65536));
    SCHED();
    SCHED();
    const char* hs4 = pins(hb + (un[4] << 12)); SCHED();
    c2[0][1] = mma32(Wf1_1, h1_0, c2[0][1]); SCHED();
    v4u h4_0; ld16c<0>(h4_0, l16, hs4);
    const v2u Wa4_0 = tr_read(tp0);
    SCHED();
    SCHED();
    c2[0][2] = mma32(Wf1_2, h1_0, c2[0][2]); SCHED();
    const v4u Wf4_0 = cat(Wa4_0, tr_read(tp0 + 512));
    SCHED();
    tp1 = pinp(xorp(pt0, 65568));
    SCHED();
    SCHED();
    c2[0][3] = mma32(Wf1_3, h1_0, c2[0][3]); SCHED();
    v4u h4_1; ld16c<1024>(h4_1, l16, hs4);
    const v2u Wa4_1 = tr_read(tp1);
    SCHED();
    SCHED();
    c2[1][0] = mma32(Wf1_0, h1_1, c2[1][0]); SCHED();
    const v4u Wf4_1 = cat(Wa4_1, tr_read(tp1 + 512));
    SCHED();
    tp0 = pinp(xorp(pt0, 65600));
    SCHED();
    SCHED();
    c2[1][1] = mma32(Wf1_1, h1_1, c2[1][1]); SCHED();
    v4u h4_2; ld16c<2048>(h4_2, l16, hs4);
    const v2u Wa4_2 = tr_read(tp0);
    SCHED();
    SCHED();
    c2[1][2] = mma32(Wf1_2, h1_1, c2[1][2]); SCHED();
    const v4u Wf4_2 = cat(Wa4_2, tr_read(tp0 + 512));
    SCHED();
    tp1 = pinp(xorp(pt0, 65632));
    SCHED();
    SCHED();
    c2[1][3] = mma32(Wf1_3, h1_1, c2[1][3]); SCHED();
    v4u h4_3; ld16c<3072>(h4_3, l16, hs4);
    const v2u Wa4_3 = tr_read(tp1);
    SCHED();
    SCHED();
    c2[2][0] = mma32(Wf1_0, h1_2, c2[2][0]); SCHED();
    const v4u Wf4_3 = cat(Wa4_3, tr_read(tp1 + 512));
    SCHED();
    SCHED();
    SCHED();
    SCHED();
    const char* hs5 = pins(hb + (un[5] << 12)); SCHED();
    c2[2][1] = mma32(Wf1_1, h1_2, c2[2][1]); SCHED();
    v4u h5_0; ld16c<0>(h5_0, l16, hs5);
    SCHED();
    c2[2][2] = mma32(Wf1_2, h1_2, c2[2][2]); SCHED();
    SCHED();
    c2[2][3] = mma32(Wf1_3, h1_2, c2[2][3]); SCHED();
    v4u h5_1; ld16c<1024>(h5_1, l16, hs5);
    SCHED();
    c2[3][0] = mma32(Wf1_0, h1_3, c2[3][0]); SCHED();
    SCHED();
    c2[3][1] = mma32(Wf1_1, h1_3, c2[3][1]); SCHED();
    v4u h5_2; ld16c<2048>(h5_2, l16, hs5);
    SCHED();
    c2[3][2] = mma32(Wf1_2, h1_3, c2[3][2]); SCHED();
    SCHED();
    c2[3][3] = mma32(Wf1_3, h1_3, c2[3][3]); SCHED();
    v4u h5_3; ld16c<3072>(h5_3, l16, hs5);
    SCHED();
    STH(20, 30, 96, 98);
    lane = lane_id(); FRESH(); LAS char* rw = pinp(smem + (wave * 4096 + (lane >> 4) * 256 + (((lane & 15) ^ (2 * (lane >> 5))) * 16)));
    const int ro_ = lane & 7, rc_ = (wave & 1) * 8 + (lane >> 3);
    LAS char* rr = pinp(smem + (0 + (wave >> 1) * SLOT + (ro_ >> 1) * 1024 + (ro_ & 1) * 512 + ((rc_ ^ (2 * ro_)) & 15) * 16));
    const unsigned oo = pinu(((wave >> 1) * 16 + rc_) * 1024 + ro_ * 16); char* op_ = pins(mkptr(ka[14], ka[15]) + (unsigned)(((bid & 7) * 4 + ((bid >> 3) & 3)) * 65536 + (bid >> 5) * 128));
    SCHED();
    WAITC(12, 15);
    asm volatile("" : "+v"(h2_0), "+v"(h2_1), "+v"(h2_2), "+v"(h2_3) :: "memory");
    c2[0][0] = mma32(Wf2_0, h2_0, c2[0][0]); SCHED();
    SCHED();
    tp0 = pinp(xorp(pt0, 98304));
    SCHED();
    SCHED();
    const char* hs6 = pins(hb + (un[6] << 12)); SCHED();
    c2[0][1] = mma32(Wf2_1, h2_0, c2[0][1]); SCHED();
    v4u h6_0; ld16c<0>(h6_0, l16, hs6);
    const v2u Wa5_0 = tr_read(tp0);
    SCHED();
    SCHED();
    c2[0][2] = mma32(Wf2_2, h2_0, c2[0][2]); SCHED();
    const v4u Wf5_0 = cat(Wa5_0, tr_read(tp0 + 512));
    SCHED();
    tp1 = pinp(xorp(pt0, 98336));
    SCHED();
    SCHED();
    c2[0][3] = mma32(Wf2_3, h2_0, c2[0][3]); SCHED();
    v4u h6_1; ld16c<1024>(h6_1, l16, hs6);
    const v2u Wa5_1 = tr_read(tp1);
    SCHED();
    SCHED();
    c2[1][0] = mma32(Wf2_0, h2_1, c2[1][0]); SCHED();
    const v4u Wf5_1 = cat(Wa5_1, tr_read(tp1 + 512));
    SCHED();
    tp0 = pinp(xorp(pt0, 98368));
    SCHED();
    SCHED();
    c2[1][1] = mma32(Wf2_1, h2_1, c2[1][1]); SCHED();
    v4u h6_2; ld16c<2048>(h6_2, l16, hs6);
    const v2u Wa5_2 = tr_read(tp0);
    SCHED();
    SCHED();
    c2[1][2] = mma32(Wf2_2, h2_1, c2[1][2]); SCHED();
    const v4u Wf5_2 = cat(Wa5_2, tr_read(tp0 + 512));
    SCHED();
    tp1 = pinp(xorp(pt0, 98400));
    SCHED();
    SCHED();
    c2[1][3] = mma32(Wf2_3, h2_1, c2[1][3]); SCHED();
    v4u h6_3; ld16c<3072>(h6_3, l16, hs6);
    const v2u Wa5_3 = tr_read(tp1);
    SCHED();
    SCHED();
    c2[2][0] = mma32(Wf2_0, h2_2, c2[2][0]); SCHED();
    const v4u Wf5_3 = cat(Wa5_3, tr_read(tp1 + 512));
    SCHED();
    SCHED();
    SCHED();
    SCHED();
    c2[2][1] = mma32(Wf2_1, h2_2, c2[2][1]); SCHED();
    SCHED();
    c2[2][2] = mma32(Wf2_2, h2_2, c2[2][2]); SCHED();
    SCHED();
    c2[2][3] = mma32(Wf2_3, h2_2, c2[2][3]); SCHED();
    SCHED();
    c2[3][0] = mma32(Wf2_0, h2_3, c2[3][0]); SCHED();
    SCHED();
    c2[3][1] = mma32(Wf2_1, h2_3, c2[3][1]); SCHED();
    SCHED();
    c2[3][2] = mma32(Wf2_2, h2_3, c2[3][2]); SCHED();
    SCHED();
    c2[3][3] = mma32(Wf2_3, h2_3, c2[3][3]); SCHED();
    WAITC(63, 0);
    SCHED();
    SCHED();
    STH(21, 20, 98, 96);
    SCHED();
    WAITC(12, 15);
    asm volatile("" : "+v"(h3_0), "+v"(h3_1), "+v"(h3_2), "+v"(h3_3) :: "memory");
    c2[0][0] = mma32(Wf3_0, h3_0, c2[0][0]); SCHED();
    SCHED();
    tp0 = pinp(xorp(pt0, 32768));
    SCHED();
    SCHED();
    const char* ws8 = pins(wob + (un[8] << 15)); setm0<98304>(lw4); SCHED();
    c2[0][1] = mma32(Wf3_1, h3_0, c2[0][1]); SCHED();
    dmaw(w2o0, ws8);
    const v2u Wa6_0 = tr_read(tp0);
    SCHED();
    SCHED();
    c2[0][2] = mma32(Wf3_2, h3_0, c2[0][2]); SCHED();
    const v4u Wf6_0 = cat(Wa6_0, tr_read(tp0 + 512));
    SCHED();
    tp1 = pinp(xorp(pt0, 32800));
    SCHED();
    SCHED();
    ws8 = pins(ws8 + 4096); setm0<99328>(lw4); SCHED();
    c2[0][3] = mma32(Wf3_3, h3_0, c2[0][3]); SCHED();
    dmaw(w2o1, ws8);
    const v2u Wa6_1 = tr_read(tp1);
    SCHED();
    SCHED();
    c2[1][0] = mma32(Wf3_0, h3_1, c2[1][0]); SCHED();
    const v4u Wf6_1 = cat(Wa6_1, tr_read(tp1 + 512));
    SCHED();
    tp0 = pinp(xorp(pt0, 32832));
    SCHED();
    SCHED();
    ws8 = pins(ws8 + 4096); setm0<100352>(lw4); SCHED();
    c2[1][1] = mma32(Wf3_1, h3_1, c2[1][1]); SCHED();
    dmaw(w2o0, ws8);
    const v2u Wa6_2 = tr_read(tp0);
    SCHED();
    SCHED();
    c2[1][2] = mma32(Wf3_2, h3_1, c2[1][2]); SCHED();
    const v4u Wf6_2 = cat(Wa6_2, tr_read(tp0 + 512));
    SCHED();
    tp1 = pinp(xorp(pt0, 32864));
    SCHED();
    SCHED();
    ws8 = pins(ws8 + 4096); setm0<101376>(lw4); SCHED();
    c2[1][3] = mma32(Wf3_3, h3_1, c2[1][3]); SCHED();
    dmaw(w2o1, ws8);
    const v2u Wa6_3 = tr_read(tp1);
    SCHED();
    SCHED();
    c2[2][0] = mma32(Wf3_0, h3_2, c2[2][0]); SCHED();
    const v4u Wf6_3 = cat(Wa6_3, tr_read(tp1 + 512));
    SCHED();
    SCHED();
    SCHED();
    SCHED();
    const char* hs7 = pins(hb + (un[7] << 12)); SCHED();
    c2[2][1] = mma32(Wf3_1, h3_2, c2[2][1]); SCHED();
    v4u h7_0; ld16c<0>(h7_0, l16, hs7);
    SCHED();
    c2[2][2] = mma32(Wf3_2, h3_2, c2[2][2]); SCHED();
    SCHED();
    c2[2][3] = mma32(Wf3_3, h3_2, c2[2][3]); SCHED();
    v4u h7_1; ld16c<1024>(h7_1, l16, hs7);
    SCHED();
    c2[3][0] = mma32(Wf3_0, h3_3, c2[3][0]); SCHED();
    SCHED();
    c2[3][1] = mma32(Wf3_1, h3_3, c2[3][1]); SCHED();
    v4u h7_2; ld16c<2048>(h7_2, l16, hs7);
    SCHED();
    c2[3][2] = mma32(Wf3_2, h3_3, c2[3][2]); SCHED();
    SCHED();
    c2[3][3] = mma32(Wf3_3, h3_3, c2[3][3]); SCHED();
    v4u h7_3; ld16c<3072>(h7_3, l16, hs7);
    SCHED();
    STH(22, 21, 96, 98);
    SCHED();
    WAITC(16, 15);
    asm volatile("" : "+v"(h4_0), "+v"(h4_1), "+v"(h4_2), "+v"(h4_3) :: "memory");
    c2[0][0] = mma32(Wf4_0, h4_0, c2[0][0]); SCHED();
    SCHED();
    tp0 = pinp(xorp(pt0, 0));
    SCHED();
    SCHED();
    const char* hs8 = pins(hb + (un[8] << 12)); SCHED();
    c2[0][1] = mma32(Wf4_1, h4_0, c2[0][1]); SCHED();
    v4u h8_0; ld16c<0>(h8_0, l16, hs8);
    const v2u Wa7_0 = tr_read(tp0);
    SCHED();
    SCHED();
    c2[0][2] = mma32(Wf4_2, h4_0, c2[0][2]); SCHED();
    const v4u Wf7_0 = cat(Wa7_0, tr_read(tp0 + 512));
    SCHED();
    tp1 = pinp(xorp(pt0, 32));
    SCHED();
    SCHED();
    c2[0][3] = mma32(Wf4_3, h4_0, c2[0][3]); SCHED();
    v4u h8_1; ld16c<1024>(h8_1, l16, hs8);
    const v2u Wa7_1 = tr_read(tp1);
    SCHED();
    SCHED();
    c2[1][0] = mma32(Wf4_0, h4_1, c2[1][0]); SCHED();
    const v4u Wf7_1 = cat(Wa7_1, tr_read(tp1 + 512));
    SCHED();
    tp0 = pinp(xorp(pt0, 64));
    SCHED();
    SCHED();
    c2[1][1] = mma32(Wf4_1, h4_1, c2[1][1]); SCHED();
    v4u h8_2; ld16c<2048>(h8_2, l16, hs8);
    const v2u Wa7_2 = tr_read(tp0);
    SCHED();
    SCHED();
    c2[1][2] = mma32(Wf4_2, h4_1, c2[1][2]); SCHED();
    const v4u Wf7_2 = cat(Wa7_2, tr_read(tp0 + 512));
    SCHED();
    tp1 = pinp(xorp(pt0, 96));
    SCHED();
    SCHED();
    c2[1][3] = mma32(Wf4_3, h4_1, c2[1][3]); SCHED();
    v4u h8_3; ld16c<3072>(h8_3, l16, hs8);
    const v2u Wa7_3 = tr_read(tp1);
    SCHED();
    SCHED();
    c2[2][0] = mma32(Wf4_0, h4_2, c2[2][0]); SCHED();
    const v4u Wf7_3 = cat(Wa7_3, tr_read(tp1 + 512));
    SCHED();
    SCHED();
    SCHED();
    SCHED();
    c2[2][1] = mma32(Wf4_1, h4_2, c2[2][1]); SCHED();
    SCHED();
    c2[2][2] = mma32(Wf4_2, h4_2, c2[2][2]); SCHED();
    SCHED();
    c2[2][3] = mma32(Wf4_3, h4_2, c2[2][3]); SCHED();
    SCHED();
    c2[3][0] = mma32(Wf4_0, h4_3, c2[3][0]); SCHED();
    SCHED();
    c2[3][1] = mma32(Wf4_1, h4_3, c2[3][1]); SCHED();
    SCHED();
    c2[3][2] = mma32(Wf4_2, h4_3, c2[3][2]); SCHED();
    SCHED();
    c2[3][3] = mma32(Wf4_3, h4_3, c2[3][3]); SCHED();
    SCHED();
    STH(23, 22, 98, 96);
    SCHED();
    WAITC(16, 15);
    asm volatile("" : "+v"(h5_0), "+v"(h5_1), "+v"(h5_2), "+v"(h5_3) :: "memory");
    c2[0][0] = mma32(Wf5_0, h5_0, c2[0][0]); SCHED();
    WAITC(8, 15);
    SCHED();
    tp0 = pinp(xorp(pt0, 98304));
    SCHED();
    SCHED();
    c2[0][1] = mma32(Wf5_1, h5_0, c2[0][1]); SCHED();
    const v2u Wa8_0 = tr_read(tp0);
    SCHED();
    SCHED();
    c2[0][2] = mma32(Wf5_2, h5_0, c2[0][2]); SCHED();
    const v4u Wf8_0 = cat(Wa8_0, tr_read(tp0 + 512));
    SCHED();
    tp1 = pinp(xorp(pt0, 98336));
    SCHED();
    SCHED();
    c2[0][3] = mma32(Wf5_3, h5_0, c2[0][3]); SCHED();
    const v2u Wa8_1 = tr_read(tp1);
    SCHED();
    SCHED();
    c2[1][0] = mma32(Wf5_0, h5_1, c2[1][0]); SCHED();
    const v4u Wf8_1 = cat(Wa8_1, tr_read(tp1 + 512));
    SCHED();
    tp0 = pinp(xorp(pt0, 98368));
    SCHED();
    SCHED();
    c2[1][1] = mma32(Wf5_1, h5_1, c2[1][1]); SCHED();
    const v2u Wa8_2 = tr_read(tp0);
    SCHED();
    SCHED();
    c2[1][2] = mma32(Wf5_2, h5_1, c2[1][2]); SCHED();
    const v4u Wf8_2 = cat(Wa8_2, tr_read(tp0 + 512));
    SCHED();
    tp1 = pinp(xorp(pt0, 98400));
    SCHED();
    SCHED();
    c2[1][3] = mma32(Wf5_3, h5_1, c2[1][3]); SCHED();
    const v2u Wa8_3 = tr_read(tp1);
    SCHED();
    SCHED();
    c2[2][0] = mma32(Wf5_0, h5_2, c2[2][0]); SCHED();
    const v4u Wf8_3 = cat(Wa8_3, tr_read(tp1 + 512));
    SCHED();
    SCHED();
    asm volatile("" : "+v"(h6_0), "+v"(h6_1), "+v"(h6_2), "+v"(h6_3) :: "memory");
    h_check(h6_0, h6_1, h6_2, h6_3, l16, hs6); SCHED();
    v4u hf0 = {h6_0[0], h6_0[1], h6_1[0], h6_1[1]};
    v4u hf1 = {h6_0[2], h6_0[3], h6_1[2], h6_1[3]};
    v4u hf2 = {h6_2[0], h6_2[1], h6_3[0], h6_3[1]};
    v4u hf3 = {h6_2[2], h6_2[3], h6_3[2], h6_3[3]};
    SCHED();
    SCHED();
    SCHED();
    SCHED();
    c2[2][1] = mma32(Wf5_1, h5_2, c2[2][1]); SCHED();
    SCHED();
    c2[2][2] = mma32(Wf5_2, h5_2, c2[2][2]); SCHED();
    SCHED();
    c2[2][3] = mma32(Wf5_3, h5_2, c2[2][3]); SCHED();
    SCHED();
    c2[3][0] = mma32(Wf5_0, h5_3, c2[3][0]); SCHED();
    SCHED();
    c2[3][1] = mma32(Wf5_1, h5_3, c2[3][1]); SCHED();
    SCHED();
    WAITC(4, 15);
    asm volatile("" : "+v"(h7_0), "+v"(h7_1), "+v"(h7_2), "+v"(h7_3) :: "memory");
    h_check(h7_0, h7_1, h7_2, h7_3, l16, hs7); SCHED();
    SCHED();
    SCHED();
    c2[3][2] = mma32(Wf5_2, h5_3, c2[3][2]); SCHED();
    SCHED();
    c2[3][3] = mma32(Wf5_3, h5_3, c2[3][3]); SCHED();
    SCHED();
    STH(24, 23, 96, 98);
    SCHED();
    WAITC(0, 15);
    asm volatile("" : "+v"(h8_0), "+v"(h8_1), "+v"(h8_2), "+v"(h8_3) :: "memory");
    h_check(h8_0, h8_1, h8_2, h8_3, l16, hs8); SCHED();
    STH(26, 24, 98, 96);
    c2[0][0] = mma32(Wf6_0, hf0, c2[0][0]); SCHED();
    SCHED();
    c2[0][1] = mma32(Wf6_1, hf0, c2[0][1]); SCHED();
    SCHED();
    c2[0][2] = mma32(Wf6_2, hf0, c2[0][2]); SCHED();
    SCHED();
    c2[0][3] = mma32(Wf6_3, hf0, c2[0][3]); SCHED();
    SCHED();
    c2[0][0] = mma32(Wf7_0, h7_0, c2[0][0]); SCHED();
    SCHED();
    c2[0][1] = mma32(Wf7_1, h7_0, c2[0][1]); SCHED();
    SCHED();
    c2[0][2] = mma32(Wf7_2, h7_0, c2[0][2]); SCHED();
    SCHED();
    c2[0][3] = mma32(Wf7_3, h7_0, c2[0][3]); SCHED();
    SCHED();
    c2[0][0] = mma32(Wf8_0, h8_0, c2[0][0]); SCHED();
    SCHED();
    c2[0][1] = mma32(Wf8_1, h8_0, c2[0][1]); SCHED();
    SCHED();
    c2[0][2] = mma32(Wf8_2, h8_0, c2[0][2]); SCHED();
    SCHED();
    c2[0][3] = mma32(Wf8_3, h8_0, c2[0][3]); SCHED();
    SCHED();
    c2[1][0] = mma32(Wf6_0, hf1, c2[1][0]); SCHED();
    SCHED();
    c2[1][1] = mma32(Wf6_1, hf1, c2[1][1]); SCHED();
    tp0 = pinp(xorp(rw, 0)); *(LAS f32x4*)tp0 = c2[0][0];
    SCHED();
    c2[1][2] = mma32(Wf6_2, hf1, c2[1][2]); SCHED();
    SCHED();
    c2[1][3] = mma32(Wf6_3, hf1, c2[1][3]); SCHED();
    SCHED();
    c2[1][0] = mma32(Wf7_0, h7_1, c2[1][0]); SCHED();
    tp0 = pinp(xorp(rw, 1088)); *(LAS f32x4*)tp0 = c2[0][1];
    SCHED();
    c2[1][1] = mma32(Wf7_1, h7_1, c2[1][1]); SCHED();
    SCHED();
    c2[1][2] = mma32(Wf7_2, h7_1, c2[1][2]); SCHED();
    SCHED();
    c2[1][3] = mma32(Wf7_3, h7_1, c2[1][3]); SCHED();
    tp0 = pinp(xorp(rw, 2176)); *(LAS f32x4*)tp0 = c2[0][2];
    SCHED();
    c2[1][0] = mma32(Wf8_0, h8_1, c2[1][0]); SCHED();
    SCHED();
    c2[1][1] = mma32(Wf8_1, h8_1, c2[1][1]); SCHED();
    SCHED();
    c2[1][2] = mma32(Wf8_2, h8_1, c2[1][2]); SCHED();
    tp0 = pinp(xorp(rw, 3264)); *(LAS f32x4*)tp0 = c2[0][3];
    SCHED();
    c2[1][3] = mma32(Wf8_3, h8_1, c2[1][3]); SCHED();
    SCHED();
    c2[2][0] = mma32(Wf6_0, hf2, c2[2][0]); SCHED();
    SCHED();
    c2[2][1] = mma32(Wf6_1, hf2, c2[2][1]); SCHED();
    tp0 = pinp(xorp(rw, 32768)); *(LAS f32x4*)tp0 = c2[1][0];
    SCHED();
    c2[2][2] = mma32(Wf6_2, hf2, c2[2][2]); SCHED();
    SCHED();
    c2[2][3] = mma32(Wf6_3, hf2, c2[2][3]); SCHED();
    SCHED();
    c2[2][0] = mma32(Wf7_0, h7_2, c2[2][0]); SCHED();
    tp0 = pinp(xorp(rw, 33856)); *(LAS f32x4*)tp0 = c2[1][1];
    SCHED();
    c2[2][1] = mma32(Wf7_1, h7_2, c2[2][1]); SCHED();
    SCHED();
    c2[2][2] = mma32(Wf7_2, h7_2, c2[2][2]); SCHED();
    SCHED();
    c2[2][3] = mma32(Wf7_3, h7_2, c2[2][3]); SCHED();
    tp0 = pinp(xorp(rw, 34944)); *(LAS f32x4*)tp0 = c2[1][2];
    SCHED();
    c2[2][0] = mma32(Wf8_0, h8_2, c2[2][0]); SCHED();
    SCHED();
    c2[2][1] = mma32(Wf8_1, h8_2, c2[2][1]); SCHED();
    SCHED();
    c2[2][2] = mma32(Wf8_2, h8_2, c2[2][2]); SCHED();
    tp0 = pinp(xorp(rw, 36032)); *(LAS f32x4*)tp0 = c2[1][3];
    SCHED();
    c2[2][3] = mma32(Wf8_3, h8_2, c2[2][3]); SCHED();
    SCHED();
    c2[3][0] = mma32(Wf6_0, hf3, c2[3][0]); SCHED();
    SCHED();
    c2[3][1] = mma32(Wf6_1, hf3, c2[3][1]); SCHED();
    tp0 = pinp(xorp(rw, 65536)); *(LAS f32x4*)tp0 = c2[2][0];
    SCHED();
    c2[3][2] = mma32(Wf6_2, hf3, c2[3][2]); SCHED();
    SCHED();
    c2[3][3] = mma32(Wf6_3, hf3, c2[3][3]); SCHED();
    SCHED();
    c2[3][0] = mma32(Wf7_0, h7_3, c2[3][0]); SCHED();
    tp0 = pinp(xorp(rw, 66624)); *(LAS f32x4*)tp0 = c2[2][1];
    SCHED();
    c2[3][1] = mma32(Wf7_1, h7_3, c2[3][1]); SCHED();
    SCHED();
    c2[3][2] = mma32(Wf7_2, h7_3, c2[3][2]); SCHED();
    SCHED();
    c2[3][3] = mma32(Wf7_3, h7_3, c2[3][3]); SCHED();
    tp0 = pinp(xorp(rw, 67712)); *(LAS f32x4*)tp0 = c2[2][2];
    SCHED();
    c2[3][0] = mma32(Wf8_0, h8_3, c2[3][0]); SCHED();
    SCHED();
    c2[3][1] = mma32(Wf8_1, h8_3, c2[3][1]); SCHED();
    SCHED();
    c2[3][2] = mma32(Wf8_2, h8_3, c2[3][2]); SCHED();
    tp0 = pinp(xorp(rw, 68800)); *(LAS f32x4*)tp0 = c2[2][3];
    SCHED();
    c2[3][3] = mma32(Wf8_3, h8_3, c2[3][3]); SCHED();
    SCHED();
    tp0 = pinp(xorp(rw, 98304)); *(LAS f32x4*)tp0 = c2[3][0];
    tp0 = pinp(xorp(rw, 99392)); *(LAS f32x4*)tp0 = c2[3][1];
    tp0 = pinp(xorp(rw, 100480)); *(LAS f32x4*)tp0 = c2[3][2];
    tp0 = pinp(xorp(rw, 101568)); *(LAS f32x4*)tp0 = c2[3][3];
    STH(27, 26, 96, 98);
    STH(27, 27, 98, 96); finish(rr, oo, op_);
  } else {
    FRESH(); asm volatile("" : "+v"(lane));
    char* ws = mkptr(kb[0], kb[1]); const unsigned l16 = lane * 16;
    const unsigned gtok = TB * 128 + ((bid & 3) * 4 + (bid >> 6)) * 8 + wave;
    unsigned kX[8];
    for (int i = 0; i < 8; ++i) kX[i] = pins(KT(i) * 128);
    const __amdgpu_buffer_rsrc_t wsr = __builtin_amdgcn_make_buffer_rsrc(ws, 0, WS_USED, 0x00020000);
    unsigned kF[8];
#pragma unroll
    for (int i = 0; i < 8; ++i) kF[i] = pins(KT(i) * (64 * 3584));
    const int r4 = bid >> 5, tb2 = (bid & 7) * 4 + ((bid >> 3) & 3);
    const char* w2b = pins(mkptr(ka[10], ka[11]) + (unsigned)((448 * (wave >> 1) + 192 * (wave & 1)) * 1024 + (bid >> 5) * 128));
    const char* wob = pins(mkptr(ka[4], ka[5]) + (unsigned)(((128 * (wave + 4) - (ka[2] & 896)) & 896) * 512 + (bid >> 5) * 128) - 7 * 32768);
    const unsigned whl = pins((384 - 160 * (wave & 1)) * 1024);
    const char* fl = pins(ws + (unsigned)(OFF_F + (tb2 * 8 + wave) * 128));
    const char* hb = pins(ws + (unsigned)(OFF_H + (tb2 * 8 + wave) * 36864));
    char* hdst = pins(ws + (unsigned)(OFF_H + ((2 * TB + (WM >> 1)) * 8 + EG) * 36864 + (WM & 1) * 2048 + WN * 4096));
    const unsigned fo = pins(OFF_F + ((2 * TB + (WM >> 1)) * 8 + EG) * 128 + 4 * (2 * WN + (WM & 1)));
    const unsigned lw4 = pins(lds0 + wave * 4096);
    v4u X[16][2]; v2u Wl[2][4], Wh[2][4];
    f32x4 ac[5][2][2], c2[4][4];
    unsigned fv, va, vp;
    LAS char *cw0, *cw1, *cx0, *cx1;
    const char* sp;
    const unsigned un[9] = {pins<unsigned>(0 + (0 ^ (r4 & 1))), pins<unsigned>(0 + (1 ^ (r4 & 1))), pins<unsigned>(2 + ((0 + r4) & 3)), pins<unsigned>(2 + ((1 + r4) & 3)), pins<unsigned>(2 + ((2 + r4) & 3)), pins<unsigned>(2 + ((3 + r4) & 3)), pins<unsigned>(6), pins<unsigned>(7 + (0 ^ (r4 & 1))), pins<unsigned>(7 + (1 ^ (r4 & 1)))};
    const char* b2A = pins(b1A + 256); const char* b2B = pins(b1B + 256);
    cw0 = pinp(smem + (16384 + (lane >> 4) * 1024 + FR(lane) + ((2 * WN) ^ SZ(lane)) * 32)); cw1 = pinp(xorp(cw0, 32));
    cx0 = pinp(smem + (0 + (WM * 32 + (lane & 15)) * 128 + 16 * ((lane >> 4) ^ ((lane & 15) >> 1)))); cx1 = pinp(xorp(cx0, 64));
    SCHED(); BAR();
    va = pinu(vo1 + kF[0]); setm0<32768>(lw1); NOP();
    dma(va, b2A);
    va = pinu(xo0 + kX[1]); setm0<40960>(lw2); NOP();
    dma(va, xw);
    va = pinu(xo1 + kX[1]); setm0<41984>(lw2); NOP();
    dma(va, xw);
    va = pinu(vo1 + kF[1]); setm0<57344>(lw1); NOP();
    dma(va, b1A);
    setm0<65536>(lw1); NOP();
    dma(va, b1B);
    setm0<73728>(lw1); NOP();
    dma(va, b2A);
    WAITC(6, 15);
    asm volatile("" :: "v"(pce));
    BAR(); SCHED();
    STH(1, 34, 96, 98);
    Wl[0][0] = tr_read(cw0 + 0);
    X[0][0] = *(LAS v4u*)(cx0 + 0);
    Wh[0][0] = tr_read(cw0 + 512);
    X[0][1] = *(LAS v4u*)(cx0 + 2048);
    Wl[0][1] = tr_read(cw1 + 0);
    Wh[0][1] = tr_read(cw1 + 512);
    Wl[0][2] = tr_read(cw0 + 4096);
    X[1][0] = *(LAS v4u*)(cx1 + 0);
    Wh[0][2] = tr_read(cw0 + 4608);
    X[1][1] = *(LAS v4u*)(cx1 + 2048);
    Wl[0][3] = tr_read(cw1 + 4096);
    Wh[0][3] = tr_read(cw1 + 4608);
    SCHED();
    ac[0][0][0] = mma32(cat(Wl[0][0], Wh[0][0]), X[0][0], zero4); SCHED();
    Wl[1][0] = tr_read(cw0 + 8192);
    SCHED();
    SCHED();
    ac[0][1][0] = mma32(cat(Wl[0][0], Wh[0][0]), X[0][1], zero4); SCHED();
    unsigned nn; ld4c(nn, 0, ws);
    Wh[1][0] = tr_read(cw0 + 8704);
    SCHED();
    PRIO(2);
    SCHED();
    ac[0][0][1] = mma32(cat(Wl[0][1], Wh[0][1]), X[0][0], zero4); SCHED();
    Wl[1][1] = tr_read(cw1 + 8192);
    SCHED();
    SCHED();
    ac[0][1][1] = mma32(cat(Wl[0][1], Wh[0][1]), X[0][1], zero4); SCHED();
    v4u gr; ld16(gr, l16, mkptr(ka[12], ka[13]));
    Wh[1][1] = tr_read(cw1 + 8704);
    SCHED();
    PRIO(1);
    SCHED();
    ac[0][0][0] = mma32(cat(Wl[0][2], Wh[0][2]), X[1][0], ac[0][0][0]); SCHED();
    Wl[1][2] = tr_read(cw0 + 12288);
    SCHED();
    SCHED();
    ac[0][1][0] = mma32(cat(Wl[0][2], Wh[0][2]), X[1][1], ac[0][1][0]); SCHED();
    v4u xr; ld16(xr, l16, mkptr(ka[0], ka[1]) + gtok * 1024);
    Wh[1][2] = tr_read(cw0 + 12800);
    SCHED();
    PRIO(0);
    SCHED();
    ac[0][0][1] = mma32(cat(Wl[0][3], Wh[0][3]), X[1][0], ac[0][0][1]); SCHED();
    Wl[1][3] = tr_read(cw1 + 12288);
    SCHED();
    SCHED();
    va = pinu(xo0 + kX[2]); setm0<81920>(lw2); SCHED();
    ac[0][1][1] = mma32(cat(Wl[0][3], Wh[0][3]), X[1][1], ac[0][1][1]); SCHED();
    dma(va, xw);
    Wh[1][3] = tr_read(cw1 + 12800);
    SCHED();
    PRIO(3);
    SCHED();
    WAITC(9, 15);
    BAR(); SCHED();
    ac[1][0][0] = mma32(cat(Wl[1][0], Wh[1][0]), X[0][0], zero4); SCHED();
    Wl[0][0] = tr_read(cw0 + 16384);
    SCHED();
    SCHED();
    va = pinu(xo1 + kX[2]); setm0<82944>(lw2); SCHED();
    ac[1][1][0] = mma32(cat(Wl[1][0], Wh[1][0]), X[0][1], zero4); SCHED();
    dma(va, xw);
    Wh[0][0] = tr_read(cw0 + 16896);
    SCHED();
    PRIO(2);
    SCHED();
    ac[1][0][1] = mma32(cat(Wl[1][1], Wh[1][1]), X[0][0], zero4); SCHED();
    Wl[0][1] = tr_read(cw1 + 16384);
    SCHED();
    SCHED();
    va = pinu(vo1 + kF[2]); setm0<98304>(lw1); SCHED();
    ac[1][1][1] = mma32(cat(Wl[1][1], Wh[1][1]), X[0][1], zero4); SCHED();
    dma(va, b1A);
    Wh[0][1] = tr_read(cw1 + 16896);
    SCHED();
    PRIO(1);
    SCHED();
    ac[1][0][0] = mma32(cat(Wl[1][2], Wh[1][2]), X[1][0], ac[1][0][0]); SCHED();
    Wl[0][2] = tr_read(cw0 + 20480);
    SCHED();
    SCHED();
    setm0<106496>(lw1); SCHED();
    ac[1][1][0] = mma32(cat(Wl[1][2], Wh[1][2]), X[1][1], ac[1][1][0]); SCHED();
    dma(va, b1B);
    Wh[0][2] = tr_read(cw0 + 20992);
    SCHED();
    PRIO(0);
    SCHED();
    ac[1][0][1] = mma32(cat(Wl[1][3], Wh[1][3]), X[1][0], ac[1][0][1]); SCHED();
    Wl[0][3] = tr_read(cw1 + 20480);
    SCHED();
    SCHED();
    setm0<114688>(lw1); SCHED();
    ac[1][1][1] = mma32(cat(Wl[1][3], Wh[1][3]), X[1][1], ac[1][1][1]); SCHED();
    dma(va, b2A);
    Wh[0][3] = tr_read(cw1 + 20992);
    SCHED();
    PRIO(3);
    SCHED();
    WAITC(8, 15);
    BAR(); SCHED();
    ac[3][0][0] = mma32(cat(Wl[0][0], Wh[0][0]), X[0][0], zero4); SCHED();
    Wl[1][0] = tr_read(cw0 + 40960);
    SCHED();
    X[2][0] = *(LAS v4u*)(cx0 + 40960);
    SCHED();
    SCHED();
    va = pinu(xo0 + kX[3]); setm0<122880>(lw2); SCHED();
    ac[3][1][0] = mma32(cat(Wl[0][0], Wh[0][0]), X[0][1], zero4); SCHED();
    dma(va, xw);
    Wh[1][0] = tr_read(cw0 + 41472);
    SCHED();
    X[2][1] = *(LAS v4u*)(cx0 + 43008);
    SCHED();
    SCHED();
    ac[3][0][1] = mma32(cat(Wl[0][1], Wh[0][1]), X[0][0], zero4); SCHED();
    Wl[1][1] = tr_read(cw1 + 40960);
    SCHED();
    SCHED();
    va = pinu(xo1 + kX[3]); setm0<123904>(lw2); SCHED();
    ac[3][1][1] = mma32(cat(Wl[0][1], Wh[0][1]), X[0][1], zero4); SCHED();
    dma(va, xw);
    Wh[1][1] = tr_read(cw1 + 41472);
    SCHED();
    SCHED();
    ac[3][0][0] = mma32(cat(Wl[0][2], Wh[0][2]), X[1][0], ac[3][0][0]); SCHED();
    Wl[1][2] = tr_read(cw0 + 45056);
    SCHED();
    X[3][0] = *(LAS v4u*)(cx1 + 40960);
    SCHED();
    SCHED();
    va = pinu(vo1 + kF[3]); setm0<139264>(lw1); SCHED();
    ac[3][1][0] = mma32(cat(Wl[0][2], Wh[0][2]), X[1][1], ac[3][1][0]); SCHED();
    dma(va, b1A);
    Wh[1][2] = tr_read(cw0 + 45568);
    SCHED();
    X[3][1] = *(LAS v4u*)(cx1 + 43008);
    SCHED();
    PRIO(2);
    SCHED();
    ac[3][0][1] = mma32(cat(Wl[0][3], Wh[0][3]), X[1][0], ac[3][0][1]); SCHED();
    Wl[1][3] = tr_read(cw1 + 45056);
    SCHED();
    SCHED();
    setm0<147456>(lw1); SCHED();
    ac[3][1][1] = mma32(cat(Wl[0][3], Wh[0][3]), X[1][1], ac[3][1][1]); SCHED();
    dma(va, b1B);
    Wh[1][3] = tr_read(cw1 + 45568);
    SCHED();
    SCHED();
    ac[0][0][0] = mma32(cat(Wl[1][0], Wh[1][0]), X[2][0], ac[0][0][0]); SCHED();
    Wl[0][0] = tr_read(cw0 + 49152);
    SCHED();
    SCHED();
    ac[0][1][0] = mma32(cat(Wl[1][0], Wh[1][0]), X[2][1], ac[0][1][0]); SCHED();
    Wh[0][0] = tr_read(cw0 + 49664);
    SCHED();
    SCHED();
    setm0<155648>(lw1); SCHED();
    ac[0][0][1] = mma32(cat(Wl[1][1], Wh[1][1]), X[2][0], ac[0][0][1]); SCHED();
    dma(va, b2A);
    Wl[0][1] = tr_read(cw1 + 49152);
    SCHED();
    SCHED();
    ac[0][1][1] = mma32(cat(Wl[1][1], Wh[1][1]), X[2][1], ac[0][1][1]); SCHED();
    Wh[0][1] = tr_read(cw1 + 49664);
    SCHED();
    PRIO(1);
    SCHED();
    ac[0][0][0] = mma32(cat(Wl[1][2], Wh[1][2]), X[3][0], ac[0][0][0]); SCHED();
    Wl[0][2] = tr_read(cw0 + 53248);
    SCHED();
    SCHED();
    ac[0][1][0] = mma32(cat(Wl[1][2], Wh[1][2]), X[3][1], ac[0][1][0]); SCHED();
    Wh[0][2] = tr_read(cw0 + 53760);
    SCHED();
    SCHED();
    va = pinu(xo0 + kX[4]); setm0<0>(lw2); SCHED();
    ac[0][0][1] = mma32(cat(Wl[1][3], Wh[1][3]), X[3][0], ac[0][0][1]); SCHED();
    dma(va, xw);
    Wl[0][3] = tr_read(cw1 + 53248);
    SCHED();
    SCHED();
    ac[0][1][1] = mma32(cat(Wl[1][3], Wh[1][3]), X[3][1], ac[0][1][1]); SCHED();
    Wh[0][3] = tr_read(cw1 + 53760);
    SCHED();
    SCHED();
    STH(2, 1, 98, 96);
    ac[1][0][0] = mma32(cat(Wl[0][0], Wh[0][0]), X[2][0], ac[1][0][0]); SCHED();
    Wl[1][0] = tr_read(cw0 + 57344);
    SCHED();
    SCHED();
    ac[1][1][0] = mma32(cat(Wl[0][0], Wh[0][0]), X[2][1], ac[1][1][0]); SCHED();
    Wh[1][0] = tr_read(cw0 + 57856);
    SCHED();
    PRIO(0);
    SCHED();
    va = pinu(xo1 + kX[4]); setm0<1024>(lw2); SCHED();
    ac[1][0][1] = mma32(cat(Wl[0][1], Wh[0][1]), X[2][0], ac[1][0][1]); SCHED();
    dma(va, xw);
    Wl[1][1] = tr_read(cw1 + 57344);
    SCHED();
    SCHED();
    ac[1][1][1] = mma32(cat(Wl[0][1], Wh[0][1]), X[2][1], ac[1][1][1]); SCHED();
    Wh[1][1] = tr_read(cw1 + 57856);
    SCHED();
    cx0 = pinp(cx0 + (81920));
    SCHED();
    SCHED();
    ac[1][0][0] = mma32(cat(Wl[0][2], Wh[0][2]), X[3][0], ac[1][0][0]); SCHED();
    Wl[1][2] = tr_read(cw0 + 61440);
    SCHED();
    SCHED();
    ac[1][1][0] = mma32(cat(Wl[0][2], Wh[0][2]), X[3][1], ac[1][1][0]); SCHED();
    Wh[1][2] = tr_read(cw0 + 61952);
    SCHED();
    cx1 = pinp(cx1 + (81920));
    SCHED();
    SCHED();
    va = pinu(vo1 + kF[4]); setm0<16384>(lw1); SCHED();
    ac[1][0][1] = mma32(cat(Wl[0][3], Wh[0][3]), X[3][0], ac[1][0][1]); SCHED();
    dma(va, b1A);
    Wl[1][3] = tr_read(cw1 + 61440);
    SCHED();
    cw0 = pinp(cw0 + (81920));
    SCHED();
    SCHED();
    ac[1][1][1] = mma32(cat(Wl[0][3], Wh[0][3]), X[3][1], ac[1][1][1]); SCHED();
    Wh[1][3] = tr_read(cw1 + 61952);
    SCHED();
    PRIO(3);
    SCHED();
    WAITC(8, 15);
    BAR(); SCHED();
    ac[3][0][0] = mma32(cat(Wl[1][0], Wh[1][0]), X[2][0], ac[3][0][0]); SCHED();
    Wl[0][0] = tr_read(cw0 + 0);
    SCHED();
    X[4][0] = *(LAS v4u*)(cx0 + 0);
    SCHED();
    cw1 = pinp(cw1 + (81920));
    SCHED();
    asm volatile("" : "+v"(nn), "+v"(gr), "+v"(xr) :: "memory");
    SCHED();
    const unsigned tag = __builtin_amdgcn_readfirstlane(nn) + 0x9E3779B9u;
    SCHED();
    SCHED();
    ac[3][1][0] = mma32(cat(Wl[1][0], Wh[1][0]), X[2][1], ac[3][1][0]); SCHED();
    Wh[0][0] = tr_read(cw0 + 512);
    SCHED();
    X[4][1] = *(LAS v4u*)(cx0 + 2048);
    SCHED();
    float gd = 0.f;
    SCHED();
    float ga0 = pin(bflo(xr[0]));
    SCHED();
    SCHED();
    setm0<24576>(lw1); SCHED();
    ac[3][0][1] = mma32(cat(Wl[1][1], Wh[1][1]), X[2][0], ac[3][0][1]); SCHED();
    dma(va, b1B);
    Wl[0][1] = tr_read(cw1 + 0);
    SCHED();
    float gb0 = pin(bflo(gr[0]));
    SCHED();
    gd = pin(__builtin_fmaf(ga0, gb0, gd));
    SCHED();
    SCHED();
    ac[3][1][1] = mma32(cat(Wl[1][1], Wh[1][1]), X[2][1], ac[3][1][1]); SCHED();
    Wh[0][1] = tr_read(cw1 + 512);
    SCHED();
    float ga1 = pin(bfhi(xr[0]));
    SCHED();
    float gb1 = pin(bfhi(gr[0]));
    SCHED();
    SCHED();
    ac[3][0][0] = mma32(cat(Wl[1][2], Wh[1][2]), X[3][0], ac[3][0][0]); SCHED();
    Wl[0][2] = tr_read(cw0 + 4096);
    SCHED();
    X[5][0] = *(LAS v4u*)(cx1 + 0);
    SCHED();
    gd = pin(__builtin_fmaf(ga1, gb1, gd));
    SCHED();
    float ga2 = pin(bflo(xr[1]));
    SCHED();
    SCHED();
    ac[3][1][0] = mma32(cat(Wl[1][2], Wh[1][2]), X[3][1], ac[3][1][0]); SCHED();
    Wh[0][2] = tr_read(cw0 + 4608);
    SCHED();
    X[5][1] = *(LAS v4u*)(cx1 + 2048);
    SCHED();
    float gb2 = pin(bflo(gr[1]));
    SCHED();
    gd = pin(__builtin_fmaf(ga2, gb2, gd));
    SCHED();
    PRIO(2);
    SCHED();
    va = pinu(xo0 + kX[5]); setm0<32768>(lw2); SCHED();
    ac[3][0][1] = mma32(cat(Wl[1][3], Wh[1][3]), X[3][0], ac[3][0][1]); SCHED();
    dma(va, xw);
    Wl[0][3] = tr_read(cw1 + 4096);
    SCHED();
    float ga3 = pin(bfhi(xr[1]));
    SCHED();
    float gb3 = pin(bfhi(gr[1]));
    SCHED();
    SCHED();
    ac[3][1][1] = mma32(cat(Wl[1][3], Wh[1][3]), X[3][1], ac[3][1][1]); SCHED();
    Wh[0][3] = tr_read(cw1 + 4608);
    SCHED();
    gd = pin(__builtin_fmaf(ga3, gb3, gd));
    SCHED();
    float ga4 = pin(bflo(xr[2]));
    SCHED();
    SCHED();
    ac[0][0][0] = mma32(cat(Wl[0][0], Wh[0][0]), X[4][0], ac[0][0][0]); SCHED();
    Wl[1][0] = tr_read(cw0 + 8192);
    SCHED();
    float gb4 = pin(bflo(gr[2]));
    SCHED();
    gd = pin(__builtin_fmaf(ga4, gb4, gd));
    SCHED();
    lane = lane_id(); FRESH(); vp = pinu(((bid * 8 + wave) * 7 + (lane * 7 >> 6)) * 128);
    SCHED();
    SCHED();
    ac[0][1][0] = mma32(cat(Wl[0][0], Wh[0][0]), X[4][1], ac[0][1][0]); SCHED();
    Wh[1][0] = tr_read(cw0 + 8704);
    SCHED();
    float ga5 = pin(bfhi(xr[2]));
    SCHED();
    float gb5 = pin(bfhi(gr[2]));
    SCHED();
    unsigned pma; ld4(pma, vp, mkptr(ka[6], ka[7]));
    SCHED();
    SCHED();
    va = pinu(xo1 + kX[5]); setm0<33792>(lw2); SCHED();
    ac[0][0][1] = mma32(cat(Wl[0][1], Wh[0][1]), X[4][0], ac[0][0][1]); SCHED();
    dma(va, xw);
    Wl[1][1] = tr_read(cw1 + 8192);
    SCHED();
    gd = pin(__builtin_fmaf(ga5, gb5, gd));
    SCHED();
    float ga6 = pin(bflo(xr[3]));
    SCHED();
    SCHED();
    ac[0][1][1] = mma32(cat(Wl[0][1], Wh[0][1]), X[4][1], ac[0][1][1]); SCHED();
    Wh[1][1] = tr_read(cw1 + 8704);
    SCHED();
    float gb6 = pin(bflo(gr[3]));
    SCHED();
    gd = pin(__builtin_fmaf(ga6, gb6, gd));
    SCHED();
    PRIO(1);
    SCHED();
    ac[0][0][0] = mma32(cat(Wl[0][2], Wh[0][2]), X[5][0], ac[0][0][0]); SCHED();
    Wl[1][2] = tr_read(cw0 + 12288);
    SCHED();
    float ga7 = pin(bfhi(xr[3]));
    SCHED();
    float gb7 = pin(bfhi(gr[3]));
    SCHED();
    SCHED();
    ac[0][1][0] = mma32(cat(Wl[0][2], Wh[0][2]), X[5][1], ac[0][1][0]); SCHED();
    Wh[1][2] = tr_read(cw0 + 12800);
    SCHED();
    gd = pin(__builtin_fmaf(ga7, gb7, gd));
    SCHED();
    gd = pin(dpp_add<0x111, 0xf>(gd));
    SCHED();
    unsigned pmb; ld4(pmb, vp, mkptr(ka[8], ka[9]));
    SCHED();
    SCHED();
    va = pinu(vo1 + kF[5]); setm0<49152>(lw1); SCHED();
    ac[0][0][1] = mma32(cat(Wl[0][3], Wh[0][3]), X[5][0], ac[0][0][1]); SCHED();
    dma(va, b1A);
    Wl[1][3] = tr_read(cw1 + 12288);
    SCHED();
    gd = pin(dpp_add<0x112, 0xf>(gd));
    SCHED();
    SCHED();
    ac[0][1][1] = mma32(cat(Wl[0][3], Wh[0][3]), X[5][1], ac[0][1][1]); SCHED();
    Wh[1][3] = tr_read(cw1 + 12800);
    SCHED();
    gd = pin(dpp_add<0x114, 0xf>(gd));
    SCHED();
    SCHED();
    ac[1][0][0] = mma32(cat(Wl[1][0], Wh[1][0]), X[4][0], ac[1][0][0]); SCHED();
    Wl[0][0] = tr_read(cw0 + 16384);
    SCHED();
    gd = pin(dpp_add<0x118, 0xf>(gd));
    SCHED();
    SCHED();
    ac[1][1][0] = mma32(cat(Wl[1][0], Wh[1][0]), X[4][1], ac[1][1][0]); SCHED();
    Wh[0][0] = tr_read(cw0 + 16896);
    SCHED();
    gd = pin(dpp_add<0x142, 0xa>(gd));
    SCHED();
    unsigned pmc; ld4(pmc, vp, mkptr(ka[10], ka[11]));
    SCHED();
    PRIO(0);
    SCHED();
    setm0<57344>(lw1); SCHED();
    ac[1][0][1] = mma32(cat(Wl[1][1], Wh[1][1]), X[4][0], ac[1][0][1]); SCHED();
    dma(va, b1B);
    Wl[0][1] = tr_read(cw1 + 16384);
    SCHED();
    gd = pin(dpp_add<0x143, 0xc>(gd));
    SCHED();
    SCHED();
    ac[1][1][1] = mma32(cat(Wl[1][1], Wh[1][1]), X[4][1], ac[1][1][1]); SCHED();
    Wh[0][1] = tr_read(cw1 + 16896);
    SCHED();
    __builtin_amdgcn_raw_buffer_store_b64((v2u){f2u(gd), tag}, wsr, (l16 ^ 1008) << 22, OFF_G + gtok * 8, SC1);
    SCHED();
    SCHED();
    ac[1][0][0] = mma32(cat(Wl[1][2], Wh[1][2]), X[5][0], ac[1][0][0]); SCHED();
    Wl[0][2] = tr_read(cw0 + 20480);
    SCHED();
    SCHED();
    ac[1][1][0] = mma32(cat(Wl[1][2], Wh[1][2]), X[5][1], ac[1][1][0]); SCHED();
    Wh[0][2] = tr_read(cw0 + 20992);
    SCHED();
    lane = lane_id(); FRESH(); vp = pinu(((bid * 8 + wave) * 2 + (lane >> 5)) * 128);
    SCHED();
    SCHED();
    ac[1][0][1] = mma32(cat(Wl[1][3], Wh[1][3]), X[5][0], ac[1][0][1]); SCHED();
    Wl[0][3] = tr_read(cw1 + 20480);
    SCHED();
    unsigned pmd; ld4(pmd, vp, mkptr(ka[4], ka[5]));
    SCHED();
    SCHED();
    ac[1][1][1] = mma32(cat(Wl[1][3], Wh[1][3]), X[5][1], ac[1][1][1]); SCHED();
    Wh[0][3] = tr_read(cw1 + 20992);
    SCHED();
    PRIO(3);
    SCHED();
    WAITC(13, 15);
    BAR(); SCHED();
    STH(3, 2, 96, 98);
    ac[3][0][0] = mma32(cat(Wl[0][0], Wh[0][0]), X[4][0], ac[3][0][0]); SCHED();
    Wl[1][0] = tr_read(cw0 + 40960);
    SCHED();
    X[6][0] = *(LAS v4u*)(cx0 + 40960);
    SCHED();
    SCHED();
    ac[3][1][0] = mma32(cat(Wl[0][0], Wh[0][0]), X[4][1], ac[3][1][0]); SCHED();
    Wh[1][0] = tr_read(cw0 + 41472);
    SCHED();
    X[6][1] = *(LAS v4u*)(cx0 + 43008);
    SCHED();
    SCHED();
    va = pinu(xo0 + kX[6]); setm0<65536>(lw2); SCHED();
    ac[3][0][1] = mma32(cat(Wl[0][1], Wh[0][1]), X[4][0], ac[3][0][1]); SCHED();
    dma(va, xw);
    Wl[1][1] = tr_read(cw1 + 40960);
    SCHED();
    lane = lane_id(); FRESH(); vp = pinu((bid * 2 + (wave >> 2)) * 3072 + ((wave & 3) * 2 + (lane >> 5)) * 128);
    SCHED();
    SCHED();
    ac[3][1][1] = mma32(cat(Wl[0][1], Wh[0][1]), X[4][1], ac[3][1][1]); SCHED();
    Wh[1][1] = tr_read(cw1 + 41472);
    SCHED();
    unsigned pme; ld4(pme, vp, mkptr(ka[2], ka[3]));
    SCHED();
    SCHED();
    ac[3][0][0] = mma32(cat(Wl[0][2], Wh[0][2]), X[5][0], ac[3][0][0]); SCHED();
    Wl[1][2] = tr_read(cw0 + 45056);
    SCHED();
    X[7][0] = *(LAS v4u*)(cx1 + 40960);
    SCHED();
    SCHED();
    ac[3][1][0] = mma32(cat(Wl[0][2], Wh[0][2]), X[5][1], ac[3][1][0]); SCHED();
    Wh[1][2] = tr_read(cw0 + 45568);
    SCHED();
    X[7][1] = *(LAS v4u*)(cx1 + 43008);
    SCHED();
    PRIO(2);
    SCHED();
    va = pinu(xo1 + kX[6]); setm0<66560>(lw2); SCHED();
    ac[3][0][1] = mma32(cat(Wl[0][3], Wh[0][3]), X[5][0], ac[3][0][1]); SCHED();
    dma(va, xw);
    Wl[1][3] = tr_read(cw1 + 45056);
    SCHED();
    SCHED();
    ac[3][1][1] = mma32(cat(Wl[0][3], Wh[0][3]), X[5][1], ac[3][1][1]); SCHED();
    Wh[1][3] = tr_read(cw1 + 45568);
    SCHED();
    SCHED();
    ac[0][0][0] = mma32(cat(Wl[1][0], Wh[1][0]), X[6][0], ac[0][0][0]); SCHED();
    Wl[0][0] = tr_read(cw0 + 49152);
    SCHED();
    SCHED();
    ac[0][1][0] = mma32(cat(Wl[1][0], Wh[1][0]), X[6][1], ac[0][1][0]); SCHED();
    Wh[0][0] = tr_read(cw0 + 49664);
    SCHED();
    SCHED();
    va = pinu(vo1 + kF[6]); setm0<81920>(lw1); SCHED();
    ac[0][0][1] = mma32(cat(Wl[1][1], Wh[1][1]), X[6][0], ac[0][0][1]); SCHED();
    dma(va, b1A);
    Wl[0][1] = tr_read(cw1 + 49152);
    SCHED();
    FRESH(); const unsigned wg = pins<unsigned>(wave >> 1); char* h2d = pins(hdst + 4 * 4096); char* h3d = pins(hdst + (6 * 4096 - WN * 3072));
    SCHED();
    SCHED();
    ac[0][1][1] = mma32(cat(Wl[1][1], Wh[1][1]), X[6][1], ac[0][1][1]); SCHED();
    Wh[0][1] = tr_read(cw1 + 49664);
    SCHED();
    PRIO(1);
    SCHED();
    ac[0][0][0] = mma32(cat(Wl[1][2], Wh[1][2]), X[7][0], ac[0][0][0]); SCHED();
    Wl[0][2] = tr_read(cw0 + 53248);
    SCHED();
    SCHED();
    ac[0][1][0] = mma32(cat(Wl[1][2], Wh[1][2]), X[7][1], ac[0][1][0]); SCHED();
    Wh[0][2] = tr_read(cw0 + 53760);
    SCHED();
    SCHED();
    setm0<90112>(lw1); SCHED();
    ac[0][0][1] = mma32(cat(Wl[1][3], Wh[1][3]), X[7][0], ac[0][0][1]); SCHED();
    dma(va, b1B);
    Wl[0][3] = tr_read(cw1 + 53248);
    SCHED();
    SCHED();
    ac[0][1][1] = mma32(cat(Wl[1][3], Wh[1][3]), X[7][1], ac[0][1][1]); SCHED();
    Wh[0][3] = tr_read(cw1 + 53760);
    SCHED();
    SCHED();
    ac[1][0][0] = mma32(cat(Wl[0][0], Wh[0][0]), X[6][0], ac[1][0][0]); SCHED();
    Wl[1][0] = tr_read(cw0 + 57344);
    SCHED();
    SCHED();
    ac[1][1][0] = mma32(cat(Wl[0][0], Wh[0][0]), X[6][1], ac[1][1][0]); SCHED();
    Wh[1][0] = tr_read(cw0 + 57856);
    SCHED();
    PRIO(0);
    SCHED();
    va = pinu(xo0 + kX[7]); setm0<98304>(lw2); SCHED();
    ac[1][0][1] = mma32(cat(Wl[0][1], Wh[0][1]), X[6][0], ac[1][0][1]); SCHED();
    dma(va, xw);
    Wl[1][1] = tr_read(cw1 + 57344);
    SCHED();
    SCHED();
    ac[1][1][1] = mma32(cat(Wl[0][1], Wh[0][1]), X[6][1], ac[1][1][1]); SCHED();
    Wh[1][1] = tr_read(cw1 + 57856);
    SCHED();
    cx0 = pinp(cx0 + (-81920));
    SCHED();
    SCHED();
    ac[1][0][0] = mma32(cat(Wl[0][2], Wh[0][2]), X[7][0], ac[1][0][0]); SCHED();
    Wl[1][2] = tr_read(cw0 + 61440);
    SCHED();
    SCHED();
    ac[1][1][0] = mma32(cat(Wl[0][2], Wh[0][2]), X[7][1], ac[1][1][0]); SCHED();
    Wh[1][2] = tr_read(cw0 + 61952);
    SCHED();
    cx1 = pinp(cx1 + (-81920));
    SCHED();
    SCHED();
    va = pinu(xo1 + kX[7]); setm0<99328>(lw2); SCHED();
    ac[1][0][1] = mma32(cat(Wl[0][3], Wh[0][3]), X[7][0], ac[1][0][1]); SCHED();
    dma(va, xw);
    Wl[1][3] = tr_read(cw1 + 61440);
    SCHED();
    cw0 = pinp(cw0 + (-81920));
    SCHED();
    SCHED();
    ac[1][1][1] = mma32(cat(Wl[0][3], Wh[0][3]), X[7][1], ac[1][1][1]); SCHED();
    Wh[1][3] = tr_read(cw1 + 61952);
    SCHED();
    PRIO(3);
    SCHED();
    WAITC(11, 15);
    asm volatile("" :: "v"(pma));
    asm volatile("" :: "v"(pmb));
    BAR(); SCHED();
    ac[3][0][0] = mma32(cat(Wl[1][0], Wh[1][0]), X[6][0], ac[3][0][0]); SCHED();
    Wl[0][0] = tr_read(cw0 + 0);
    SCHED();
    X[8][0] = *(LAS v4u*)(cx0 + 0);
    SCHED();
    cw1 = pinp(cw1 + (-81920));
    SCHED();
    SCHED();
    ac[3][1][0] = mma32(cat(Wl[1][0], Wh[1][0]), X[6][1], ac[3][1][0]); SCHED();
    Wh[0][0] = tr_read(cw0 + 512);
    SCHED();
    X[8][1] = *(LAS v4u*)(cx0 + 2048);
    SCHED();
    SCHED();
    va = pinu(vo1 + kF[7]); setm0<114688>(lw1); SCHED();
    ac[3][0][1] = mma32(cat(Wl[1][1], Wh[1][1]), X[6][0], ac[3][0][1]); SCHED();
    dma(va, b1A);
    Wl[0][1] = tr_read(cw1 + 0);
    SCHED();
    SCHED();
    ac[3][1][1] = mma32(cat(Wl[1][1], Wh[1][1]), X[6][1], ac[3][1][1]); SCHED();
    Wh[0][1] = tr_read(cw1 + 512);
    SCHED();
    SCHED();
    ac[3][0][0] = mma32(cat(Wl[1][2], Wh[1][2]), X[7][0], ac[3][0][0]); SCHED();
    Wl[0][2] = tr_read(cw0 + 4096);
    SCHED();
    X[9][0] = *(LAS v4u*)(cx1 + 0);
    SCHED();
    SCHED();
    ac[3][1][0] = mma32(cat(Wl[1][2], Wh[1][2]), X[7][1], ac[3][1][0]); SCHED();
    Wh[0][2] = tr_read(cw0 + 4608);
    SCHED();
    X[9][1] = *(LAS v4u*)(cx1 + 2048);
    SCHED();
    PRIO(2);
    SCHED();
    setm0<122880>(lw1); SCHED();
    ac[3][0][1] = mma32(cat(Wl[1][3], Wh[1][3]), X[7][0], ac[3][0][1]); SCHED();
    dma(va, b1B);
    Wl[0][3] = tr_read(cw1 + 4096);
    SCHED();
    SCHED();
    ac[3][1][1] = mma32(cat(Wl[1][3], Wh[1][3]), X[7][1], ac[3][1][1]); SCHED();
    Wh[0][3] = tr_read(cw1 + 4608);
    SCHED();
    SCHED();
    STH(4, 3, 98, 96);
    ac[0][0][0] = mma32(cat(Wl[0][0], Wh[0][0]), X[8][0], ac[0][0][0]); SCHED();
    Wl[1][0] = tr_read(cw0 + 8192);
    SCHED();
    SCHED();
    ac[0][1][0] = mma32(cat(Wl[0][0], Wh[0][0]), X[8][1], ac[0][1][0]); SCHED();
    Wh[1][0] = tr_read(cw0 + 8704);
    SCHED();
    SCHED();
    va = pinu(vo1 + kF[4]); setm0<131072>(lw1); SCHED();
    ac[0][0][1] = mma32(cat(Wl[0][1], Wh[0][1]), X[8][0], ac[0][0][1]); SCHED();
    dma(va, b2A);
    Wl[1][1] = tr_read(cw1 + 8192);
    SCHED();
    SCHED();
    ac[0][1][1] = mma32(cat(Wl[0][1], Wh[0][1]), X[8][1], ac[0][1][1]); SCHED();
    Wh[1][1] = tr_read(cw1 + 8704);
    SCHED();
    PRIO(1);
    SCHED();
    ac[0][0][0] = mma32(cat(Wl[0][2], Wh[0][2]), X[9][0], ac[0][0][0]); SCHED();
    Wl[1][2] = tr_read(cw0 + 12288);
    SCHED();
    SCHED();
    ac[0][1][0] = mma32(cat(Wl[0][2], Wh[0][2]), X[9][1], ac[0][1][0]); SCHED();
    Wh[1][2] = tr_read(cw0 + 12800);
    SCHED();
    SCHED();
    va = pinu(vo1 + kF[5]); setm0<139264>(lw1); SCHED();
    ac[0][0][1] = mma32(cat(Wl[0][3], Wh[0][3]), X[9][0], ac[0][0][1]); SCHED();
    dma(va, b2A);
    Wl[1][3] = tr_read(cw1 + 12288);
    SCHED();
    SCHED();
    ac[0][1][1] = mma32(cat(Wl[0][3], Wh[0][3]), X[9][1], ac[0][1][1]); SCHED();
    Wh[1][3] = tr_read(cw1 + 12800);
    SCHED();
    SCHED();
    ac[1][0][0] = mma32(cat(Wl[1][0], Wh[1][0]), X[8][0], ac[1][0][0]); SCHED();
    Wl[0][0] = tr_read(cw0 + 32768);
    SCHED();
    X[10][0] = *(LAS v4u*)(cx0 + 32768);
    SCHED();
    SCHED();
    ac[1][1][0] = mma32(cat(Wl[1][0], Wh[1][0]), X[8][1], ac[1][1][0]); SCHED();
    Wh[0][0] = tr_read(cw0 + 33280);
    SCHED();
    X[10][1] = *(LAS v4u*)(cx0 + 34816);
    SCHED();
    lane = lane_id(); FRESH(); const unsigned go = ((lane >> 5) * 16 + (lane & 15)) * 8; const char* gsrc = ws + (unsigned)(OFF_G + (TB * 128 + WM * 32) * 8);
    SCHED();
    PRIO(0);
    SCHED();
    va = pinu(vo1 + kF[6]); setm0<147456>(lw1); SCHED();
    ac[1][0][1] = mma32(cat(Wl[1][1], Wh[1][1]), X[8][0], ac[1][0][1]); SCHED();
    dma(va, b2A);
    Wl[0][1] = tr_read(cw1 + 32768);
    SCHED();
    v2u gg; ld8c(gg, go, gsrc);
    SCHED();
    SCHED();
    ac[1][1][1] = mma32(cat(Wl[1][1], Wh[1][1]), X[8][1], ac[1][1][1]); SCHED();
    Wh[0][1] = tr_read(cw1 + 33280);
    SCHED();
    SCHED();
    ac[1][0][0] = mma32(cat(Wl[1][2], Wh[1][2]), X[9][0], ac[1][0][0]); SCHED();
    Wl[0][2] = tr_read(cw0 + 36864);
    SCHED();
    X[11][0] = *(LAS v4u*)(cx1 + 32768);
    SCHED();
    SCHED();
    ac[1][1][0] = mma32(cat(Wl[1][2], Wh[1][2]), X[9][1], ac[1][1][0]); SCHED();
    Wh[0][2] = tr_read(cw0 + 37376);
    SCHED();
    X[11][1] = *(LAS v4u*)(cx1 + 34816);
    SCHED();
    SCHED();
    ac[1][0][1] = mma32(cat(Wl[1][3], Wh[1][3]), X[9][0], ac[1][0][1]); SCHED();
    Wl[0][3] = tr_read(cw1 + 36864);
    SCHED();
    SCHED();
    ac[1][1][1] = mma32(cat(Wl[1][3], Wh[1][3]), X[9][1], ac[1][1][1]); SCHED();
    Wh[0][3] = tr_read(cw1 + 37376);
    SCHED();
    PRIO(3);
    SCHED();
    WAITC(9, 15);
    asm volatile("" :: "v"(pmc));
    asm volatile("" :: "v"(pmd));
    asm volatile("" :: "v"(pme));
    BAR(); SCHED();
    ac[0][0][0] = mma32(cat(Wl[0][0], Wh[0][0]), X[10][0], ac[0][0][0]); SCHED();
    Wl[1][0] = tr_read(cw0 + 40960);
    SCHED();
    SCHED();
    ac[0][1][0] = mma32(cat(Wl[0][0], Wh[0][0]), X[10][1], ac[0][1][0]); SCHED();
    Wh[1][0] = tr_read(cw0 + 41472);
    SCHED();
    SCHED();
    va = pinu(vo1 + kF[7]); setm0<155648>(lw1); SCHED();
    ac[0][0][1] = mma32(cat(Wl[0][1], Wh[0][1]), X[10][0], ac[0][0][1]); SCHED();
    dma(va, b2A);
    Wl[1][1] = tr_read(cw1 + 40960);
    SCHED();
    SCHED();
    ac[0][1][1] = mma32(cat(Wl[0][1], Wh[0][1]), X[10][1], ac[0][1][1]); SCHED();
    Wh[1][1] = tr_read(cw1 + 41472);
    SCHED();
    cx0 = pinp(cx0 + (65536));
    SCHED();
    PRIO(2);
    SCHED();
    ac[0][0][0] = mma32(cat(Wl[0][2], Wh[0][2]), X[11][0], ac[0][0][0]); SCHED();
    Wl[1][2] = tr_read(cw0 + 45056);
    SCHED();
    SCHED();
    ac[0][1][0] = mma32(cat(Wl[0][2], Wh[0][2]), X[11][1], ac[0][1][0]); SCHED();
    Wh[1][2] = tr_read(cw0 + 45568);
    SCHED();
    cx1 = pinp(cx1 + (65536));
    SCHED();
    SCHED();
    va = pinu(vo1 + kF[0]); setm0<0>(lw1); SCHED();
    ac[0][0][1] = mma32(cat(Wl[0][3], Wh[0][3]), X[11][0], ac[0][0][1]); SCHED();
    dma(va, b2B);
    Wl[1][3] = tr_read(cw1 + 45056);
    SCHED();
    cw0 = pinp(cw0 + (65536));
    SCHED();
    SCHED();
    ac[0][1][1] = mma32(cat(Wl[0][3], Wh[0][3]), X[11][1], ac[0][1][1]); SCHED();
    Wh[1][3] = tr_read(cw1 + 45568);
    SCHED();
    PRIO(1);
    SCHED();
    ac[1][0][0] = mma32(cat(Wl[1][0], Wh[1][0]), X[10][0], ac[1][0][0]); SCHED();
    Wl[0][0] = tr_read(cw0 + 0);
    SCHED();
    X[12][0] = *(LAS v4u*)(cx0 + 0);
    SCHED();
    cw1 = pinp(cw1 + (65536));
    SCHED();
    SCHED();
    ac[1][1][0] = mma32(cat(Wl[1][0], Wh[1][0]), X[10][1], ac[1][1][0]); SCHED();
    Wh[0][0] = tr_read(cw0 + 512);
    SCHED();
    X[12][1] = *(LAS v4u*)(cx0 + 2048);
    SCHED();
    SCHED();
    va = pinu(vo1 + kF[1]); setm0<8192>(lw1); SCHED();
    ac[1][0][1] = mma32(cat(Wl[1][1], Wh[1][1]), X[10][0], ac[1][0][1]); SCHED();
    dma(va, b2B);
    Wl[0][1] = tr_read(cw1 + 0);
    SCHED();
    SCHED();
    ac[1][1][1] = mma32(cat(Wl[1][1], Wh[1][1]), X[10][1], ac[1][1][1]); SCHED();
    Wh[0][1] = tr_read(cw1 + 512);
    SCHED();
    PRIO(0);
    SCHED();
    ac[1][0][0] = mma32(cat(Wl[1][2], Wh[1][2]), X[11][0], ac[1][0][0]); SCHED();
    Wl[0][2] = tr_read(cw0 + 4096);
    SCHED();
    X[13][0] = *(LAS v4u*)(cx1 + 0);
    SCHED();
    SCHED();
    ac[1][1][0] = mma32(cat(Wl[1][2], Wh[1][2]), X[11][1], ac[1][1][0]); SCHED();
    Wh[0][2] = tr_read(cw0 + 4608);
    SCHED();
    X[13][1] = *(LAS v4u*)(cx1 + 2048);
    SCHED();
    SCHED();
    va = pinu(vo1 + kF[2]); setm0<16384>(lw1); SCHED();
    ac[1][0][1] = mma32(cat(Wl[1][3], Wh[1][3]), X[11][0], ac[1][0][1]); SCHED();
    dma(va, b2B);
    Wl[0][3] = tr_read(cw1 + 4096);
    SCHED();
    SCHED();
    ac[1][1][1] = mma32(cat(Wl[1][3], Wh[1][3]), X[11][1], ac[1][1][1]); SCHED();
    Wh[0][3] = tr_read(cw1 + 4608);
    SCHED();
    PRIO(3);
    SCHED();
    WAITC(9, 15);
    BAR(); SCHED();
    STH(5, 4, 96, 98);
    ac[0][0][0] = mma32(cat(Wl[0][0], Wh[0][0]), X[12][0], ac[0][0][0]); SCHED();
    Wl[1][0] = tr_read(cw0 + 8192);
    SCHED();
    lane = lane_id(); const unsigned w2o0 = ((((lane & 7) >> 1) ^ ((lane >> 4) & 1)) * 32) + (lane & 1) * 16 + (16 * (lane >> 5) + ((lane >> 3) & 3)) * 1024, w2o1 = w2o0 ^ 64;
    SCHED();
    SCHED();
    ac[0][1][0] = mma32(cat(Wl[0][0], Wh[0][0]), X[12][1], ac[0][1][0]); SCHED();
    Wh[1][0] = tr_read(cw0 + 8704);
    SCHED();
    SCHED();
    SCHED();
    va = pinu(vo1 + kF[3]); setm0<24576>(lw1); SCHED();
    ac[0][0][1] = mma32(cat(Wl[0][1], Wh[0][1]), X[12][0], ac[0][0][1]); SCHED();
    dma(va, b2B);
    Wl[1][1] = tr_read(cw1 + 8192);
    SCHED();
    SCHED();
    ac[0][1][1] = mma32(cat(Wl[0][1], Wh[0][1]), X[12][1], ac[0][1][1]); SCHED();
    Wh[1][1] = tr_read(cw1 + 8704);
    SCHED();
    PRIO(2);
    SCHED();
    ac[0][0][0] = mma32(cat(Wl[0][2], Wh[0][2]), X[13][0], ac[0][0][0]); SCHED();
    Wl[1][2] = tr_read(cw0 + 12288);
    SCHED();
    SCHED();
    ac[0][1][0] = mma32(cat(Wl[0][2], Wh[0][2]), X[13][1], ac[0][1][0]); SCHED();
    Wh[1][2] = tr_read(cw0 + 12800);
    SCHED();
    SCHED();
    va = pinu(vo1 + kF[4]); setm0<32768>(lw1); SCHED();
    ac[0][0][1] = mma32(cat(Wl[0][3], Wh[0][3]), X[13][0], ac[0][0][1]); SCHED();
    dma(va, b2B);
    Wl[1][3] = tr_read(cw1 + 12288);
    SCHED();
    SCHED();
    ac[0][1][1] = mma32(cat(Wl[0][3], Wh[0][3]), X[13][1], ac[0][1][1]); SCHED();
    Wh[1][3] = tr_read(cw1 + 12800);
    SCHED();
    PRIO(1);
    SCHED();
    ac[1][0][0] = mma32(cat(Wl[1][0], Wh[1][0]), X[12][0], ac[1][0][0]); SCHED();
    Wl[0][0] = tr_read(cw0 + 32768);
    SCHED();
    X[14][0] = *(LAS v4u*)(cx0 + 32768);
    SCHED();
    SCHED();
    ac[1][1][0] = mma32(cat(Wl[1][0], Wh[1][0]), X[12][1], ac[1][1][0]); SCHED();
    Wh[0][0] = tr_read(cw0 + 33280);
    SCHED();
    X[14][1] = *(LAS v4u*)(cx0 + 34816);
    SCHED();
    SCHED();
    va = pinu(vo1 + kF[5]); setm0<40960>(lw1); SCHED();
    ac[1][0][1] = mma32(cat(Wl[1][1], Wh[1][1]), X[12][0], ac[1][0][1]); SCHED();
    dma(va, b2B);
    Wl[0][1] = tr_read(cw1 + 32768);
    SCHED();
    SCHED();
    ac[1][1][1] = mma32(cat(Wl[1][1], Wh[1][1]), X[12][1], ac[1][1][1]); SCHED();
    Wh[0][1] = tr_read(cw1 + 33280);
    SCHED();
    PRIO(0);
    SCHED();
    ac[1][0][0] = mma32(cat(Wl[1][2], Wh[1][2]), X[13][0], ac[1][0][0]); SCHED();
    Wl[0][2] = tr_read(cw0 + 36864);
    SCHED();
    X[15][0] = *(LAS v4u*)(cx1 + 32768);
    SCHED();
    WAITC(7, 15);
    asm volatile("" : "+v"(gg) :: "memory");
    SCHED();
    g_retry(gg, go, gsrc, tag);
    SCHED();
    SCHED();
    SCHED();
    ac[1][1][0] = mma32(cat(Wl[1][2], Wh[1][2]), X[13][1], ac[1][1][0]); SCHED();
    Wh[0][2] = tr_read(cw0 + 37376);
    SCHED();
    X[15][1] = *(LAS v4u*)(cx1 + 34816);
    SCHED();
    float gx = pin(u2f(gg[0]) * L2E);
    SCHED();
    SCHED();
    va = pinu(vo1 + kF[6]); setm0<49152>(lw1); SCHED();
    ac[1][0][1] = mma32(cat(Wl[1][3], Wh[1][3]), X[13][0], ac[1][0][1]); SCHED();
    dma(va, b2B);
    Wl[0][3] = tr_read(cw1 + 36864);
    SCHED();
    float gy = pin(ex2(gx));
    SCHED();
    gy = pin(1.f + gy);
    SCHED();
    SCHED();
    ac[1][1][1] = mma32(cat(Wl[1][3], Wh[1][3]), X[13][1], ac[1][1][1]); SCHED();
    Wh[0][3] = tr_read(cw1 + 37376);
    SCHED();
    gy = pin(__builtin_fminf(gy, 1e30f));
    SCHED();
    PRIO(3);
    SCHED();
    BAR(); SCHED();
    ac[0][0][0] = mma32(cat(Wl[0][0], Wh[0][0]), X[14][0], ac[0][0][0]); SCHED();
    Wl[1][0] = tr_read(cw0 + 40960);
    SCHED();
    lane = lane_id(); const char *pg0 = W1, *pu0 = W3; FRESH(); const char* pH0 = ((VC(lane) & 32) ? pu0 : pg0) + (unsigned)(896 * (bid & 3) + 768 + 64 * (bid >> 7) + (wave * 8 + (lane >> 3)) * 3584 + ((VC(lane) >> 6) & 1) * 32 + (lane & 1) * 16);
    SCHED();
    SCHED();
    ac[0][1][0] = mma32(cat(Wl[0][0], Wh[0][0]), X[14][1], ac[0][1][0]); SCHED();
    Wh[1][0] = tr_read(cw0 + 41472);
    SCHED();
    SCHED();
    va = pinu(vo1 + kF[7]); setm0<57344>(lw1); SCHED();
    ac[0][0][1] = mma32(cat(Wl[0][1], Wh[0][1]), X[14][0], ac[0][0][1]); SCHED();
    dma(va, b2B);
    Wl[1][1] = tr_read(cw1 + 40960);
    SCHED();
    float io0, io1; { auto s = __builtin_amdgcn_permlane32_swap(f2u(gy), f2u(gy), false, false); io0 = u2f(s[0]); io1 = u2f(s[1]); }
    SCHED();
    SCHED();
    ac[0][1][1] = mma32(cat(Wl[0][1], Wh[0][1]), X[14][1], ac[0][1][1]); SCHED();
    Wh[1][1] = tr_read(cw1 + 41472);
    SCHED();
    PRIO(2);
    SCHED();
    ac[0][0][0] = mma32(cat(Wl[0][2], Wh[0][2]), X[15][0], ac[0][0][0]); SCHED();
    Wl[1][2] = tr_read(cw0 + 45056);
    SCHED();
    SCHED();
    ac[0][1][0] = mma32(cat(Wl[0][2], Wh[0][2]), X[15][1], ac[0][1][0]); SCHED();
    Wh[1][2] = tr_read(cw0 + 45568);
    SCHED();
    SCHED();
    const char* ws0 = pins(w2b + (un[0] << 15)); setm0<65536>(lw4); SCHED();
    ac[0][0][1] = mma32(cat(Wl[0][3], Wh[0][3]), X[15][0], ac[0][0][1]); SCHED();
    dmaw(w2o0, ws0);
    Wl[1][3] = tr_read(cw1 + 45056);
    SCHED();
    SCHED();
    ac[0][1][1] = mma32(cat(Wl[0][3], Wh[0][3]), X[15][1], ac[0][1][1]); SCHED();
    Wh[1][3] = tr_read(cw1 + 45568);
    SCHED();
    PRIO(1);
    SCHED();
    ac[1][0][0] = mma32(cat(Wl[1][0], Wh[1][0]), X[14][0], ac[1][0][0]); SCHED();
    Wl[0][0] = tr_read(cw0 + 49152);
    SCHED();
    SCHED();
    ac[1][1][0] = mma32(cat(Wl[1][0], Wh[1][0]), X[14][1], ac[1][1][0]); SCHED();
    Wh[0][0] = tr_read(cw0 + 49664);
    SCHED();
    SCHED();
    ws0 = pins(ws0 + 4096); setm0<66560>(lw4); SCHED();
    ac[1][0][1] = mma32(cat(Wl[1][1], Wh[1][1]), X[14][0], ac[1][0][1]); SCHED();
    dmaw(w2o1, ws0);
    Wl[0][1] = tr_read(cw1 + 49152);
    SCHED();
    SCHED();
    ac[1][1][1] = mma32(cat(Wl[1][1], Wh[1][1]), X[14][1], ac[1][1][1]); SCHED();
    Wh[0][1] = tr_read(cw1 + 49664);
    SCHED();
    PRIO(0);
    SCHED();
    ac[1][0][0] = mma32(cat(Wl[1][2], Wh[1][2]), X[15][0], ac[1][0][0]); SCHED();
    Wl[0][2] = tr_read(cw0 + 53248);
    SCHED();
    SCHED();
    ac[1][1][0] = mma32(cat(Wl[1][2], Wh[1][2]), X[15][1], ac[1][1][0]); SCHED();
    Wh[0][2] = tr_read(cw0 + 53760);
    SCHED();
    SCHED();
    ws0 = pins(ws0 + 4096); setm0<67584>(lw4); SCHED();
    ac[1][0][1] = mma32(cat(Wl[1][3], Wh[1][3]), X[15][0], ac[1][0][1]); SCHED();
    dmaw(w2o0, ws0);
    Wl[0][3] = tr_read(cw1 + 53248);
    SCHED();
    SCHED();
    ac[1][1][1] = mma32(cat(Wl[1][3], Wh[1][3]), X[15][1], ac[1][1][1]); SCHED();
    Wh[0][3] = tr_read(cw1 + 53760);
    SCHED();
    PRIO(3);
    SCHED();
    WAITC(10, 15);
    BAR(); SCHED();
    STH(6, 5, 98, 96);
    ac[3][0][0] = mma32(cat(Wl[0][0], Wh[0][0]), X[8][0], ac[3][0][0]); SCHED();
    Wl[1][0] = tr_read(cw0 + 57344);
    SCHED();
    v4u hv1[2];
    SCHED();
    SCHED();
    ac[3][1][0] = mma32(cat(Wl[0][0], Wh[0][0]), X[8][1], ac[3][1][0]); SCHED();
    Wh[1][0] = tr_read(cw0 + 57856);
    SCHED();
    float hv1_t0 = pin(ac[0][0][0][0] * -L2E);
    SCHED();
    SCHED();
    ws0 = pins(ws0 + 4096); setm0<68608>(lw4); SCHED();
    ac[3][0][1] = mma32(cat(Wl[0][1], Wh[0][1]), X[8][0], ac[3][0][1]); SCHED();
    dmaw(w2o1, ws0);
    Wl[1][1] = tr_read(cw1 + 57344);
    SCHED();
    SCHED();
    ac[3][1][1] = mma32(cat(Wl[0][1], Wh[0][1]), X[8][1], ac[3][1][1]); SCHED();
    Wh[1][1] = tr_read(cw1 + 57856);
    SCHED();
    SCHED();
    ac[3][0][0] = mma32(cat(Wl[0][2], Wh[0][2]), X[9][0], ac[3][0][0]); SCHED();
    Wl[1][2] = tr_read(cw0 + 61440);
    SCHED();
    hv1_t0 = pin(ex2(hv1_t0));
    SCHED();
    float hv1_t1 = pin(ac[0][0][0][1] * -L2E);
    SCHED();
    SCHED();
    ac[3][1][0] = mma32(cat(Wl[0][2], Wh[0][2]), X[9][1], ac[3][1][0]); SCHED();
    Wh[1][2] = tr_read(cw0 + 61952);
    SCHED();
    SCHED();
    sp = pH0 + kF[0]; asm volatile("" : "+v"(sp)); setm0<98304>(lw1); SCHED();
    ac[3][0][1] = mma32(cat(Wl[0][3], Wh[0][3]), X[9][0], ac[3][0][1]); SCHED();
    dma(sp);
    Wl[1][3] = tr_read(cw1 + 61440);
    SCHED();
    cw0 = pinp(cw0 + (65536));
    SCHED();
    hv1_t0 = pin(__builtin_fmaf(hv1_t0, io0, io0));
    SCHED();
    SCHED();
    ac[3][1][1] = mma32(cat(Wl[0][3], Wh[0][3]), X[9][1], ac[3][1][1]); SCHED();
    Wh[1][3] = tr_read(cw1 + 61952);
    SCHED();
    hv1_t1 = pin(ex2(hv1_t1));
    SCHED();
    float hv1_t2 = pin(ac[0][0][0][2] * -L2E);
    SCHED();
    PRIO(2);
    SCHED();
    ac[3][0][0] = mma32(cat(Wl[1][0], Wh[1][0]), X[10][0], ac[3][0][0]); SCHED();
    Wl[0][0] = tr_read(cw0 + 0);
    SCHED();
    hv1_t0 = pin(frcp(hv1_t0));
    SCHED();
    cw1 = pinp(cw1 + (65536));
    SCHED();
    float hv1_p0 = pin(ac[0][0][0][0] * ac[1][0][0][0]);
    SCHED();
    SCHED();
    ac[3][1][0] = mma32(cat(Wl[1][0], Wh[1][0]), X[10][1], ac[3][1][0]); SCHED();
    Wh[0][0] = tr_read(cw0 + 512);
    SCHED();
    hv1_p0 = pin(hv1_p0 * hv1_t0);
    SCHED();
    hv1_t1 = pin(__builtin_fmaf(hv1_t1, io0, io0));
    SCHED();
    SCHED();
    sp = pH0 + kF[1]; asm volatile("" : "+v"(sp)); setm0<106496>(lw1); SCHED();
    ac[3][0][1] = mma32(cat(Wl[1][1], Wh[1][1]), X[10][0], ac[3][0][1]); SCHED();
    dma(sp);
    Wl[0][1] = tr_read(cw1 + 0);
    SCHED();
    hv1_t2 = pin(ex2(hv1_t2));
    SCHED();
    float hv1_t3 = pin(ac[0][0][0][3] * -L2E);
    SCHED();
    SCHED();
    ac[3][1][1] = mma32(cat(Wl[1][1], Wh[1][1]), X[10][1], ac[3][1][1]); SCHED();
    Wh[0][1] = tr_read(cw1 + 512);
    SCHED();
    hv1_t1 = pin(frcp(hv1_t1));
    SCHED();
    float hv1_p1 = pin(ac[0][0][0][1] * ac[1][0][0][1]);
    SCHED();
    SCHED();
    ac[3][0][0] = mma32(cat(Wl[1][2], Wh[1][2]), X[11][0], ac[3][0][0]); SCHED();
    Wl[0][2] = tr_read(cw0 + 4096);
    SCHED();
    hv1_p1 = pin(hv1_p1 * hv1_t1);
    SCHED();
    hv1_t2 = pin(__builtin_fmaf(hv1_t2, io0, io0));
    SCHED();
    SCHED();
    ac[3][1][0] = mma32(cat(Wl[1][2], Wh[1][2]), X[11][1], ac[3][1][0]); SCHED();
    Wh[0][2] = tr_read(cw0 + 4608);
    SCHED();
    hv1_t3 = pin(ex2(hv1_t3));
    SCHED();
    float hv1_t4 = pin(ac[0][0][1][0] * -L2E);
    SCHED();
    SCHED();
    sp = pH0 + kF[2]; asm volatile("" : "+v"(sp)); setm0<114688>(lw1); SCHED();
    ac[3][0][1] = mma32(cat(Wl[1][3], Wh[1][3]), X[11][0], ac[3][0][1]); SCHED();
    dma(sp);
    Wl[0][3] = tr_read(cw1 + 4096);
    SCHED();
    hv1_t2 = pin(frcp(hv1_t2));
    SCHED();
    float hv1_p2 = pin(ac[0][0][0][2] * ac[1][0][0][2]);
    SCHED();
    SCHED();
    ac[3][1][1] = mma32(cat(Wl[1][3], Wh[1][3]), X[11][1], ac[3][1][1]); SCHED();
    Wh[0][3] = tr_read(cw1 + 4608);
    SCHED();
    hv1[0][0] = pinu(pk2(hv1_p0, hv1_p1));
    SCHED();
    hv1_p2 = pin(hv1_p2 * hv1_t2);
    SCHED();
    hv1_t3 = pin(__builtin_fmaf(hv1_t3, io0, io0));
    SCHED();
    PRIO(1);
    SCHED();
    ac[3][0][0] = mma32(cat(Wl[0][0], Wh[0][0]), X[12][0], ac[3][0][0]); SCHED();
    Wl[1][0] = tr_read(cw0 + 8192);
    SCHED();
    hv1_t4 = pin(ex2(hv1_t4));
    SCHED();
    lane = lane_id(); LAS char* pt0 = pinp(smem + (wave * 4096 + (lane >> 4) * 1024 + FR(lane) + SZ(lane) * 32)); LAS char *tp0, *tp1;
    SCHED();
    float hv1_t5 = pin(ac[0][0][1][1] * -L2E);
    SCHED();
    SCHED();
    ac[3][1][0] = mma32(cat(Wl[0][0], Wh[0][0]), X[12][1], ac[3][1][0]); SCHED();
    Wh[1][0] = tr_read(cw0 + 8704);
    SCHED();
    hv1_t3 = pin(frcp(hv1_t3));
    SCHED();
    float hv1_p3 = pin(ac[0][0][0][3] * ac[1][0][0][3]);
    SCHED();
    SCHED();
    sp = pH0 + kF[3]; asm volatile("" : "+v"(sp)); setm0<122880>(lw1); SCHED();
    ac[3][0][1] = mma32(cat(Wl[0][1], Wh[0][1]), X[12][0], ac[3][0][1]); SCHED();
    dma(sp);
    Wl[1][1] = tr_read(cw1 + 8192);
    SCHED();
    hv1_p3 = pin(hv1_p3 * hv1_t3);
    SCHED();
    hv1_t4 = pin(__builtin_fmaf(hv1_t4, io0, io0));
    SCHED();
    SCHED();
    ac[3][1][1] = mma32(cat(Wl[0][1], Wh[0][1]), X[12][1], ac[3][1][1]); SCHED();
    Wh[1][1] = tr_read(cw1 + 8704);
    SCHED();
    hv1_t5 = pin(ex2(hv1_t5));
    SCHED();
    float hv1_t6 = pin(ac[0][0][1][2] * -L2E);
    SCHED();
    SCHED();
    ac[3][0][0] = mma32(cat(Wl[0][2], Wh[0][2]), X[13][0], ac[3][0][0]); SCHED();
    Wl[1][2] = tr_read(cw0 + 12288);
    SCHED();
    hv1_t4 = pin(frcp(hv1_t4));
    SCHED();
    float hv1_p4 = pin(ac[0][0][1][0] * ac[1][0][1][0]);
    SCHED();
    SCHED();
    ac[3][1][0] = mma32(cat(Wl[0][2], Wh[0][2]), X[13][1], ac[3][1][0]); SCHED();
    Wh[1][2] = tr_read(cw0 + 12800);
    SCHED();
    hv1[0][1] = pinu(pk2(hv1_p2, hv1_p3));
    SCHED();
    hv1_p4 = pin(hv1_p4 * hv1_t4);
    SCHED();
    hv1_t5 = pin(__builtin_fmaf(hv1_t5, io0, io0));
    SCHED();
    SCHED();
    ac[3][0][1] = mma32(cat(Wl[0][3], Wh[0][3]), X[13][0], ac[3][0][1]); SCHED();
    Wl[1][3] = tr_read(cw1 + 12288);
    SCHED();
    hv1_t6 = pin(ex2(hv1_t6));
    SCHED();
    cw0 = pinp(cw0 + (-147456));
    SCHED();
    float hv1_t7 = pin(ac[0][0][1][3] * -L2E);
    SCHED();
    SCHED();
    ac[3][1][1] = mma32(cat(Wl[0][3], Wh[0][3]), X[13][1], ac[3][1][1]); SCHED();
    Wh[1][3] = tr_read(cw1 + 12800);
    SCHED();
    hv1_t5 = pin(frcp(hv1_t5));
    SCHED();
    float hv1_p5 = pin(ac[0][0][1][1] * ac[1][0][1][1]);
    SCHED();
    PRIO(0);
    SCHED();
    ac[3][0][0] = mma32(cat(Wl[1][0], Wh[1][0]), X[14][0], ac[3][0][0]); SCHED();
    Wl[0][0] = tr_read(cw0 + 0);
    SCHED();
    cw1 = pinp(cw1 + (-147456));
    SCHED();
    hv1_p5 = pin(hv1_p5 * hv1_t5);
    SCHED();
    hv1_t6 = pin(__builtin_fmaf(hv1_t6, io0, io0));
    SCHED();
    SCHED();
    ac[3][1][0] = mma32(cat(Wl[1][0], Wh[1][0]), X[14][1], ac[3][1][0]); SCHED();
    Wh[0][0] = tr_read(cw0 + 512);
    SCHED();
    hv1_t7 = pin(ex2(hv1_t7));
    SCHED();
    float hv1_t8 = pin(ac[0][1][0][0] * -L2E);
    SCHED();
    SCHED();
    ac[3][0][1] = mma32(cat(Wl[1][1], Wh[1][1]), X[14][0], ac[3][0][1]); SCHED();
    Wl[0][1] = tr_read(cw1 + 0);
    SCHED();
    hv1_t6 = pin(frcp(hv1_t6));
    SCHED();
    float hv1_p6 = pin(ac[0][0][1][2] * ac[1][0][1][2]);
    SCHED();
    SCHED();
    ac[3][1][1] = mma32(cat(Wl[1][1], Wh[1][1]), X[14][1], ac[3][1][1]); SCHED();
    Wh[0][1] = tr_read(cw1 + 512);
    SCHED();
    hv1[0][2] = pinu(pk2(hv1_p4, hv1_p5));
    SCHED();
    hv1_p6 = pin(hv1_p6 * hv1_t6);
    SCHED();
    hv1_t7 = pin(__builtin_fmaf(hv1_t7, io0, io0));
    SCHED();
    SCHED();
    ac[3][0][0] = mma32(cat(Wl[1][2], Wh[1][2]), X[15][0], ac[3][0][0]); SCHED();
    Wl[0][2] = tr_read(cw0 + 4096);
    SCHED();
    hv1_t8 = pin(ex2(hv1_t8));
    SCHED();
    WAITC(4, 15);
    SCHED();
    tp0 = pinp(xorp(pt0, 65536));
    SCHED();
    float hv1_t9 = pin(ac[0][1][0][1] * -L2E);
    SCHED();
    SCHED();
    ac[3][1][0] = mma32(cat(Wl[1][2], Wh[1][2]), X[15][1], ac[3][1][0]); SCHED();
    Wh[0][2] = tr_read(cw0 + 4608);
    SCHED();
    const v2u Wa0_0 = tr_read(tp0);
    SCHED();
    hv1_t7 = pin(frcp(hv1_t7));
    SCHED();
    float hv1_p7 = pin(ac[0][0][1][3] * ac[1][0][1][3]);
    SCHED();
    SCHED();
    ac[3][0][1] = mma32(cat(Wl[1][3], Wh[1][3]), X[15][0], ac[3][0][1]); SCHED();
    Wl[0][3] = tr_read(cw1 + 4096);
    SCHED();
    const v4u Wf0_0 = cat(Wa0_0, tr_read(tp0 + 512));
    SCHED();
    tp1 = pinp(xorp(pt0, 65568));
    SCHED();
    hv1_p7 = pin(hv1_p7 * hv1_t7);
    SCHED();
    hv1_t8 = pin(__builtin_fmaf(hv1_t8, io1, io1));
    SCHED();
    SCHED();
    ac[3][1][1] = mma32(cat(Wl[1][3], Wh[1][3]), X[15][1], ac[3][1][1]); SCHED();
    Wh[0][3] = tr_read(cw1 + 4608);
    SCHED();
    const v2u Wa0_1 = tr_read(tp1);
    SCHED();
    hv1_t9 = pin(ex2(hv1_t9));
    SCHED();
    float hv1_t10 = pin(ac[0][1][0][2] * -L2E);
    SCHED();
    PRIO(3);
    SCHED();
    BAR(); SCHED();
    STH(7, 6, 96, 98);
    ac[4][0][0] = mma32(cat(Wl[0][0], Wh[0][0]), X[0][0], zero4); SCHED();
    Wl[1][0] = tr_read(cw0 + 8192);
    SCHED();
    const v4u Wf0_1 = cat(Wa0_1, tr_read(tp1 + 512));
    SCHED();
    hv1_t8 = pin(frcp(hv1_t8));
    SCHED();
    tp0 = pinp(xorp(pt0, 65600));
    SCHED();
    float hv1_p8 = pin(ac[0][1][0][0] * ac[1][1][0][0]);
    SCHED();
    SCHED();
    ac[4][1][0] = mma32(cat(Wl[0][0], Wh[0][0]), X[0][1], zero4); SCHED();
    Wh[1][0] = tr_read(cw0 + 8704);
    SCHED();
    const v2u Wa0_2 = tr_read(tp0);
    SCHED();
    hv1[0][3] = pinu(pk2(hv1_p6, hv1_p7));
    SCHED();
    hv1_p8 = pin(hv1_p8 * hv1_t8);
    SCHED();
    hv1_t9 = pin(__builtin_fmaf(hv1_t9, io1, io1));
    SCHED();
    SCHED();
    const char* ws2 = pins(w2b + (un[2] << 15)); setm0<131072>(lw4); SCHED();
    ac[4][0][1] = mma32(cat(Wl[0][1], Wh[0][1]), X[0][0], zero4); SCHED();
    dmaw(w2o0, ws2);
    Wl[1][1] = tr_read(cw1 + 8192);
    SCHED();
    const v4u Wf0_2 = cat(Wa0_2, tr_read(tp0 + 512));
    SCHED();
    st16c<0>(hv1[0], l16, hdst);
    SCHED();
    hv1_t10 = pin(ex2(hv1_t10));
    SCHED();
    tp1 = pinp(xorp(pt0, 65632));
    SCHED();
    float hv1_t11 = pin(ac[0][1][0][3] * -L2E);
    SCHED();
    SCHED();
    ac[4][1][1] = mma32(cat(Wl[0][1], Wh[0][1]), X[0][1], zero4); SCHED();
    Wh[1][1] = tr_read(cw1 + 8704);
    SCHED();
    const v2u Wa0_3 = tr_read(tp1);
    SCHED();
    hv1_t9 = pin(frcp(hv1_t9));
    SCHED();
    float hv1_p9 = pin(ac[0][1][0][1] * ac[1][1][0][1]);
    SCHED();
    SCHED();
    ac[4][0][0] = mma32(cat(Wl[0][2], Wh[0][2]), X[1][0], ac[4][0][0]); SCHED();
    Wl[1][2] = tr_read(cw0 + 12288);
    SCHED();
    const v4u Wf0_3 = cat(Wa0_3, tr_read(tp1 + 512));
    SCHED();
    SCHED();
    SCHED();
    hv1_p9 = pin(hv1_p9 * hv1_t9);
    SCHED();
    hv1_t10 = pin(__builtin_fmaf(hv1_t10, io1, io1));
    SCHED();
    SCHED();
    ac[4][1][0] = mma32(cat(Wl[0][2], Wh[0][2]), X[1][1], ac[4][1][0]); SCHED();
    Wh[1][2] = tr_read(cw0 + 12800);
    SCHED();
    hv1_t11 = pin(ex2(hv1_t11));
    SCHED();
    float hv1_t12 = pin(ac[0][1][1][0] * -L2E);
    SCHED();
    SCHED();
    ws2 = pins(ws2 + 4096); setm0<132096>(lw4); SCHED();
    ac[4][0][1] = mma32(cat(Wl[0][3], Wh[0][3]), X[1][0], ac[4][0][1]); SCHED();
    dmaw(w2o1, ws2);
    Wl[1][3] = tr_read(cw1 + 12288);
    SCHED();
    hv1_t10 = pin(frcp(hv1_t10));
    SCHED();
    float hv1_p10 = pin(ac[0][1][0][2] * ac[1][1][0][2]);
    SCHED();
    SCHED();
    ac[4][1][1] = mma32(cat(Wl[0][3], Wh[0][3]), X[1][1], ac[4][1][1]); SCHED();
    Wh[1][3] = tr_read(cw1 + 12800);
    SCHED();
    hv1[1][0] = pinu(pk2(hv1_p8, hv1_p9));
    SCHED();
    hv1_p10 = pin(hv1_p10 * hv1_t10);
    SCHED();
    hv1_t11 = pin(__builtin_fmaf(hv1_t11, io1, io1));
    SCHED();
    PRIO(2);
    SCHED();
    ac[4][0][0] = mma32(cat(Wl[1][0], Wh[1][0]), X[2][0], ac[4][0][0]); SCHED();
    Wl[0][0] = tr_read(cw0 + 16384);
    SCHED();
    hv1_t12 = pin(ex2(hv1_t12));
    SCHED();
    float hv1_t13 = pin(ac[0][1][1][1] * -L2E);
    SCHED();
    SCHED();
    ac[4][1][0] = mma32(cat(Wl[1][0], Wh[1][0]), X[2][1], ac[4][1][0]); SCHED();
    Wh[0][0] = tr_read(cw0 + 16896);
    SCHED();
    hv1_t11 = pin(frcp(hv1_t11));
    SCHED();
    float hv1_p11 = pin(ac[0][1][0][3] * ac[1][1][0][3]);
    SCHED();
    SCHED();
    ws2 = pins(ws2 + 4096); setm0<133120>(lw4); SCHED();
    ac[4][0][1] = mma32(cat(Wl[1][1], Wh[1][1]), X[2][0], ac[4][0][1]); SCHED();
    dmaw(w2o0, ws2);
    Wl[0][1] = tr_read(cw1 + 16384);
    SCHED();
    hv1_p11 = pin(hv1_p11 * hv1_t11);
    SCHED();
    hv1_t12 = pin(__builtin_fmaf(hv1_t12, io1, io1));
    SCHED();
    SCHED();
    ac[4][1][1] = mma32(cat(Wl[1][1], Wh[1][1]), X[2][1], ac[4][1][1]); SCHED();
    Wh[0][1] = tr_read(cw1 + 16896);
    SCHED();
    hv1_t13 = pin(ex2(hv1_t13));
    SCHED();
    WAITC(63, 7);
    SCHED();
    float hv1_t14 = pin(ac[0][1][1][2] * -L2E);
    SCHED();
    SCHED();
    ac[4][0][0] = mma32(cat(Wl[1][2], Wh[1][2]), X[3][0], ac[4][0][0]); SCHED();
    Wl[0][2] = tr_read(cw0 + 20480);
    SCHED();
    hv1_t12 = pin(frcp(hv1_t12));
    SCHED();
    float hv1_p12 = pin(ac[0][1][1][0] * ac[1][1][1][0]);
    SCHED();
    SCHED();
    ac[4][1][0] = mma32(cat(Wl[1][2], Wh[1][2]), X[3][1], ac[4][1][0]); SCHED();
    Wh[0][2] = tr_read(cw0 + 20992);
    SCHED();
    hv1[1][1] = pinu(pk2(hv1_p10, hv1_p11));
    SCHED();
    hv1_p12 = pin(hv1_p12 * hv1_t12);
    SCHED();
    hv1_t13 = pin(__builtin_fmaf(hv1_t13, io1, io1));
    SCHED();
    SCHED();
    const char* ws1 = pins(w2b + (un[1] << 15)); setm0<65536>(lw4); SCHED();
    ac[4][0][1] = mma32(cat(Wl[1][3], Wh[1][3]), X[3][0], ac[4][0][1]); SCHED();
    dmaw(w2o0, ws1);
    Wl[0][3] = tr_read(cw1 + 20480);
    SCHED();
    hv1_t14 = pin(ex2(hv1_t14));
    SCHED();
    float hv1_t15 = pin(ac[0][1][1][3] * -L2E);
    SCHED();
    SCHED();
    ac[4][1][1] = mma32(cat(Wl[1][3], Wh[1][3]), X[3][1], ac[4][1][1]); SCHED();
    Wh[0][3] = tr_read(cw1 + 20992);
    SCHED();
    hv1_t13 = pin(frcp(hv1_t13));
    SCHED();
    float hv1_p13 = pin(ac[0][1][1][1] * ac[1][1][1][1]);
    SCHED();
    PRIO(1);
    SCHED();
    ac[4][0][0] = mma32(cat(Wl[0][0], Wh[0][0]), X[4][0], ac[4][0][0]); SCHED();
    Wl[1][0] = tr_read(cw0 + 24576);
    SCHED();
    hv1_p13 = pin(hv1_p13 * hv1_t13);
    SCHED();
    hv1_t14 = pin(__builtin_fmaf(hv1_t14, io1, io1));
    SCHED();
    SCHED();
    ac[4][1][0] = mma32(cat(Wl[0][0], Wh[0][0]), X[4][1], ac[4][1][0]); SCHED();
    Wh[1][0] = tr_read(cw0 + 25088);
    SCHED();
    hv1_t15 = pin(ex2(hv1_t15));
    SCHED();
    SCHED();
    ws1 = pins(ws1 + 4096); setm0<66560>(lw4); SCHED();
    ac[4][0][1] = mma32(cat(Wl[0][1], Wh[0][1]), X[4][0], ac[4][0][1]); SCHED();
    dmaw(w2o1, ws1);
    Wl[1][1] = tr_read(cw1 + 24576);
    SCHED();
    hv1_t14 = pin(frcp(hv1_t14));
    SCHED();
    float hv1_p14 = pin(ac[0][1][1][2] * ac[1][1][1][2]);
    SCHED();
    SCHED();
    ac[4][1][1] = mma32(cat(Wl[0][1], Wh[0][1]), X[4][1], ac[4][1][1]); SCHED();
    Wh[1][1] = tr_read(cw1 + 25088);
    SCHED();
    hv1[1][2] = pinu(pk2(hv1_p12, hv1_p13));
    SCHED();
    hv1_p14 = pin(hv1_p14 * hv1_t14);
    SCHED();
    hv1_t15 = pin(__builtin_fmaf(hv1_t15, io1, io1));
    SCHED();
    SCHED();
    ac[4][0][0] = mma32(cat(Wl[0][2], Wh[0][2]), X[5][0], ac[4][0][0]); SCHED();
    Wl[1][2] = tr_read(cw0 + 28672);
    SCHED();
    SCHED();
    ac[4][1][0] = mma32(cat(Wl[0][2], Wh[0][2]), X[5][1], ac[4][1][0]); SCHED();
    Wh[1][2] = tr_read(cw0 + 29184);
    SCHED();
    hv1_t15 = pin(frcp(hv1_t15));
    SCHED();
    float hv1_p15 = pin(ac[0][1][1][3] * ac[1][1][1][3]);
    SCHED();
    SCHED();
    ws1 = pins(ws1 + 4096); setm0<67584>(lw4); SCHED();
    ac[4][0][1] = mma32(cat(Wl[0][3], Wh[0][3]), X[5][0], ac[4][0][1]); SCHED();
    dmaw(w2o0, ws1);
    Wl[1][3] = tr_read(cw1 + 28672);
    SCHED();
    hv1_p15 = pin(hv1_p15 * hv1_t15);
    SCHED();
    SCHED();
    ac[4][1][1] = mma32(cat(Wl[0][3], Wh[0][3]), X[5][1], ac[4][1][1]); SCHED();
    Wh[1][3] = tr_read(cw1 + 29184);
    SCHED();
    PRIO(0);
    SCHED();
    ac[4][0][0] = mma32(cat(Wl[1][0], Wh[1][0]), X[6][0], ac[4][0][0]); SCHED();
    Wl[0][0] = tr_read(cw0 + 32768);
    SCHED();
    SCHED();
    ac[4][1][0] = mma32(cat(Wl[1][0], Wh[1][0]), X[6][1], ac[4][1][0]); SCHED();
    Wh[0][0] = tr_read(cw0 + 33280);
    SCHED();
    hv1[1][3] = pinu(pk2(hv1_p14, hv1_p15));
    SCHED();
    SCHED();
    ws1 = pins(ws1 + 4096); setm0<68608>(lw4); SCHED();
    ac[4][0][1] = mma32(cat(Wl[1][1], Wh[1][1]), X[6][0], ac[4][0][1]); SCHED();
    dmaw(w2o1, ws1);
    Wl[0][1] = tr_read(cw1 + 32768);
    SCHED();
    st16c<1024>(hv1[1], l16, hdst);
    SCHED();
    SCHED();
    ac[4][1][1] = mma32(cat(Wl[1][1], Wh[1][1]), X[6][1], ac[4][1][1]); SCHED();
    Wh[0][1] = tr_read(cw1 + 33280);
    SCHED();
    SCHED();
    ac[4][0][0] = mma32(cat(Wl[1][2], Wh[1][2]), X[7][0], ac[4][0][0]); SCHED();
    Wl[0][2] = tr_read(cw0 + 36864);
    SCHED();
    SCHED();
    ac[4][1][0] = mma32(cat(Wl[1][2], Wh[1][2]), X[7][1], ac[4][1][0]); SCHED();
    Wh[0][2] = tr_read(cw0 + 37376);
    SCHED();
    SCHED();
    ws2 = pins(ws2 + 4096); setm0<134144>(lw4); SCHED();
    ac[4][0][1] = mma32(cat(Wl[1][3], Wh[1][3]), X[7][0], ac[4][0][1]); SCHED();
    dmaw(w2o1, ws2);
    Wl[0][3] = tr_read(cw1 + 36864);
    SCHED();
    SCHED();
    ac[4][1][1] = mma32(cat(Wl[1][3], Wh[1][3]), X[7][1], ac[4][1][1]); SCHED();
    Wh[0][3] = tr_read(cw1 + 37376);
    SCHED();
    PRIO(3);
    SCHED();
    WAITC(13, 15);
    BAR(); SCHED();
    STH(8, 7, 98, 96);
    ac[4][0][0] = mma32(cat(Wl[0][0], Wh[0][0]), X[8][0], ac[4][0][0]); SCHED();
    Wl[1][0] = tr_read(cw0 + 40960);
    SCHED();
    SCHED();
    ac[4][1][0] = mma32(cat(Wl[0][0], Wh[0][0]), X[8][1], ac[4][1][0]); SCHED();
    Wh[1][0] = tr_read(cw0 + 41472);
    SCHED();
    SCHED();
    sp = pH0 + kF[4]; asm volatile("" : "+v"(sp)); setm0<0>(lw1); SCHED();
    ac[4][0][1] = mma32(cat(Wl[0][1], Wh[0][1]), X[8][0], ac[4][0][1]); SCHED();
    dma(sp);
    Wl[1][1] = tr_read(cw1 + 40960);
    SCHED();
    SCHED();
    ac[4][1][1] = mma32(cat(Wl[0][1], Wh[0][1]), X[8][1], ac[4][1][1]); SCHED();
    Wh[1][1] = tr_read(cw1 + 41472);
    SCHED();
    SCHED();
    ac[4][0][0] = mma32(cat(Wl[0][2], Wh[0][2]), X[9][0], ac[4][0][0]); SCHED();
    Wl[1][2] = tr_read(cw0 + 45056);
    SCHED();
    SCHED();
    ac[4][1][0] = mma32(cat(Wl[0][2], Wh[0][2]), X[9][1], ac[4][1][0]); SCHED();
    Wh[1][2] = tr_read(cw0 + 45568);
    SCHED();
    SCHED();
    sp = pH0 + kF[5]; asm volatile("" : "+v"(sp)); setm0<8192>(lw1); SCHED();
    ac[4][0][1] = mma32(cat(Wl[0][3], Wh[0][3]), X[9][0], ac[4][0][1]); SCHED();
    dma(sp);
    Wl[1][3] = tr_read(cw1 + 45056);
    SCHED();
    SCHED();
    ac[4][1][1] = mma32(cat(Wl[0][3], Wh[0][3]), X[9][1], ac[4][1][1]); SCHED();
    Wh[1][3] = tr_read(cw1 + 45568);
    SCHED();
    PRIO(2);
    SCHED();
    ac[4][0][0] = mma32(cat(Wl[1][0], Wh[1][0]), X[10][0], ac[4][0][0]); SCHED();
    Wl[0][0] = tr_read(cw0 + 49152);
    SCHED();
    SCHED();
    ac[4][1][0] = mma32(cat(Wl[1][0], Wh[1][0]), X[10][1], ac[4][1][0]); SCHED();
    Wh[0][0] = tr_read(cw0 + 49664);
    SCHED();
    SCHED();
    sp = pH0 + kF[6]; asm volatile("" : "+v"(sp)); setm0<16384>(lw1); SCHED();
    ac[4][0][1] = mma32(cat(Wl[1][1], Wh[1][1]), X[10][0], ac[4][0][1]); SCHED();
    dma(sp);
    Wl[0][1] = tr_read(cw1 + 49152);
    SCHED();
    SCHED();
    ac[4][1][1] = mma32(cat(Wl[1][1], Wh[1][1]), X[10][1], ac[4][1][1]); SCHED();
    Wh[0][1] = tr_read(cw1 + 49664);
    SCHED();
    SCHED();
    ac[4][0][0] = mma32(cat(Wl[1][2], Wh[1][2]), X[11][0], ac[4][0][0]); SCHED();
    Wl[0][2] = tr_read(cw0 + 53248);
    SCHED();
    SCHED();
    ac[4][1][0] = mma32(cat(Wl[1][2], Wh[1][2]), X[11][1], ac[4][1][0]); SCHED();
    Wh[0][2] = tr_read(cw0 + 53760);
    SCHED();
    SCHED();
    sp = pH0 + kF[7]; asm volatile("" : "+v"(sp)); setm0<24576>(lw1); SCHED();
    ac[4][0][1] = mma32(cat(Wl[1][3], Wh[1][3]), X[11][0], ac[4][0][1]); SCHED();
    dma(sp);
    Wl[0][3] = tr_read(cw1 + 53248);
    SCHED();
    SCHED();
    ac[4][1][1] = mma32(cat(Wl[1][3], Wh[1][3]), X[11][1], ac[4][1][1]); SCHED();
    Wh[0][3] = tr_read(cw1 + 53760);
    SCHED();
    PRIO(1);
    SCHED();
    ac[4][0][0] = mma32(cat(Wl[0][0], Wh[0][0]), X[12][0], ac[4][0][0]); SCHED();
    Wl[1][0] = tr_read(cw0 + 57344);
    SCHED();
    SCHED();
    ac[4][1][0] = mma32(cat(Wl[0][0], Wh[0][0]), X[12][1], ac[4][1][0]); SCHED();
    Wh[1][0] = tr_read(cw0 + 57856);
    SCHED();
    SCHED();
    ac[4][0][1] = mma32(cat(Wl[0][1], Wh[0][1]), X[12][0], ac[4][0][1]); SCHED();
    Wl[1][1] = tr_read(cw1 + 57344);
    SCHED();
    WAITC(5, 15);
    __builtin_amdgcn_raw_buffer_store_b32(tag, wsr, l16 << 22, fo + 0, SC1);
    SCHED();
    SCHED();
    ac[4][1][1] = mma32(cat(Wl[0][1], Wh[0][1]), X[12][1], ac[4][1][1]); SCHED();
    Wh[1][1] = tr_read(cw1 + 57856);
    SCHED();
    SCHED();
    ac[4][0][0] = mma32(cat(Wl[0][2], Wh[0][2]), X[13][0], ac[4][0][0]); SCHED();
    Wl[1][2] = tr_read(cw0 + 61440);
    SCHED();
    SCHED();
    ac[4][1][0] = mma32(cat(Wl[0][2], Wh[0][2]), X[13][1], ac[4][1][0]); SCHED();
    Wh[1][2] = tr_read(cw0 + 61952);
    SCHED();
    SCHED();
    ac[4][0][1] = mma32(cat(Wl[0][3], Wh[0][3]), X[13][0], ac[4][0][1]); SCHED();
    Wl[1][3] = tr_read(cw1 + 61440);
    SCHED();
    cw0 = pinp(cw0 + (98304));
    SCHED();
    SCHED();
    ac[4][1][1] = mma32(cat(Wl[0][3], Wh[0][3]), X[13][1], ac[4][1][1]); SCHED();
    Wh[1][3] = tr_read(cw1 + 61952);
    SCHED();
    PRIO(0);
    SCHED();
    ac[4][0][0] = mma32(cat(Wl[1][0], Wh[1][0]), X[14][0], ac[4][0][0]); SCHED();
    Wl[0][0] = tr_read(cw0 + 0);
    SCHED();
    cw1 = pinp(cw1 + (98304));
    SCHED();
    v4u fil0 = {~0u, ~0u, ~0u, ~0u}; asm volatile("" : "+v"(fil0));
    SCHED();
    SCHED();
    ac[4][1][0] = mma32(cat(Wl[1][0], Wh[1][0]), X[14][1], ac[4][1][0]); SCHED();
    Wh[0][0] = tr_read(cw0 + 512);
    SCHED();
    st16c<0>(fil0, l16, h3d);
    SCHED();
    SCHED();
    ac[4][0][1] = mma32(cat(Wl[1][1], Wh[1][1]), X[14][0], ac[4][0][1]); SCHED();
    Wl[0][1] = tr_read(cw1 + 0);
    SCHED();
    SCHED();
    ac[4][1][1] = mma32(cat(Wl[1][1], Wh[1][1]), X[14][1], ac[4][1][1]); SCHED();
    Wh[0][1] = tr_read(cw1 + 512);
    SCHED();
    SCHED();
    ac[4][0][0] = mma32(cat(Wl[1][2], Wh[1][2]), X[15][0], ac[4][0][0]); SCHED();
    Wl[0][2] = tr_read(cw0 + 4096);
    SCHED();
    SCHED();
    ac[4][1][0] = mma32(cat(Wl[1][2], Wh[1][2]), X[15][1], ac[4][1][0]); SCHED();
    Wh[0][2] = tr_read(cw0 + 4608);
    SCHED();
    SCHED();
    ac[4][0][1] = mma32(cat(Wl[1][3], Wh[1][3]), X[15][0], ac[4][0][1]); SCHED();
    Wl[0][3] = tr_read(cw1 + 4096);
    SCHED();
    SCHED();
    ac[4][1][1] = mma32(cat(Wl[1][3], Wh[1][3]), X[15][1], ac[4][1][1]); SCHED();
    Wh[0][3] = tr_read(cw1 + 4608);
    SCHED();
    PRIO(3);
    SCHED();
    WAITC(5, 15);
    BAR(); SCHED();
    STH(9, 8, 96, 98);
    ac[2][0][0] = mma32(cat(Wl[0][0], Wh[0][0]), X[0][0], zero4); SCHED();
    Wl[1][0] = tr_read(cw0 + 8192);
    SCHED();
    v4u hv2[2];
    SCHED();
    SCHED();
    const char* ws3 = pins(w2b + (un[3] << 15)); setm0<32768>(lw4); SCHED();
    ac[2][1][0] = mma32(cat(Wl[0][0], Wh[0][0]), X[0][1], zero4); SCHED();
    dmaw(w2o0, ws3);
    Wh[1][0] = tr_read(cw0 + 8704);
    SCHED();
    float hv2_t0 = pin(ac[3][0][0][0] * -L2E);
    SCHED();
    SCHED();
    ac[2][0][1] = mma32(cat(Wl[0][1], Wh[0][1]), X[0][0], zero4); SCHED();
    Wl[1][1] = tr_read(cw1 + 8192);
    SCHED();
    SCHED();
    ac[2][1][1] = mma32(cat(Wl[0][1], Wh[0][1]), X[0][1], zero4); SCHED();
    Wh[1][1] = tr_read(cw1 + 8704);
    SCHED();
    hv2_t0 = pin(ex2(hv2_t0));
    SCHED();
    float hv2_t1 = pin(ac[3][0][0][1] * -L2E);
    SCHED();
    SCHED();
    ws3 = pins(ws3 + 4096); setm0<33792>(lw4); SCHED();
    ac[2][0][0] = mma32(cat(Wl[0][2], Wh[0][2]), X[1][0], ac[2][0][0]); SCHED();
    dmaw(w2o1, ws3);
    Wl[1][2] = tr_read(cw0 + 12288);
    SCHED();
    float hv2_p0 = pin(ac[3][0][0][0] * ac[4][0][0][0]);
    SCHED();
    SCHED();
    ac[2][1][0] = mma32(cat(Wl[0][2], Wh[0][2]), X[1][1], ac[2][1][0]); SCHED();
    Wh[1][2] = tr_read(cw0 + 12800);
    SCHED();
    hv2_t1 = pin(ex2(hv2_t1));
    SCHED();
    hv2_t0 = pin(__builtin_fmaf(hv2_t0, io0, io0));
    SCHED();
    float hv2_t2 = pin(ac[3][0][0][2] * -L2E);
    SCHED();
    SCHED();
    ac[2][0][1] = mma32(cat(Wl[0][3], Wh[0][3]), X[1][0], ac[2][0][1]); SCHED();
    Wl[1][3] = tr_read(cw1 + 12288);
    SCHED();
    hv2_t0 = pin(frcp(hv2_t0));
    SCHED();
    float hv2_p1 = pin(ac[3][0][0][1] * ac[4][0][0][1]);
    SCHED();
    SCHED();
    ws3 = pins(ws3 + 4096); setm0<34816>(lw4); SCHED();
    ac[2][1][1] = mma32(cat(Wl[0][3], Wh[0][3]), X[1][1], ac[2][1][1]); SCHED();
    dmaw(w2o0, ws3);
    Wh[1][3] = tr_read(cw1 + 12800);
    SCHED();
    hv2_t2 = pin(ex2(hv2_t2));
    SCHED();
    hv2_p0 = pin(hv2_p0 * hv2_t0);
    SCHED();
    hv2_t1 = pin(__builtin_fmaf(hv2_t1, io0, io0));
    SCHED();
    float hv2_t3 = pin(ac[3][0][0][3] * -L2E);
    SCHED();
    PRIO(2);
    SCHED();
    ac[2][0][0] = mma32(cat(Wl[1][0], Wh[1][0]), X[2][0], ac[2][0][0]); SCHED();
    Wl[0][0] = tr_read(cw0 + 16384);
    SCHED();
    hv2_t1 = pin(frcp(hv2_t1));
    SCHED();
    float hv2_p2 = pin(ac[3][0][0][2] * ac[4][0][0][2]);
    SCHED();
    SCHED();
    ws3 = pins(ws3 + 4096); setm0<35840>(lw4); SCHED();
    ac[2][1][0] = mma32(cat(Wl[1][0], Wh[1][0]), X[2][1], ac[2][1][0]); SCHED();
    dmaw(w2o1, ws3);
    Wh[0][0] = tr_read(cw0 + 16896);
    SCHED();
    hv2_t3 = pin(ex2(hv2_t3));
    SCHED();
    hv2_p1 = pin(hv2_p1 * hv2_t1);
    SCHED();
    hv2_t2 = pin(__builtin_fmaf(hv2_t2, io0, io0));
    SCHED();
    float hv2_t4 = pin(ac[3][0][1][0] * -L2E);
    SCHED();
    SCHED();
    ac[2][0][1] = mma32(cat(Wl[1][1], Wh[1][1]), X[2][0], ac[2][0][1]); SCHED();
    Wl[0][1] = tr_read(cw1 + 16384);
    SCHED();
    hv2_t2 = pin(frcp(hv2_t2));
    SCHED();
    SCHED();
    tp0 = pinp(xorp(pt0, 65536));
    SCHED();
    hv2[0][0] = pinu(pk2(hv2_p0, hv2_p1));
    SCHED();
    float hv2_p3 = pin(ac[3][0][0][3] * ac[4][0][0][3]);
    SCHED();
    SCHED();
    ac[2][1][1] = mma32(cat(Wl[1][1], Wh[1][1]), X[2][1], ac[2][1][1]); SCHED();
    Wh[0][1] = tr_read(cw1 + 16896);
    SCHED();
    const v2u Wa1_0 = tr_read(tp0);
    SCHED();
    hv2_t4 = pin(ex2(hv2_t4));
    SCHED();
    hv2_p2 = pin(hv2_p2 * hv2_t2);
    SCHED();
    hv2_t3 = pin(__builtin_fmaf(hv2_t3, io0, io0));
    SCHED();
    float hv2_t5 = pin(ac[3][0][1][1] * -L2E);
    SCHED();
    SCHED();
    ac[2][0][0] = mma32(cat(Wl[1][2], Wh[1][2]), X[3][0], ac[2][0][0]); SCHED();
    Wl[0][2] = tr_read(cw0 + 20480);
    SCHED();
    const v4u Wf1_0 = cat(Wa1_0, tr_read(tp0 + 512));
    SCHED();
    hv2_t3 = pin(frcp(hv2_t3));
    SCHED();
    tp1 = pinp(xorp(pt0, 65568));
    SCHED();
    float hv2_p4 = pin(ac[3][0][1][0] * ac[4][0][1][0]);
    SCHED();
    SCHED();
    ac[2][1][0] = mma32(cat(Wl[1][2], Wh[1][2]), X[3][1], ac[2][1][0]); SCHED();
    Wh[0][2] = tr_read(cw0 + 20992);
    SCHED();
    const v2u Wa1_1 = tr_read(tp1);
    SCHED();
    hv2_t5 = pin(ex2(hv2_t5));
    SCHED();
    hv2_p3 = pin(hv2_p3 * hv2_t3);
    SCHED();
    hv2_t4 = pin(__builtin_fmaf(hv2_t4, io0, io0));
    SCHED();
    float hv2_t6 = pin(ac[3][0][1][2] * -L2E);
    SCHED();
    SCHED();
    ac[2][0][1] = mma32(cat(Wl[1][3], Wh[1][3]), X[3][0], ac[2][0][1]); SCHED();
    Wl[0][3] = tr_read(cw1 + 20480);
    SCHED();
    const v4u Wf1_1 = cat(Wa1_1, tr_read(tp1 + 512));
    SCHED();
    hv2_t4 = pin(frcp(hv2_t4));
    SCHED();
    tp0 = pinp(xorp(pt0, 65600));
    SCHED();
    hv2[0][1] = pinu(pk2(hv2_p2, hv2_p3));
    SCHED();
    float hv2_p5 = pin(ac[3][0][1][1] * ac[4][0][1][1]);
    SCHED();
    SCHED();
    ac[2][1][1] = mma32(cat(Wl[1][3], Wh[1][3]), X[3][1], ac[2][1][1]); SCHED();
    Wh[0][3] = tr_read(cw1 + 20992);
    SCHED();
    const v2u Wa1_2 = tr_read(tp0);
    SCHED();
    hv2_t6 = pin(ex2(hv2_t6));
    SCHED();
    hv2_p4 = pin(hv2_p4 * hv2_t4);
    SCHED();
    hv2_t5 = pin(__builtin_fmaf(hv2_t5, io0, io0));
    SCHED();
    float hv2_t7 = pin(ac[3][0][1][3] * -L2E);
    SCHED();
    PRIO(1);
    SCHED();
    ac[2][0][0] = mma32(cat(Wl[0][0], Wh[0][0]), X[4][0], ac[2][0][0]); SCHED();
    Wl[1][0] = tr_read(cw0 + 24576);
    SCHED();
    const v4u Wf1_2 = cat(Wa1_2, tr_read(tp0 + 512));
    SCHED();
    hv2_t5 = pin(frcp(hv2_t5));
    SCHED();
    tp1 = pinp(xorp(pt0, 65632));
    SCHED();
    float hv2_p6 = pin(ac[3][0][1][2] * ac[4][0][1][2]);
    SCHED();
    SCHED();
    ac[2][1][0] = mma32(cat(Wl[0][0], Wh[0][0]), X[4][1], ac[2][1][0]); SCHED();
    Wh[1][0] = tr_read(cw0 + 25088);
    SCHED();
    const v2u Wa1_3 = tr_read(tp1);
    SCHED();
    hv2_t7 = pin(ex2(hv2_t7));
    SCHED();
    hv2_p5 = pin(hv2_p5 * hv2_t5);
    SCHED();
    hv2_t6 = pin(__builtin_fmaf(hv2_t6, io0, io0));
    SCHED();
    float hv2_t8 = pin(ac[3][1][0][0] * -L2E);
    SCHED();
    SCHED();
    ac[2][0][1] = mma32(cat(Wl[0][1], Wh[0][1]), X[4][0], ac[2][0][1]); SCHED();
    Wl[1][1] = tr_read(cw1 + 24576);
    SCHED();
    const v4u Wf1_3 = cat(Wa1_3, tr_read(tp1 + 512));
    SCHED();
    hv2_t6 = pin(frcp(hv2_t6));
    SCHED();
    SCHED();
    SCHED();
    hv2[0][2] = pinu(pk2(hv2_p4, hv2_p5));
    SCHED();
    float hv2_p7 = pin(ac[3][0][1][3] * ac[4][0][1][3]);
    SCHED();
    SCHED();
    ac[2][1][1] = mma32(cat(Wl[0][1], Wh[0][1]), X[4][1], ac[2][1][1]); SCHED();
    Wh[1][1] = tr_read(cw1 + 25088);
    SCHED();
    hv2_t8 = pin(ex2(hv2_t8));
    SCHED();
    hv2_p6 = pin(hv2_p6 * hv2_t6);
    SCHED();
    hv2_t7 = pin(__builtin_fmaf(hv2_t7, io0, io0));
    SCHED();
    float hv2_t9 = pin(ac[3][1][0][1] * -L2E);
    SCHED();
    SCHED();
    ac[2][0][0] = mma32(cat(Wl[0][2], Wh[0][2]), X[5][0], ac[2][0][0]); SCHED();
    Wl[1][2] = tr_read(cw0 + 28672);
    SCHED();
    hv2_t7 = pin(frcp(hv2_t7));
    SCHED();
    SCHED();
    tp0 = pinp(xorp(pt0, 131072));
    SCHED();
    float hv2_p8 = pin(ac[3][1][0][0] * ac[4][1][0][0]);
    SCHED();
    SCHED();
    ac[2][1][0] = mma32(cat(Wl[0][2], Wh[0][2]), X[5][1], ac[2][1][0]); SCHED();
    Wh[1][2] = tr_read(cw0 + 29184);
    SCHED();
    const v2u Wa2_0 = tr_read(tp0);
    SCHED();
    hv2_t9 = pin(ex2(hv2_t9));
    SCHED();
    hv2_p7 = pin(hv2_p7 * hv2_t7);
    SCHED();
    hv2_t8 = pin(__builtin_fmaf(hv2_t8, io1, io1));
    SCHED();
    float hv2_t10 = pin(ac[3][1][0][2] * -L2E);
    SCHED();
    SCHED();
    ac[2][0][1] = mma32(cat(Wl[0][3], Wh[0][3]), X[5][0], ac[2][0][1]); SCHED();
    Wl[1][3] = tr_read(cw1 + 28672);
    SCHED();
    const v4u Wf2_0 = cat(Wa2_0, tr_read(tp0 + 512));
    SCHED();
    hv2_t8 = pin(frcp(hv2_t8));
    SCHED();
    cw0 = pinp(cw0 + (-98304));
    SCHED();
    tp1 = pinp(xorp(pt0, 131104));
    SCHED();
    hv2[0][3] = pinu(pk2(hv2_p6, hv2_p7));
    SCHED();
    float hv2_p9 = pin(ac[3][1][0][1] * ac[4][1][0][1]);
    SCHED();
    SCHED();
    ac[2][1][1] = mma32(cat(Wl[0][3], Wh[0][3]), X[5][1], ac[2][1][1]); SCHED();
    Wh[1][3] = tr_read(cw1 + 29184);
    SCHED();
    const v2u Wa2_1 = tr_read(tp1);
    SCHED();
    st16c<0>(hv2[0], l16, h2d);
    SCHED();
    hv2_t10 = pin(ex2(hv2_t10));
    SCHED();
    hv2_p8 = pin(hv2_p8 * hv2_t8);
    SCHED();
    hv2_t9 = pin(__builtin_fmaf(hv2_t9, io1, io1));
    SCHED();
    float hv2_t11 = pin(ac[3][1][0][3] * -L2E);
    SCHED();
    PRIO(0);
    SCHED();
    ac[2][0][0] = mma32(cat(Wl[1][0], Wh[1][0]), X[6][0], ac[2][0][0]); SCHED();
    Wl[0][0] = tr_read(cw0 + 0);
    SCHED();
    const v4u Wf2_1 = cat(Wa2_1, tr_read(tp1 + 512));
    SCHED();
    hv2_t9 = pin(frcp(hv2_t9));
    SCHED();
    cw1 = pinp(cw1 + (-98304));
    SCHED();
    tp0 = pinp(xorp(pt0, 131136));
    SCHED();
    float hv2_p10 = pin(ac[3][1][0][2] * ac[4][1][0][2]);
    SCHED();
    SCHED();
    ac[2][1][0] = mma32(cat(Wl[1][0], Wh[1][0]), X[6][1], ac[2][1][0]); SCHED();
    Wh[0][0] = tr_read(cw0 + 512);
    SCHED();
    const v2u Wa2_2 = tr_read(tp0);
    SCHED();
    hv2_t11 = pin(ex2(hv2_t11));
    SCHED();
    WAITC(63, 12);
    SCHED();
    hv2_p9 = pin(hv2_p9 * hv2_t9);
    SCHED();
    hv2_t10 = pin(__builtin_fmaf(hv2_t10, io1, io1));
    SCHED();
    float hv2_t12 = pin(ac[3][1][1][0] * -L2E);
    SCHED();
    SCHED();
    ac[2][0][1] = mma32(cat(Wl[1][1], Wh[1][1]), X[6][0], ac[2][0][1]); SCHED();
    Wl[0][1] = tr_read(cw1 + 0);
    SCHED();
    const v4u Wf2_2 = cat(Wa2_2, tr_read(tp0 + 512));
    SCHED();
    hv2_t10 = pin(frcp(hv2_t10));
    SCHED();
    tp1 = pinp(xorp(pt0, 131168));
    SCHED();
    hv2[1][0] = pinu(pk2(hv2_p8, hv2_p9));
    SCHED();
    float hv2_p11 = pin(ac[3][1][0][3] * ac[4][1][0][3]);
    SCHED();
    SCHED();
    ac[2][1][1] = mma32(cat(Wl[1][1], Wh[1][1]), X[6][1], ac[2][1][1]); SCHED();
    Wh[0][1] = tr_read(cw1 + 512);
    SCHED();
    const v2u Wa2_3 = tr_read(tp1);
    SCHED();
    hv2_t12 = pin(ex2(hv2_t12));
    SCHED();
    hv2_p10 = pin(hv2_p10 * hv2_t10);
    SCHED();
    hv2_t11 = pin(__builtin_fmaf(hv2_t11, io1, io1));
    SCHED();
    float hv2_t13 = pin(ac[3][1][1][1] * -L2E);
    SCHED();
    SCHED();
    const char* ws4 = pins(w2b + (un[4] << 15)); setm0<65536>(lw4); SCHED();
    ac[2][0][0] = mma32(cat(Wl[1][2], Wh[1][2]), X[7][0], ac[2][0][0]); SCHED();
    dmaw(w2o0, ws4);
    Wl[0][2] = tr_read(cw0 + 4096);
    SCHED();
    const v4u Wf2_3 = cat(Wa2_3, tr_read(tp1 + 512));
    SCHED();
    hv2_t11 = pin(frcp(hv2_t11));
    SCHED();
    SCHED();
    SCHED();
    float hv2_p12 = pin(ac[3][1][1][0] * ac[4][1][1][0]);
    SCHED();
    SCHED();
    ac[2][1][0] = mma32(cat(Wl[1][2], Wh[1][2]), X[7][1], ac[2][1][0]); SCHED();
    Wh[0][2] = tr_read(cw0 + 4608);
    SCHED();
    hv2_t13 = pin(ex2(hv2_t13));
    SCHED();
    hv2_p11 = pin(hv2_p11 * hv2_t11);
    SCHED();
    hv2_t12 = pin(__builtin_fmaf(hv2_t12, io1, io1));
    SCHED();
    float hv2_t14 = pin(ac[3][1][1][2] * -L2E);
    SCHED();
    SCHED();
    ac[2][0][1] = mma32(cat(Wl[1][3], Wh[1][3]), X[7][0], ac[2][0][1]); SCHED();
    Wl[0][3] = tr_read(cw1 + 4096);
    SCHED();
    hv2_t12 = pin(frcp(hv2_t12));
    SCHED();
    hv2[1][1] = pinu(pk2(hv2_p10, hv2_p11));
    SCHED();
    float hv2_p13 = pin(ac[3][1][1][1] * ac[4][1][1][1]);
    SCHED();
    SCHED();
    ws4 = pins(ws4 + 4096); setm0<66560>(lw4); SCHED();
    ac[2][1][1] = mma32(cat(Wl[1][3], Wh[1][3]), X[7][1], ac[2][1][1]); SCHED();
    dmaw(w2o1, ws4);
    Wh[0][3] = tr_read(cw1 + 4608);
    SCHED();
    hv2_t14 = pin(ex2(hv2_t14));
    SCHED();
    hv2_p12 = pin(hv2_p12 * hv2_t12);
    SCHED();
    hv2_t13 = pin(__builtin_fmaf(hv2_t13, io1, io1));
    SCHED();
    float hv2_t15 = pin(ac[3][1][1][3] * -L2E);
    SCHED();
    PRIO(3);
    SCHED();
    WAITC(9, 15);
    BAR(); SCHED();
    STH(10, 9, 98, 96);
    ac[2][0][0] = mma32(cat(Wl[0][0], Wh[0][0]), X[8][0], ac[2][0][0]); SCHED();
    Wl[1][0] = tr_read(cw0 + 8192);
    SCHED();
    hv2_t13 = pin(frcp(hv2_t13));
    SCHED();
    float hv2_p14 = pin(ac[3][1][1][2] * ac[4][1][1][2]);
    SCHED();
    SCHED();
    ws4 = pins(ws4 + 4096); setm0<67584>(lw4); SCHED();
    ac[2][1][0] = mma32(cat(Wl[0][0], Wh[0][0]), X[8][1], ac[2][1][0]); SCHED();
    dmaw(w2o0, ws4);
    Wh[1][0] = tr_read(cw0 + 8704);
    SCHED();
    hv2_t15 = pin(ex2(hv2_t15));
    SCHED();
    hv2_p13 = pin(hv2_p13 * hv2_t13);
    SCHED();
    hv2_t14 = pin(__builtin_fmaf(hv2_t14, io1, io1));
    SCHED();
    SCHED();
    ac[2][0][1] = mma32(cat(Wl[0][1], Wh[0][1]), X[8][0], ac[2][0][1]); SCHED();
    Wl[1][1] = tr_read(cw1 + 8192);
    SCHED();
    hv2_t14 = pin(frcp(hv2_t14));
    SCHED();
    hv2[1][2] = pinu(pk2(hv2_p12, hv2_p13));
    SCHED();
    float hv2_p15 = pin(ac[3][1][1][3] * ac[4][1][1][3]);
    SCHED();
    lane = lane_id(); const unsigned l4 = pinu((lane & 31) * 4);
    SCHED();
    SCHED();
    ac[2][1][1] = mma32(cat(Wl[0][1], Wh[0][1]), X[8][1], ac[2][1][1]); SCHED();
    Wh[1][1] = tr_read(cw1 + 8704);
    SCHED();
    WAITC(63, 7);
    SCHED();
    hv2_p14 = pin(hv2_p14 * hv2_t14);
    SCHED();
    hv2_t15 = pin(__builtin_fmaf(hv2_t15, io1, io1));
    SCHED();
    ld4c(fv, l4, fl);
    SCHED();
    SCHED();
    ws4 = pins(ws4 + 4096); setm0<68608>(lw4); SCHED();
    ac[2][0][0] = mma32(cat(Wl[0][2], Wh[0][2]), X[9][0], ac[2][0][0]); SCHED();
    dmaw(w2o1, ws4);
    Wl[1][2] = tr_read(cw0 + 12288);
    SCHED();
    hv2_t15 = pin(frcp(hv2_t15));
    SCHED();
    WAITC(6, 15);
    SCHED();
    tp0 = pinp(xorp(pt0, 32768));
    SCHED();
    SCHED();
    ac[2][1][0] = mma32(cat(Wl[0][2], Wh[0][2]), X[9][1], ac[2][1][0]); SCHED();
    Wh[1][2] = tr_read(cw0 + 12800);
    SCHED();
    const v2u Wa3_0 = tr_read(tp0);
    SCHED();
    hv2_p15 = pin(hv2_p15 * hv2_t15);
    SCHED();
    SCHED();
    ac[2][0][1] = mma32(cat(Wl[0][3], Wh[0][3]), X[9][0], ac[2][0][1]); SCHED();
    Wl[1][3] = tr_read(cw1 + 12288);
    SCHED();
    const v4u Wf3_0 = cat(Wa3_0, tr_read(tp0 + 512));
    SCHED();
    tp1 = pinp(xorp(pt0, 32800));
    SCHED();
    hv2[1][3] = pinu(pk2(hv2_p14, hv2_p15));
    SCHED();
    SCHED();
    const char* ws5 = pins(w2b + (un[5] << 15)); setm0<131072>(lw4); SCHED();
    ac[2][1][1] = mma32(cat(Wl[0][3], Wh[0][3]), X[9][1], ac[2][1][1]); SCHED();
    dmaw(w2o0, ws5);
    Wh[1][3] = tr_read(cw1 + 12800);
    SCHED();
    const v2u Wa3_1 = tr_read(tp1);
    SCHED();
    st16c<1024>(hv2[1], l16, h2d);
    SCHED();
    PRIO(3);
    SCHED();
    ac[2][0][0] = mma32(cat(Wl[1][0], Wh[1][0]), X[10][0], ac[2][0][0]); SCHED();
    Wl[0][0] = tr_read(cw0 + 16384);
    SCHED();
    const v4u Wf3_1 = cat(Wa3_1, tr_read(tp1 + 512));
    SCHED();
    tp0 = pinp(xorp(pt0, 32832));
    SCHED();
    SCHED();
    ws5 = pins(ws5 + 4096); setm0<132096>(lw4); SCHED();
    ac[2][1][0] = mma32(cat(Wl[1][0], Wh[1][0]), X[10][1], ac[2][1][0]); SCHED();
    dmaw(w2o1, ws5);
    Wh[0][0] = tr_read(cw0 + 16896);
    SCHED();
    const v2u Wa3_2 = tr_read(tp0);
    SCHED();
    SCHED();
    ac[2][0][1] = mma32(cat(Wl[1][1], Wh[1][1]), X[10][0], ac[2][0][1]); SCHED();
    Wl[0][1] = tr_read(cw1 + 16384);
    SCHED();
    const v4u Wf3_2 = cat(Wa3_2, tr_read(tp0 + 512));
    SCHED();
    tp1 = pinp(xorp(pt0, 32864));
    SCHED();
    SCHED();
    ac[2][1][1] = mma32(cat(Wl[1][1], Wh[1][1]), X[10][1], ac[2][1][1]); SCHED();
    Wh[0][1] = tr_read(cw1 + 16896);
    SCHED();
    const v2u Wa3_3 = tr_read(tp1);
    SCHED();
    SCHED();
    ws5 = pins(ws5 + 4096); setm0<133120>(lw4); SCHED();
    ac[2][0][0] = mma32(cat(Wl[1][2], Wh[1][2]), X[11][0], ac[2][0][0]); SCHED();
    dmaw(w2o0, ws5);
    Wl[0][2] = tr_read(cw0 + 20480);
    SCHED();
    const v4u Wf3_3 = cat(Wa3_3, tr_read(tp1 + 512));
    SCHED();
    SCHED();
    SCHED();
    SCHED();
    ac[2][1][0] = mma32(cat(Wl[1][2], Wh[1][2]), X[11][1], ac[2][1][0]); SCHED();
    Wh[0][2] = tr_read(cw0 + 20992);
    SCHED();
    SCHED();
    ac[2][0][1] = mma32(cat(Wl[1][3], Wh[1][3]), X[11][0], ac[2][0][1]); SCHED();
    Wl[0][3] = tr_read(cw1 + 20480);
    SCHED();
    SCHED();
    ws5 = pins(ws5 + 4096); setm0<134144>(lw4); SCHED();
    ac[2][1][1] = mma32(cat(Wl[1][3], Wh[1][3]), X[11][1], ac[2][1][1]); SCHED();
    dmaw(w2o1, ws5);
    Wh[0][3] = tr_read(cw1 + 20992);
    SCHED();
    WAITC(6, 15);
    asm volatile("" : "+v"(fv) :: "memory");
    poll(fv, l4, fl, tag, 0xfu);
    SCHED();
    PRIO(2);
    SCHED();
    ac[2][0][0] = mma32(cat(Wl[0][0], Wh[0][0]), X[12][0], ac[2][0][0]); SCHED();
    Wl[1][0] = tr_read(cw0 + 24576);
    SCHED();
    SCHED();
    const char* hs0 = pins(hb + (un[0] << 12)); SCHED();
    ac[2][1][0] = mma32(cat(Wl[0][0], Wh[0][0]), X[12][1], ac[2][1][0]); SCHED();
    v4u h0_0; ld16c<0>(h0_0, l16, hs0);
    Wh[1][0] = tr_read(cw0 + 25088);
    SCHED();
    SCHED();
    ac[2][0][1] = mma32(cat(Wl[0][1], Wh[0][1]), X[12][0], ac[2][0][1]); SCHED();
    Wl[1][1] = tr_read(cw1 + 24576);
    SCHED();
    SCHED();
    ac[2][1][1] = mma32(cat(Wl[0][1], Wh[0][1]), X[12][1], ac[2][1][1]); SCHED();
    Wh[1][1] = tr_read(cw1 + 25088);
    SCHED();
    WAITC(63, 7);
    SCHED();
    SCHED();
    ac[2][0][0] = mma32(cat(Wl[0][2], Wh[0][2]), X[13][0], ac[2][0][0]); SCHED();
    v4u h0_1; ld16c<1024>(h0_1, l16, hs0);
    Wl[1][2] = tr_read(cw0 + 28672);
    SCHED();
    SCHED();
    ac[2][1][0] = mma32(cat(Wl[0][2], Wh[0][2]), X[13][1], ac[2][1][0]); SCHED();
    Wh[1][2] = tr_read(cw0 + 29184);
    SCHED();
    SCHED();
    ac[2][0][1] = mma32(cat(Wl[0][3], Wh[0][3]), X[13][0], ac[2][0][1]); SCHED();
    Wl[1][3] = tr_read(cw1 + 28672);
    SCHED();
    SCHED();
    ac[2][1][1] = mma32(cat(Wl[0][3], Wh[0][3]), X[13][1], ac[2][1][1]); SCHED();
    v4u h0_2; ld16c<2048>(h0_2, l16, hs0);
    Wh[1][3] = tr_read(cw1 + 29184);
    SCHED();
    PRIO(2);
    SCHED();
    ac[2][0][0] = mma32(cat(Wl[1][0], Wh[1][0]), X[14][0], ac[2][0][0]); SCHED();
    SCHED();
    ac[2][1][0] = mma32(cat(Wl[1][0], Wh[1][0]), X[14][1], ac[2][1][0]); SCHED();
    v4u h0_3; ld16c<3072>(h0_3, l16, hs0);
    SCHED();
    ac[2][0][1] = mma32(cat(Wl[1][1], Wh[1][1]), X[14][0], ac[2][0][1]); SCHED();
    WAITC(7, 15);
    __builtin_amdgcn_raw_buffer_store_b32(tag, wsr, l16 << 22, fo + 16, SC1);
    SCHED();
    SCHED();
    ac[2][1][1] = mma32(cat(Wl[1][1], Wh[1][1]), X[14][1], ac[2][1][1]); SCHED();
    SCHED();
    const char* hs1 = pins(hb + (un[1] << 12)); SCHED();
    ac[2][0][0] = mma32(cat(Wl[1][2], Wh[1][2]), X[15][0], ac[2][0][0]); SCHED();
    v4u h1_0; ld16c<0>(h1_0, l16, hs1);
    SCHED();
    ac[2][1][0] = mma32(cat(Wl[1][2], Wh[1][2]), X[15][1], ac[2][1][0]); SCHED();
    SCHED();
    ac[2][0][1] = mma32(cat(Wl[1][3], Wh[1][3]), X[15][0], ac[2][0][1]); SCHED();
    SCHED();
    ac[2][1][1] = mma32(cat(Wl[1][3], Wh[1][3]), X[15][1], ac[2][1][1]); SCHED();
    v4u h1_1; ld16c<1024>(h1_1, l16, hs1);
    PRIO(1);
    SCHED();
    STH(16, 10, 96, 98);
    STH(15, 16, 98, 96);
    v4u hl;
    PRIO(1);
    v4u h1_2; ld16c<2048>(h1_2, l16, hs1);
    v4u h1_3; ld16c<3072>(h1_3, l16, hs1);
    hl[0] = swiglu2(lo2(ac[2][0][0]), lo2(ac[2][0][1]), io0);
    hl[1] = swiglu2(hi2(ac[2][0][0]), hi2(ac[2][0][1]), io0);
    SCHED();
    PRIO(1);
    const char* ws6 = pins(w2b + whl); setm0<98304>(lw4); NOP();
    dmaw(w2o0, ws6);
    ws6 = pins(ws6 + 4096); setm0<99328>(lw4); NOP();
    dmaw(w2o1, ws6);
    hl[2] = swiglu2(lo2(ac[2][1][0]), lo2(ac[2][1][1]), io1);
    hl[3] = swiglu2(hi2(ac[2][1][0]), hi2(ac[2][1][1]), io1);
    st16c<0>(hl, l16, h3d);
    SCHED();
    STH(18, 15, 96, 98);
    PRIO(0);
    STH(17, 18, 98, 96);
    ld4c(fv, l4, fl);
    SCHED();
    ws6 = pins(ws6 + 4096); setm0<100352>(lw4); NOP();
    dmaw(w2o0, ws6);
    ws6 = pins(ws6 + 4096); setm0<101376>(lw4); NOP();
    dmaw(w2o1, ws6);
    const char* ws7 = pins(wob + (un[7] << 15)); setm0<32768>(lw4); NOP();
    dmaw(w2o0, ws7);
    WAITC(12, 15);
    asm volatile("" : "+v"(h0_0), "+v"(h0_1), "+v"(h0_2), "+v"(h0_3) :: "memory");
    c2[0][0] = mma32(Wf0_0, h0_0, zero4); SCHED();
    SCHED();
    ws7 = pins(ws7 + 4096); setm0<33792>(lw4); SCHED();
    c2[0][1] = mma32(Wf0_1, h0_0, zero4); SCHED();
    dmaw(w2o1, ws7);
    SCHED();
    c2[0][2] = mma32(Wf0_2, h0_0, zero4); SCHED();
    SCHED();
    ws7 = pins(ws7 + 4096); setm0<34816>(lw4); SCHED();
    c2[0][3] = mma32(Wf0_3, h0_0, zero4); SCHED();
    dmaw(w2o0, ws7);
    SCHED();
    c2[1][0] = mma32(Wf0_0, h0_1, zero4); SCHED();
    SCHED();
    ws7 = pins(ws7 + 4096); setm0<35840>(lw4); SCHED();
    c2[1][1] = mma32(Wf0_1, h0_1, zero4); SCHED();
    dmaw(w2o1, ws7);
    SCHED();
    c2[1][2] = mma32(Wf0_2, h0_1, zero4); SCHED();
    SCHED();
    c2[1][3] = mma32(Wf0_3, h0_1, zero4); SCHED();
    SCHED();
    c2[2][0] = mma32(Wf0_0, h0_2, zero4); SCHED();
    SCHED();
    c2[2][1] = mma32(Wf0_1, h0_2, zero4); SCHED();
    SCHED();
    c2[2][2] = mma32(Wf0_2, h0_2, zero4); SCHED();
    SCHED();
    c2[2][3] = mma32(Wf0_3, h0_2, zero4); SCHED();
    SCHED();
    c2[3][0] = mma32(Wf0_0, h0_3, zero4); SCHED();
    SCHED();
    c2[3][1] = mma32(Wf0_1, h0_3, zero4); SCHED();
    SCHED();
    c2[3][2] = mma32(Wf0_2, h0_3, zero4); SCHED();
    SCHED();
    c2[3][3] = mma32(Wf0_3, h0_3, zero4); SCHED();
    SCHED();
    STH(19, 17, 96, 98);
    WAITC(6, 15);
    asm volatile("" : "+v"(fv) :: "memory");
    poll(fv, l4, fl, tag, 0xff0u);
    STH(30, 19, 98, 96);
    const char* hs2 = pins(hb + (un[2] << 12));
    v4u h2_0; ld16c<0>(h2_0, l16, hs2);
    v4u h2_1; ld16c<1024>(h2_1, l16, hs2);
    v4u h2_2; ld16c<2048>(h2_2, l16, hs2);
    v4u h2_3; ld16c<3072>(h2_3, l16, hs2);
    const char* hs3 = pins(hb + (un[3] << 12));
    v4u h3_0; ld16c<0>(h3_0, l16, hs3);
    v4u h3_1; ld16c<1024>(h3_1, l16, hs3);
    v4u h3_2; ld16c<2048>(h3_2, l16, hs3);
    v4u h3_3; ld16c<3072>(h3_3, l16, hs3);
    SCHED();
    asm volatile("" : "+v"(h1_0), "+v"(h1_1), "+v"(h1_2), "+v"(h1_3) :: "memory");
    c2[0][0] = mma32(Wf1_0, h1_0, c2[0][0]); SCHED();
    SCHED();
    tp0 = pinp(xorp(pt0, 65536));
    SCHED();
    SCHED();
    const char* hs4 = pins(hb + (un[4] << 12)); SCHED();
    c2[0][1] = mma32(Wf1_1, h1_0, c2[0][1]); SCHED();
    v4u h4_0; ld16c<0>(h4_0, l16, hs4);
    const v2u Wa4_0 = tr_read(tp0);
    SCHED();
    SCHED();
    c2[0][2] = mma32(Wf1_2, h1_0, c2[0][2]); SCHED();
    const v4u Wf4_0 = cat(Wa4_0, tr_read(tp0 + 512));
    SCHED();
    tp1 = pinp(xorp(pt0, 65568));
    SCHED();
    SCHED();
    c2[0][3] = mma32(Wf1_3, h1_0, c2[0][3]); SCHED();
    v4u h4_1; ld16c<1024>(h4_1, l16, hs4);
    const v2u Wa4_1 = tr_read(tp1);
    SCHED();
    SCHED();
    c2[1][0] = mma32(Wf1_0, h1_1, c2[1][0]); SCHED();
    const v4u Wf4_1 = cat(Wa4_1, tr_read(tp1 + 512));
    SCHED();
    tp0 = pinp(xorp(pt0, 65600));
    SCHED();
    SCHED();
    c2[1][1] = mma32(Wf1_1, h1_1, c2[1][1]); SCHED();
    v4u h4_2; ld16c<2048>(h4_2, l16, hs4);
    const v2u Wa4_2 = tr_read(tp0);
    SCHED();
    SCHED();
    c2[1][2] = mma32(Wf1_2, h1_1, c2[1][2]); SCHED();
    const v4u Wf4_2 = cat(Wa4_2, tr_read(tp0 + 512));
    SCHED();
    tp1 = pinp(xorp(pt0, 65632));
    SCHED();
    SCHED();
    c2[1][3] = mma32(Wf1_3, h1_1, c2[1][3]); SCHED();
    v4u h4_3; ld16c<3072>(h4_3, l16, hs4);
    const v2u Wa4_3 = tr_read(tp1);
    SCHED();
    SCHED();
    c2[2][0] = mma32(Wf1_0, h1_2, c2[2][0]); SCHED();
    const v4u Wf4_3 = cat(Wa4_3, tr_read(tp1 + 512));
    SCHED();
    SCHED();
    SCHED();
    SCHED();
    const char* hs5 = pins(hb + (un[5] << 12)); SCHED();
    c2[2][1] = mma32(Wf1_1, h1_2, c2[2][1]); SCHED();
    v4u h5_0; ld16c<0>(h5_0, l16, hs5);
    SCHED();
    c2[2][2] = mma32(Wf1_2, h1_2, c2[2][2]); SCHED();
    SCHED();
    c2[2][3] = mma32(Wf1_3, h1_2, c2[2][3]); SCHED();
    v4u h5_1; ld16c<1024>(h5_1, l16, hs5);
    SCHED();
    c2[3][0] = mma32(Wf1_0, h1_3, c2[3][0]); SCHED();
    SCHED();
    c2[3][1] = mma32(Wf1_1, h1_3, c2[3][1]); SCHED();
    v4u h5_2; ld16c<2048>(h5_2, l16, hs5);
    SCHED();
    c2[3][2] = mma32(Wf1_2, h1_3, c2[3][2]); SCHED();
    SCHED();
    c2[3][3] = mma32(Wf1_3, h1_3, c2[3][3]); SCHED();
    v4u h5_3; ld16c<3072>(h5_3, l16, hs5);
    SCHED();
    STH(20, 30, 96, 98);
    lane = lane_id(); FRESH(); LAS char* rw = pinp(smem + (wave * 4096 + (lane >> 4) * 256 + (((lane & 15) ^ (2 * (lane >> 5))) * 16)));
    const int ro_ = lane & 7, rc_ = (wave & 1) * 8 + (lane >> 3);
    LAS char* rr = pinp(smem + (32768 + (wave >> 1) * SLOT + (ro_ >> 1) * 1024 + (ro_ & 1) * 512 + ((rc_ ^ (2 * ro_)) & 15) * 16));
    const unsigned oo = pinu(((wave >> 1) * 16 + rc_) * 1024 + ro_ * 16); char* op_ = pins(mkptr(ka[14], ka[15]) + (unsigned)(((bid & 7) * 4 + ((bid >> 3) & 3)) * 65536 + (bid >> 5) * 128));
    SCHED();
    WAITC(12, 15);
    asm volatile("" : "+v"(h2_0), "+v"(h2_1), "+v"(h2_2), "+v"(h2_3) :: "memory");
    c2[0][0] = mma32(Wf2_0, h2_0, c2[0][0]); SCHED();
    SCHED();
    tp0 = pinp(xorp(pt0, 131072));
    SCHED();
    SCHED();
    const char* hs6 = pins(hb + (un[6] << 12)); SCHED();
    c2[0][1] = mma32(Wf2_1, h2_0, c2[0][1]); SCHED();
    v4u h6_0; ld16c<0>(h6_0, l16, hs6);
    const v2u Wa5_0 = tr_read(tp0);
    SCHED();
    SCHED();
    c2[0][2] = mma32(Wf2_2, h2_0, c2[0][2]); SCHED();
    const v4u Wf5_0 = cat(Wa5_0, tr_read(tp0 + 512));
    SCHED();
    tp1 = pinp(xorp(pt0, 131104));
    SCHED();
    SCHED();
    c2[0][3] = mma32(Wf2_3, h2_0, c2[0][3]); SCHED();
    v4u h6_1; ld16c<1024>(h6_1, l16, hs6);
    const v2u Wa5_1 = tr_read(tp1);
    SCHED();
    SCHED();
    c2[1][0] = mma32(Wf2_0, h2_1, c2[1][0]); SCHED();
    const v4u Wf5_1 = cat(Wa5_1, tr_read(tp1 + 512));
    SCHED();
    tp0 = pinp(xorp(pt0, 131136));
    SCHED();
    SCHED();
    c2[1][1] = mma32(Wf2_1, h2_1, c2[1][1]); SCHED();
    v4u h6_2; ld16c<2048>(h6_2, l16, hs6);
    const v2u Wa5_2 = tr_read(tp0);
    SCHED();
    SCHED();
    c2[1][2] = mma32(Wf2_2, h2_1, c2[1][2]); SCHED();
    const v4u Wf5_2 = cat(Wa5_2, tr_read(tp0 + 512));
    SCHED();
    tp1 = pinp(xorp(pt0, 131168));
    SCHED();
    SCHED();
    c2[1][3] = mma32(Wf2_3, h2_1, c2[1][3]); SCHED();
    v4u h6_3; ld16c<3072>(h6_3, l16, hs6);
    const v2u Wa5_3 = tr_read(tp1);
    SCHED();
    SCHED();
    c2[2][0] = mma32(Wf2_0, h2_2, c2[2][0]); SCHED();
    const v4u Wf5_3 = cat(Wa5_3, tr_read(tp1 + 512));
    SCHED();
    SCHED();
    SCHED();
    SCHED();
    c2[2][1] = mma32(Wf2_1, h2_2, c2[2][1]); SCHED();
    SCHED();
    c2[2][2] = mma32(Wf2_2, h2_2, c2[2][2]); SCHED();
    SCHED();
    c2[2][3] = mma32(Wf2_3, h2_2, c2[2][3]); SCHED();
    SCHED();
    c2[3][0] = mma32(Wf2_0, h2_3, c2[3][0]); SCHED();
    SCHED();
    c2[3][1] = mma32(Wf2_1, h2_3, c2[3][1]); SCHED();
    SCHED();
    c2[3][2] = mma32(Wf2_2, h2_3, c2[3][2]); SCHED();
    SCHED();
    c2[3][3] = mma32(Wf2_3, h2_3, c2[3][3]); SCHED();
    WAITC(63, 0);
    SCHED();
    SCHED();
    STH(21, 20, 98, 96);
    SCHED();
    WAITC(12, 15);
    asm volatile("" : "+v"(h3_0), "+v"(h3_1), "+v"(h3_2), "+v"(h3_3) :: "memory");
    c2[0][0] = mma32(Wf3_0, h3_0, c2[0][0]); SCHED();
    SCHED();
    tp0 = pinp(xorp(pt0, 98304));
    SCHED();
    SCHED();
    const char* ws8 = pins(wob + (un[8] << 15)); setm0<131072>(lw4); SCHED();
    c2[0][1] = mma32(Wf3_1, h3_0, c2[0][1]); SCHED();
    dmaw(w2o0, ws8);
    const v2u Wa6_0 = tr_read(tp0);
    SCHED();
    SCHED();
    c2[0][2] = mma32(Wf3_2, h3_0, c2[0][2]); SCHED();
    const v4u Wf6_0 = cat(Wa6_0, tr_read(tp0 + 512));
    SCHED();
    tp1 = pinp(xorp(pt0, 98336));
    SCHED();
    SCHED();
    ws8 = pins(ws8 + 4096); setm0<132096>(lw4); SCHED();
    c2[0][3] = mma32(Wf3_3, h3_0, c2[0][3]); SCHED();
    dmaw(w2o1, ws8);
    const v2u Wa6_1 = tr_read(tp1);
    SCHED();
    SCHED();
    c2[1][0] = mma32(Wf3_0, h3_1, c2[1][0]); SCHED();
    const v4u Wf6_1 = cat(Wa6_1, tr_read(tp1 + 512));
    SCHED();
    tp0 = pinp(xorp(pt0, 98368));
    SCHED();
    SCHED();
    ws8 = pins(ws8 + 4096); setm0<133120>(lw4); SCHED();
    c2[1][1] = mma32(Wf3_1, h3_1, c2[1][1]); SCHED();
    dmaw(w2o0, ws8);
    const v2u Wa6_2 = tr_read(tp0);
    SCHED();
    SCHED();
    c2[1][2] = mma32(Wf3_2, h3_1, c2[1][2]); SCHED();
    const v4u Wf6_2 = cat(Wa6_2, tr_read(tp0 + 512));
    SCHED();
    tp1 = pinp(xorp(pt0, 98400));
    SCHED();
    SCHED();
    ws8 = pins(ws8 + 4096); setm0<134144>(lw4); SCHED();
    c2[1][3] = mma32(Wf3_3, h3_1, c2[1][3]); SCHED();
    dmaw(w2o1, ws8);
    const v2u Wa6_3 = tr_read(tp1);
    SCHED();
    SCHED();
    c2[2][0] = mma32(Wf3_0, h3_2, c2[2][0]); SCHED();
    const v4u Wf6_3 = cat(Wa6_3, tr_read(tp1 + 512));
    SCHED();
    SCHED();
    SCHED();
    SCHED();
    const char* hs7 = pins(hb + (un[7] << 12)); SCHED();
    c2[2][1] = mma32(Wf3_1, h3_2, c2[2][1]); SCHED();
    v4u h7_0; ld16c<0>(h7_0, l16, hs7);
    SCHED();
    c2[2][2] = mma32(Wf3_2, h3_2, c2[2][2]); SCHED();
    SCHED();
    c2[2][3] = mma32(Wf3_3, h3_2, c2[2][3]); SCHED();
    v4u h7_1; ld16c<1024>(h7_1, l16, hs7);
    SCHED();
    c2[3][0] = mma32(Wf3_0, h3_3, c2[3][0]); SCHED();
    SCHED();
    c2[3][1] = mma32(Wf3_1, h3_3, c2[3][1]); SCHED();
    v4u h7_2; ld16c<2048>(h7_2, l16, hs7);
    SCHED();
    c2[3][2] = mma32(Wf3_2, h3_3, c2[3][2]); SCHED();
    SCHED();
    c2[3][3] = mma32(Wf3_3, h3_3, c2[3][3]); SCHED();
    v4u h7_3; ld16c<3072>(h7_3, l16, hs7);
    SCHED();
    STH(22, 21, 96, 98);
    SCHED();
    WAITC(16, 15);
    asm volatile("" : "+v"(h4_0), "+v"(h4_1), "+v"(h4_2), "+v"(h4_3) :: "memory");
    c2[0][0] = mma32(Wf4_0, h4_0, c2[0][0]); SCHED();
    SCHED();
    tp0 = pinp(xorp(pt0, 32768));
    SCHED();
    SCHED();
    const char* hs8 = pins(hb + (un[8] << 12)); SCHED();
    c2[0][1] = mma32(Wf4_1, h4_0, c2[0][1]); SCHED();
    v4u h8_0; ld16c<0>(h8_0, l16, hs8);
    const v2u Wa7_0 = tr_read(tp0);
    SCHED();
    SCHED();
    c2[0][2] = mma32(Wf4_2, h4_0, c2[0][2]); SCHED();
    const v4u Wf7_0 = cat(Wa7_0, tr_read(tp0 + 512));
    SCHED();
    tp1 = pinp(xorp(pt0, 32800));
    SCHED();
    SCHED();
    c2[0][3] = mma32(Wf4_3, h4_0, c2[0][3]); SCHED();
    v4u h8_1; ld16c<1024>(h8_1, l16, hs8);
    const v2u Wa7_1 = tr_read(tp1);
    SCHED();
    SCHED();
    c2[1][0] = mma32(Wf4_0, h4_1, c2[1][0]); SCHED();
    const v4u Wf7_1 = cat(Wa7_1, tr_read(tp1 + 512));
    SCHED();
    tp0 = pinp(xorp(pt0, 32832));
    SCHED();
    SCHED();
    c2[1][1] = mma32(Wf4_1, h4_1, c2[1][1]); SCHED();
    v4u h8_2; ld16c<2048>(h8_2, l16, hs8);
    const v2u Wa7_2 = tr_read(tp0);
    SCHED();
    SCHED();
    c2[1][2] = mma32(Wf4_2, h4_1, c2[1][2]); SCHED();
    const v4u Wf7_2 = cat(Wa7_2, tr_read(tp0 + 512));
    SCHED();
    tp1 = pinp(xorp(pt0, 32864));
    SCHED();
    SCHED();
    c2[1][3] = mma32(Wf4_3, h4_1, c2[1][3]); SCHED();
    v4u h8_3; ld16c<3072>(h8_3, l16, hs8);
    const v2u Wa7_3 = tr_read(tp1);
    SCHED();
    SCHED();
    c2[2][0] = mma32(Wf4_0, h4_2, c2[2][0]); SCHED();
    const v4u Wf7_3 = cat(Wa7_3, tr_read(tp1 + 512));
    SCHED();
    SCHED();
    SCHED();
    SCHED();
    c2[2][1] = mma32(Wf4_1, h4_2, c2[2][1]); SCHED();
    SCHED();
    c2[2][2] = mma32(Wf4_2, h4_2, c2[2][2]); SCHED();
    SCHED();
    c2[2][3] = mma32(Wf4_3, h4_2, c2[2][3]); SCHED();
    SCHED();
    c2[3][0] = mma32(Wf4_0, h4_3, c2[3][0]); SCHED();
    SCHED();
    c2[3][1] = mma32(Wf4_1, h4_3, c2[3][1]); SCHED();
    SCHED();
    c2[3][2] = mma32(Wf4_2, h4_3, c2[3][2]); SCHED();
    SCHED();
    c2[3][3] = mma32(Wf4_3, h4_3, c2[3][3]); SCHED();
    SCHED();
    STH(23, 22, 98, 96);
    SCHED();
    WAITC(16, 15);
    asm volatile("" : "+v"(h5_0), "+v"(h5_1), "+v"(h5_2), "+v"(h5_3) :: "memory");
    c2[0][0] = mma32(Wf5_0, h5_0, c2[0][0]); SCHED();
    WAITC(8, 15);
    SCHED();
    tp0 = pinp(xorp(pt0, 131072));
    SCHED();
    SCHED();
    c2[0][1] = mma32(Wf5_1, h5_0, c2[0][1]); SCHED();
    const v2u Wa8_0 = tr_read(tp0);
    SCHED();
    SCHED();
    c2[0][2] = mma32(Wf5_2, h5_0, c2[0][2]); SCHED();
    const v4u Wf8_0 = cat(Wa8_0, tr_read(tp0 + 512));
    SCHED();
    tp1 = pinp(xorp(pt0, 131104));
    SCHED();
    SCHED();
    c2[0][3] = mma32(Wf5_3, h5_0, c2[0][3]); SCHED();
    const v2u Wa8_1 = tr_read(tp1);
    SCHED();
    SCHED();
    c2[1][0] = mma32(Wf5_0, h5_1, c2[1][0]); SCHED();
    const v4u Wf8_1 = cat(Wa8_1, tr_read(tp1 + 512));
    SCHED();
    tp0 = pinp(xorp(pt0, 131136));
    SCHED();
    SCHED();
    c2[1][1] = mma32(Wf5_1, h5_1, c2[1][1]); SCHED();
    const v2u Wa8_2 = tr_read(tp0);
    SCHED();
    SCHED();
    c2[1][2] = mma32(Wf5_2, h5_1, c2[1][2]); SCHED();
    const v4u Wf8_2 = cat(Wa8_2, tr_read(tp0 + 512));
    SCHED();
    tp1 = pinp(xorp(pt0, 131168));
    SCHED();
    SCHED();
    c2[1][3] = mma32(Wf5_3, h5_1, c2[1][3]); SCHED();
    const v2u Wa8_3 = tr_read(tp1);
    SCHED();
    SCHED();
    c2[2][0] = mma32(Wf5_0, h5_2, c2[2][0]); SCHED();
    const v4u Wf8_3 = cat(Wa8_3, tr_read(tp1 + 512));
    SCHED();
    SCHED();
    asm volatile("" : "+v"(h6_0), "+v"(h6_1), "+v"(h6_2), "+v"(h6_3) :: "memory");
    h_check(h6_0, h6_1, h6_2, h6_3, l16, hs6); SCHED();
    v4u hf0 = {h6_0[0], h6_0[1], h6_1[0], h6_1[1]};
    v4u hf1 = {h6_0[2], h6_0[3], h6_1[2], h6_1[3]};
    v4u hf2 = {h6_2[0], h6_2[1], h6_3[0], h6_3[1]};
    v4u hf3 = {h6_2[2], h6_2[3], h6_3[2], h6_3[3]};
    SCHED();
    SCHED();
    SCHED();
    SCHED();
    c2[2][1] = mma32(Wf5_1, h5_2, c2[2][1]); SCHED();
    SCHED();
    c2[2][2] = mma32(Wf5_2, h5_2, c2[2][2]); SCHED();
    SCHED();
    c2[2][3] = mma32(Wf5_3, h5_2, c2[2][3]); SCHED();
    SCHED();
    c2[3][0] = mma32(Wf5_0, h5_3, c2[3][0]); SCHED();
    SCHED();
    c2[3][1] = mma32(Wf5_1, h5_3, c2[3][1]); SCHED();
    SCHED();
    WAITC(4, 15);
    asm volatile("" : "+v"(h7_0), "+v"(h7_1), "+v"(h7_2), "+v"(h7_3) :: "memory");
    h_check(h7_0, h7_1, h7_2, h7_3, l16, hs7); SCHED();
    FRESH(); __builtin_amdgcn_raw_buffer_store_b32(tag, wsr, (l16 << 22) | (((bid ^ 64) | wave) << 24), 0, SC1);
    SCHED();
    SCHED();
    c2[3][2] = mma32(Wf5_2, h5_3, c2[3][2]); SCHED();
    SCHED();
    c2[3][3] = mma32(Wf5_3, h5_3, c2[3][3]); SCHED();
    SCHED();
    STH(24, 23, 96, 98);
    SCHED();
    WAITC(1, 15);
    asm volatile("" : "+v"(h8_0), "+v"(h8_1), "+v"(h8_2), "+v"(h8_3) :: "memory");
    h_check(h8_0, h8_1, h8_2, h8_3, l16, hs8); SCHED();
    STH(26, 24, 98, 96);
    c2[0][0] = mma32(Wf6_0, hf0, c2[0][0]); SCHED();
    SCHED();
    c2[0][1] = mma32(Wf6_1, hf0, c2[0][1]); SCHED();
    SCHED();
    c2[0][2] = mma32(Wf6_2, hf0, c2[0][2]); SCHED();
    SCHED();
    c2[0][3] = mma32(Wf6_3, hf0, c2[0][3]); SCHED();
    SCHED();
    c2[0][0] = mma32(Wf7_0, h7_0, c2[0][0]); SCHED();
    SCHED();
    c2[0][1] = mma32(Wf7_1, h7_0, c2[0][1]); SCHED();
    SCHED();
    c2[0][2] = mma32(Wf7_2, h7_0, c2[0][2]); SCHED();
    SCHED();
    c2[0][3] = mma32(Wf7_3, h7_0, c2[0][3]); SCHED();
    SCHED();
    c2[0][0] = mma32(Wf8_0, h8_0, c2[0][0]); SCHED();
    SCHED();
    c2[0][1] = mma32(Wf8_1, h8_0, c2[0][1]); SCHED();
    SCHED();
    c2[0][2] = mma32(Wf8_2, h8_0, c2[0][2]); SCHED();
    SCHED();
    c2[0][3] = mma32(Wf8_3, h8_0, c2[0][3]); SCHED();
    SCHED();
    c2[1][0] = mma32(Wf6_0, hf1, c2[1][0]); SCHED();
    SCHED();
    c2[1][1] = mma32(Wf6_1, hf1, c2[1][1]); SCHED();
    tp0 = pinp(xorp(rw, 32768)); *(LAS f32x4*)tp0 = c2[0][0];
    SCHED();
    c2[1][2] = mma32(Wf6_2, hf1, c2[1][2]); SCHED();
    SCHED();
    c2[1][3] = mma32(Wf6_3, hf1, c2[1][3]); SCHED();
    SCHED();
    c2[1][0] = mma32(Wf7_0, h7_1, c2[1][0]); SCHED();
    tp0 = pinp(xorp(rw, 33856)); *(LAS f32x4*)tp0 = c2[0][1];
    SCHED();
    c2[1][1] = mma32(Wf7_1, h7_1, c2[1][1]); SCHED();
    SCHED();
    c2[1][2] = mma32(Wf7_2, h7_1, c2[1][2]); SCHED();
    SCHED();
    c2[1][3] = mma32(Wf7_3, h7_1, c2[1][3]); SCHED();
    tp0 = pinp(xorp(rw, 34944)); *(LAS f32x4*)tp0 = c2[0][2];
    SCHED();
    c2[1][0] = mma32(Wf8_0, h8_1, c2[1][0]); SCHED();
    SCHED();
    c2[1][1] = mma32(Wf8_1, h8_1, c2[1][1]); SCHED();
    SCHED();
    c2[1][2] = mma32(Wf8_2, h8_1, c2[1][2]); SCHED();
    tp0 = pinp(xorp(rw, 36032)); *(LAS f32x4*)tp0 = c2[0][3];
    SCHED();
    c2[1][3] = mma32(Wf8_3, h8_1, c2[1][3]); SCHED();
    SCHED();
    c2[2][0] = mma32(Wf6_0, hf2, c2[2][0]); SCHED();
    SCHED();
    c2[2][1] = mma32(Wf6_1, hf2, c2[2][1]); SCHED();
    tp0 = pinp(xorp(rw, 65536)); *(LAS f32x4*)tp0 = c2[1][0];
    SCHED();
    c2[2][2] = mma32(Wf6_2, hf2, c2[2][2]); SCHED();
    SCHED();
    c2[2][3] = mma32(Wf6_3, hf2, c2[2][3]); SCHED();
    SCHED();
    c2[2][0] = mma32(Wf7_0, h7_2, c2[2][0]); SCHED();
    tp0 = pinp(xorp(rw, 66624)); *(LAS f32x4*)tp0 = c2[1][1];
    SCHED();
    c2[2][1] = mma32(Wf7_1, h7_2, c2[2][1]); SCHED();
    SCHED();
    c2[2][2] = mma32(Wf7_2, h7_2, c2[2][2]); SCHED();
    SCHED();
    c2[2][3] = mma32(Wf7_3, h7_2, c2[2][3]); SCHED();
    tp0 = pinp(xorp(rw, 67712)); *(LAS f32x4*)tp0 = c2[1][2];
    SCHED();
    c2[2][0] = mma32(Wf8_0, h8_2, c2[2][0]); SCHED();
    SCHED();
    c2[2][1] = mma32(Wf8_1, h8_2, c2[2][1]); SCHED();
    SCHED();
    c2[2][2] = mma32(Wf8_2, h8_2, c2[2][2]); SCHED();
    tp0 = pinp(xorp(rw, 68800)); *(LAS f32x4*)tp0 = c2[1][3];
    SCHED();
    c2[2][3] = mma32(Wf8_3, h8_2, c2[2][3]); SCHED();
    SCHED();
    c2[3][0] = mma32(Wf6_0, hf3, c2[3][0]); SCHED();
    SCHED();
    c2[3][1] = mma32(Wf6_1, hf3, c2[3][1]); SCHED();
    tp0 = pinp(xorp(rw, 98304)); *(LAS f32x4*)tp0 = c2[2][0];
    SCHED();
    c2[3][2] = mma32(Wf6_2, hf3, c2[3][2]); SCHED();
    SCHED();
    c2[3][3] = mma32(Wf6_3, hf3, c2[3][3]); SCHED();
    SCHED();
    c2[3][0] = mma32(Wf7_0, h7_3, c2[3][0]); SCHED();
    tp0 = pinp(xorp(rw, 99392)); *(LAS f32x4*)tp0 = c2[2][1];
    SCHED();
    c2[3][1] = mma32(Wf7_1, h7_3, c2[3][1]); SCHED();
    SCHED();
    c2[3][2] = mma32(Wf7_2, h7_3, c2[3][2]); SCHED();
    SCHED();
    c2[3][3] = mma32(Wf7_3, h7_3, c2[3][3]); SCHED();
    tp0 = pinp(xorp(rw, 100480)); *(LAS f32x4*)tp0 = c2[2][2];
    SCHED();
    c2[3][0] = mma32(Wf8_0, h8_3, c2[3][0]); SCHED();
    SCHED();
    c2[3][1] = mma32(Wf8_1, h8_3, c2[3][1]); SCHED();
    SCHED();
    c2[3][2] = mma32(Wf8_2, h8_3, c2[3][2]); SCHED();
    tp0 = pinp(xorp(rw, 101568)); *(LAS f32x4*)tp0 = c2[2][3];
    SCHED();
    c2[3][3] = mma32(Wf8_3, h8_3, c2[3][3]); SCHED();
    SCHED();
    tp0 = pinp(xorp(rw, 131072)); *(LAS f32x4*)tp0 = c2[3][0];
    tp0 = pinp(xorp(rw, 132160)); *(LAS f32x4*)tp0 = c2[3][1];
    tp0 = pinp(xorp(rw, 133248)); *(LAS f32x4*)tp0 = c2[3][2];
    tp0 = pinp(xorp(rw, 134336)); *(LAS f32x4*)tp0 = c2[3][3];
    STH(27, 26, 96, 98);
    STH(27, 27, 98, 96); finish(rr, oo, op_);
  }
}
}

extern "C" void kernel_launch(void* const* d_in, const int* in_sizes, int n_in, void* d_out, int out_size, void* d_ws, size_t ws_size,
                              hipStream_t stream) {
  fk::fused<<<256, 512, 0, stream>>>((const char*)d_in[0], (const char*)d_in[1], (const char*)d_in[2], (const char*)d_in[4],
                                     (const char*)d_in[5], (const char*)d_in[6], (const char*)d_in[7], (char*)d_out, (char*)d_ws);
}
```
